```python
import torch

def _softmax(x, axis=-1):
    return torch.softmax(x, dim=axis)
def _gelu(x):
    return torch.nn.functional.gelu(x, approximate="tanh")
def _silu(x):
    return torch.nn.functional.silu(x)
def _sigmoid(x):
    return torch.sigmoid(x)
def _layernorm(x, axis=-1, eps=1e-5):
    m = x.mean(dim=axis, keepdim=True)
    v = x.var(dim=axis, keepdim=True, unbiased=False)
    return (x - m) / torch.sqrt(v + eps)
def _scatter_add(tgt, idx, src, axis=0):
    return tgt.clone().index_add_(axis, idx, src)
def _segment_sum(src, seg, n):
    out = torch.zeros((n,) + src.shape[1:], dtype=src.dtype)
    return out.index_add_(0, seg, src)
def _pad(x, pw):
    flat = tuple(v for pair in reversed(pw) for v in pair)
    return torch.nn.functional.pad(x, flat)

kernel_name = 'parallel_sum__linear_kernel_hedgehog__geglu_ffn__prompt'

def setup_inputs():

    _w = (lambda a: (lambda t: t.to(torch.bfloat16) if t.is_floating_point() else t)(torch.as_tensor(a)))
    def _A_setup():
        B, S, D, H = 1, 2048, 448, 16
        Dh = D // H
        return {
            "x": _w((lambda *s: torch.randn(*s, dtype=torch.bfloat16) * 0.05)(B, S, D)),
            "wqkv": _w((lambda *s: torch.randn(*s, dtype=torch.bfloat16) * 0.8)(D, 3 * D)),
            "w_phi": _w((lambda *s: torch.randn(*s, dtype=torch.bfloat16) * 0.8)(H, Dh, Dh) * (Dh ** -0.5)),
            "wo": _w((lambda *s: torch.randn(*s, dtype=torch.bfloat16) * 0.2)(D, D)),
        }
    _A_in = _A_setup()

    def _B_setup():
        B, S, D, F = 1, 2048, 448, 1792
        return {
            "x": _w((lambda *s: torch.randn(*s, dtype=torch.bfloat16) * 0.05)(B, S, D)),
            "w_gate": _w((lambda *s: torch.randn(*s, dtype=torch.bfloat16) * 0.1)(D, F)),
            "w_up": _w((lambda *s: torch.randn(*s, dtype=torch.bfloat16) * 0.1)(D, F)),
            "w_down": _w((lambda *s: torch.randn(*s, dtype=torch.bfloat16) * 0.1)(F, D)),
        }
    _B_in = _B_setup()

    return {
        "A_x": _A_in["x"],
        "A_wqkv": _A_in["wqkv"],
        "A_w_phi": _A_in["w_phi"],
        "A_wo": _A_in["wo"],
        "B_x": _B_in["x"],
        "B_w_gate": _B_in["w_gate"],
        "B_w_up": _B_in["w_up"],
        "B_w_down": _B_in["w_down"],
    }

def reference(A_x, A_wqkv, A_w_phi, A_wo, B_x, B_w_gate, B_w_up, B_w_down):
    def _A_ref(x, wqkv, w_phi, wo):
        B, S, D = x.shape
        H = 16; Dh = D // H
        qkv = (lambda x, *d: x.permute(*d))((x @ wqkv).reshape(B, S, 3, H, Dh), 2, 0, 3, 1, 4)
        q, k, v = qkv[0], qkv[1], qkv[2]
        pq = torch.einsum("bhsd,hdf->bhsf", q, w_phi)
        pk = torch.einsum("bhsd,hdf->bhsf", k, w_phi)
        phi_q = torch.concatenate([_softmax(pq, axis=-1), _softmax(-pq, axis=-1)], axis=-1)
        phi_k = torch.concatenate([_softmax(pk, axis=-1), _softmax(-pk, axis=-1)], axis=-1)
        kv = torch.einsum("bhsf,bhsd->bhfd", phi_k, v)
        z  = torch.einsum("bhsf->bhf", phi_k)
        num = torch.einsum("bhqf,bhfd->bhqd", phi_q, kv)
        den = torch.einsum("bhqf,bhf->bhq", phi_q, z)[..., None] + 1e-6
        o = num / den
        return (lambda x, *d: x.permute(*d))(o, 0, 2, 1, 3).reshape(B, S, D) @ wo

    def _B_ref(x, w_gate, w_up, w_down):
        return (_gelu(x @ w_gate) * (x @ w_up)) @ w_down

    x = A_x
    xn = _layernorm(x, axis=-1)
    return x + _A_ref(xn, A_wqkv, A_w_phi, A_wo) + _B_ref(xn, B_w_gate, B_w_up, B_w_down)
```

```cpp
#include <hip/hip_runtime.h>
#include <hip/hip_bf16.h>
typedef __hip_bfloat16 bf16;
constexpr int S=2048, D=448, H=16, DH=28, F=1792;
__device__ inline float b2f(bf16 x){return __bfloat162float(x);}
__device__ inline float rb(float x){return __bfloat162float(__float2bfloat16(x));}

__global__ void k_ln(const bf16* x, float* xn){
  int r=blockIdx.x; int t=threadIdx.x;
  float v[7]; float s=0;
  for(int i=0;i<7;i++){v[i]=b2f(x[r*D+t+64*i]); s+=v[i];}
  for(int o=32;o>0;o>>=1) s+=__shfl_xor(s,o);
  float m=s/D; float q=0;
  for(int i=0;i<7;i++){q+=(v[i]-m)*(v[i]-m);}
  for(int o=32;o>0;o>>=1) q+=__shfl_xor(q,o);
  float rs=rsqrtf(q/D+1e-5f);
  for(int i=0;i<7;i++) xn[r*D+t+64*i]=rb((v[i]-m)*rs);
}
__global__ void k_gemm(const float* A, const bf16* W, float* C, int K, int N, int acc){
  int n=blockIdx.x*64+threadIdx.x; int m=blockIdx.y*4+threadIdx.y;
  if(n>=N) return;
  float s=0; for(int k=0;k<K;k++) s+=A[m*K+k]*b2f(W[k*N+n]);
  if(acc) C[m*N+n]+=s; else C[m*N+n]=s;
}
__global__ __launch_bounds__(64) void k_phi(const float* qkv, const bf16* wphi, float* phiq, float* phik){
  int s=blockIdx.x; int h=threadIdx.x>>1; int w=threadIdx.x&1;
  float p[DH];
  #pragma unroll
  for(int f=0;f<DH;f++){float a=0; _Pragma("unroll 1") for(int d=0;d<DH;d++) a+=rb(qkv[s*3*D+w*D+h*DH+d])*b2f(wphi[h*DH*DH+d*DH+f]); p[f]=a;}
  float mx=-1e30f, mn=1e30f;
  #pragma unroll
  for(int f=0;f<DH;f++){mx=fmaxf(mx,p[f]); mn=fminf(mn,p[f]);}
  float s1=0,s2=0;
  #pragma unroll
  for(int f=0;f<DH;f++){s1+=__expf(p[f]-mx); s2+=__expf(mn-p[f]);}
  float* o=(w?phik:phiq)+(s*H+h)*56;
  #pragma unroll
  for(int f=0;f<DH;f++){o[f]=__expf(p[f]-mx)/s1; o[28+f]=__expf(mn-p[f])/s2;}
}
__global__ void k_kv(const float* phik, const float* qkv, float* kv){
  int h=blockIdx.x/56, f=blockIdx.x%56, d=threadIdx.x;
  if(d>28) return;
  float a=0; for(int s=0;s<S;s++) a+=phik[(s*H+h)*56+f]*(d<28?qkv[s*3*D+2*D+h*DH+d]:1.f);
  kv[(h*56+f)*32+d]=a;
}
__global__ void k_o(const float* phiq, const float* kv, float* o){
  int s=blockIdx.x, h=threadIdx.x/28, d=threadIdx.x%28;
  float n=0,dn=0; for(int f=0;f<56;f++){float p=phiq[(s*H+h)*56+f]; n+=p*kv[(h*56+f)*32+d]; dn+=p*kv[(h*56+f)*32+28];}
  o[s*D+threadIdx.x]=n/(dn+1e-6f);
}
__global__ void k_act(float* g, const float* u){ int i=blockIdx.x*256+threadIdx.x; float x=g[i];
  float t=tanhf(0.7978845608f*(x+0.044715f*x*x*x)); g[i]=0.5f*x*(1.f+t)*u[i]; }
__global__ void k_fin(const bf16* x, const float* y, bf16* out){ int i=blockIdx.x*256+threadIdx.x; out[i]=__float2bfloat16(b2f(x[i])+y[i]); }

extern "C" void kernel_launch(void* const* d_in, const int* in_sizes, int n_in, void* d_out, int out_size, void* d_ws, size_t ws_size, hipStream_t stream) {
  const bf16* x=(const bf16*)d_in[0]; const bf16* wqkv=(const bf16*)d_in[1]; const bf16* wphi=(const bf16*)d_in[2]; const bf16* wo=(const bf16*)d_in[3];
  const bf16* wg=(const bf16*)d_in[5]; const bf16* wu=(const bf16*)d_in[6]; const bf16* wd=(const bf16*)d_in[7];
  float* ws=(float*)d_ws;
  float* xn=ws; float* qkv=xn+S*D; float* phiq=qkv+S*3*D; float* phik=phiq+S*H*56; float* kv=phik+S*H*56; float* o=kv+H*56*32; float* g=o+S*D; float* u=g+S*F; float* y=u+S*F;
  k_ln<<<S,64,0,stream>>>(x,xn);
  k_gemm<<<dim3(21,S/4),dim3(64,4),0,stream>>>(xn,wqkv,qkv,D,3*D,0);
  k_phi<<<S,32,0,stream>>>(qkv,wphi,phiq,phik);
  k_kv<<<16*56,32,0,stream>>>(phik,qkv,kv);
  k_o<<<S,448,0,stream>>>(phiq,kv,o);
  k_gemm<<<dim3(28,S/4),dim3(64,4),0,stream>>>(xn,wg,g,D,F,0);
  k_gemm<<<dim3(28,S/4),dim3(64,4),0,stream>>>(xn,wu,u,D,F,0);
  k_act<<<S*F/256,256,0,stream>>>(g,u);
  k_gemm<<<dim3(7,S/4),dim3(64,4),0,stream>>>(o,wo,y,D,D,0);
  k_gemm<<<dim3(7,S/4),dim3(64,4),0,stream>>>(g,wd,y,F,D,1);
  k_fin<<<S*D/256,256,0,stream>>>(x,y,(bf16*)d_out);
}
```

```cpp
#include <hip/hip_runtime.h>
#include <hip/hip_bf16.h>
#include <stdint.h>
#include <type_traits>
#pragma clang diagnostic ignored "-Wint-to-pointer-cast"

#define DI __device__ __forceinline__
#define LAS __attribute__((address_space(3)))
#define GAS __attribute__((address_space(1)))
typedef __attribute__((ext_vector_type(8))) short s16x8;
typedef __attribute__((ext_vector_type(4))) short s16x4;
typedef __attribute__((ext_vector_type(16))) float f32x16;
typedef __attribute__((ext_vector_type(4))) float f32x4;
typedef __attribute__((ext_vector_type(2))) float f32x2;
typedef __attribute__((ext_vector_type(4))) unsigned u32x4;
typedef __attribute__((ext_vector_type(2))) unsigned u32x2;
typedef __attribute__((ext_vector_type(2))) __bf16 bf16x2;
typedef GAS unsigned gu32;
typedef __amdgpu_buffer_rsrc_t rsrc_t;
#define RLX_AGENT __ATOMIC_RELAXED, __HIP_MEMORY_SCOPE_AGENT

constexpr unsigned WS_EPOCH = 0;
constexpr unsigned WS_HFLAG = 1024;
constexpr unsigned WS_OFLAG = WS_HFLAG + 48 * 128;
constexpr unsigned WS_KVFLAG = WS_OFLAG + 16 * 128;
constexpr unsigned WS_RDFLAG = WS_KVFLAG + 4 * 128;
constexpr unsigned WS_STAT = 16384;
constexpr unsigned WS_CS = WS_STAT + 2048 * 16;
constexpr unsigned CS_PART = 4928 * 8;
constexpr unsigned WS_KVRED = 262144;
constexpr unsigned WS_KVPART = WS_KVRED + 65536;
constexpr unsigned WS_AFRAG = WS_KVPART + (2u << 20);
constexpr unsigned WS_STAMP = WS_AFRAG + 64u * 144u * 1024u;
constexpr unsigned WS_BYTES = WS_STAMP + 256 * 16 * 8;
static_assert(WS_CS + 3 * CS_PART <= WS_KVRED, "ws map");

constexpr int LDS_BYTES = 163840;
constexpr unsigned SLOT = 16384;
constexpr unsigned LDS_MISC_F = 6 * 16384, LDS_MISC_A = 163840 - 2048;
constexpr unsigned LDS_PHI = 135168 + 64;
constexpr int SPIN_CAP = 1 << 14;

#define MFMA(a, b, c) __builtin_amdgcn_mfma_f32_32x32x16_bf16((a), (b), (c), 0, 0, 0)
#define WAITV(n) asm volatile("s_waitcnt vmcnt(" #n ")" ::: "memory")
#define BAR() do { asm volatile("s_waitcnt lgkmcnt(0)" ::: "memory"); __builtin_amdgcn_s_barrier(); asm volatile("" ::: "memory"); } while (0)

#define SB() __builtin_amdgcn_sched_barrier(0)
#define ALD4(dst, voff, soff) asm volatile("buffer_load_dword %0, %1, %2, %3 offen sc1" : "=v"(dst) : "v"(voff), "s"(rs), "s"(soff) : "memory")
template <int N> DI void waitv() { asm volatile("s_waitcnt vmcnt(%0)" :: "n"(N) : "memory"); }
#define TK(i)
#define STAMP(i)
#define STAMPC(i)

#define DMA1I(voff, rsrc, ldsb, LIMM, SOFF, tmp) asm volatile("s_add_u32 m0, %3, %4\n\ts_mov_b32 %0, %5\n\tbuffer_load_dwordx4 %1, %2, %0 offen lds" \
    : "=&s"(tmp) : "v"(voff), "s"(rsrc), "s"(ldsb), "n"(LIMM), "n"(SOFF) : "memory", "scc")
#define DMA1A(voff, rsrc, ldsb, LIMM, soff, sinc) asm volatile("s_add_u32 m0, %3, %4\n\ts_add_u32 %0, %0, %5\n\tbuffer_load_dwordx4 %1, %2, %0 offen lds" \
    : "+s"(soff) : "v"(voff), "s"(rsrc), "s"(ldsb), "n"(LIMM), "s"(sinc) : "memory", "scc")
#define DMA2(voff, rsrc, soff, IMM) asm volatile("buffer_load_dwordx4 %0, %1, %2 offen offset:%3 lds" :: "v"(voff), "s"(rsrc), "s"(soff), "n"(IMM) : "memory")
#define OPAQUE(v) asm volatile("" : "+v"(v))
DI rsrc_t mkrs(const void* p, unsigned bytes) { return __builtin_amdgcn_make_buffer_rsrc((void*)p, 0, bytes, 0x00020000); }
template <int N> using IC = std::integral_constant<int, N>;
DI s16x8 trfrag(unsigned a0, unsigned a1) {
  s16x4 lo = __builtin_amdgcn_ds_read_tr16_b64_v4i16((LAS s16x4*)a0);
  s16x4 hi = __builtin_amdgcn_ds_read_tr16_b64_v4i16((LAS s16x4*)a1);
  return __builtin_shufflevector(lo, hi, 0, 1, 2, 3, 4, 5, 6, 7);
}
DI unsigned pk2(float a, float b) { f32x2 p = {a, b}; return __builtin_bit_cast(unsigned, __builtin_convertvector(p, bf16x2)); }
DI float blo(unsigned u) { return __uint_as_float(u << 16); }
DI float bhi(unsigned u) { return __uint_as_float(u & 0xffff0000u); }
DI float add32(float a, float b) { auto r = __builtin_amdgcn_permlane32_swap(__float_as_uint(a), __float_as_uint(b), false, false); return __uint_as_float(r[0]) + __uint_as_float(r[1]); }
DI float add16(float a, float b) { auto r = __builtin_amdgcn_permlane16_swap(__float_as_uint(a), __float_as_uint(b), false, false); return __uint_as_float(r[0]) + __uint_as_float(r[1]); }
DI float max32(float a) { auto r = __builtin_amdgcn_permlane32_swap(__float_as_uint(a), __float_as_uint(a), false, false); return fmaxf(__uint_as_float(r[0]), __uint_as_float(r[1])); }
DI float min32(float a) { auto r = __builtin_amdgcn_permlane32_swap(__float_as_uint(a), __float_as_uint(a), false, false); return fminf(__uint_as_float(r[0]), __uint_as_float(r[1])); }
template <int CTRL> DI float dpp(float x) { return __builtin_bit_cast(float, __builtin_amdgcn_mov_dpp(__builtin_bit_cast(int, x), CTRL, 0xf, 0xf, true)); }
DI float wave_sum(float v) {
  v += dpp<0xB1>(v); v += dpp<0x4E>(v); v += dpp<0x141>(v); v += dpp<0x128>(v);
  v = add16(v, v); return add32(v, v);
}
DI unsigned split2(float v) {
  const unsigned h = pk2(v, 0.f);
  return pk2(__uint_as_float(h << 16), v - __uint_as_float(h << 16));
}
DI s16x8 pack8(const f32x16& x, int s) {
  u32x4 u = {pk2(x[8 * s], x[8 * s + 1]), pk2(x[8 * s + 2], x[8 * s + 3]), pk2(x[8 * s + 4], x[8 * s + 5]), pk2(x[8 * s + 6], x[8 * s + 7])};
  return __builtin_bit_cast(s16x8, u);
}
DI unsigned rmask(unsigned row) { return (((row >> 1) & 1) << 2) | ((row >> 2) & 1) | ((row >> 3) & 2); }
DI f32x16 zero16() { f32x16 z; for (int i = 0; i < 16; i++) z[i] = 0.f; return z; }
DI void st16(rsrc_t rs, unsigned off, u32x4 v) { __builtin_amdgcn_raw_buffer_store_b128(v, rs, off, 0, 16); }
DI u32x4 ld16(rsrc_t rs, unsigned off) { return __builtin_amdgcn_raw_buffer_load_b128(rs, off, 0, 16); }
#define ALD16(dst, voff, soff) asm volatile("buffer_load_dwordx4 %0, %1, %2, %3 offen sc1" : "=v"(dst) : "v"(voff), "s"(rs), "s"(soff) : "memory")
#define ALD8(dst, voff, soff) asm volatile("buffer_load_dwordx2 %0, %1, %2, %3 offen sc1" : "=v"(dst) : "v"(voff), "s"(rs), "s"(soff) : "memory")
DI void poll(char* ws, unsigned off, bool active, unsigned tag) {
#pragma unroll 1
  for (int it = 0; it < SPIN_CAP; ++it) {
    unsigned v = __hip_atomic_load((gu32*)(ws + off), RLX_AGENT);
    if (__all(!active || v == tag)) break;
    __builtin_amdgcn_s_sleep(1);
  }
  __builtin_amdgcn_fence(__ATOMIC_ACQUIRE, "wavefront");
}
DI void publish(char* ws, unsigned off, unsigned tag) { __hip_atomic_store((gu32*)(ws + off), tag, RLX_AGENT); }

struct CsG { u32x2 a[3], b[3]; };
DI void cs_issue(rsrc_t rs, CsG& g, unsigned va, unsigned vb) {
  ALD8(g.a[0], va, WS_CS); ALD8(g.a[1], va, WS_CS + CS_PART); ALD8(g.a[2], va, WS_CS + 2 * CS_PART);
  ALD8(g.b[0], vb, WS_CS); ALD8(g.b[1], vb, WS_CS + CS_PART); ALD8(g.b[2], vb, WS_CS + 2 * CS_PART);
}
#define CS_PIN(g, WAIT) asm volatile(WAIT : "+v"(g.a[0]), "+v"(g.a[1]), "+v"(g.a[2]), "+v"(g.b[0]), "+v"(g.b[1]), "+v"(g.b[2])::"memory")
DI void cs_check(rsrc_t rs, CsG& g, unsigned va, unsigned vb, unsigned tag) {
#pragma unroll 1
  for (int it = 0; it < SPIN_CAP; ++it) {
    const bool ok = g.a[0][1] == tag && g.a[1][1] == tag && g.a[2][1] == tag && g.b[0][1] == tag && g.b[1][1] == tag && g.b[2][1] == tag;
    if (__all(ok)) break;
    cs_issue(rs, g, va, vb);
    CS_PIN(g, "s_waitcnt vmcnt(0)");
  }
}
DI void st_check(rsrc_t rs, u32x4& s, unsigned v, unsigned tag) {
#pragma unroll 1
  for (int it = 0; it < SPIN_CAP; ++it) {
    if (__all(s[1] == tag && s[3] == tag)) break;
    ALD16(s, v, WS_STAT);
    asm volatile("s_waitcnt vmcnt(0)" : "+v"(s)::"memory");
  }
}
DI s16x8 fold_a(const u32x2 (&g)[3]) { u32x4 u = {g[0][0], g[1][0], g[2][0], 0u}; return __builtin_bit_cast(s16x8, u); }
DI s16x8 fold_b(unsigned nmu, bool hi) { unsigned h = hi ? (nmu >> 16) : (nmu & 0xffffu); h |= h << 16; u32x4 u = {h, h, h, 0u}; return __builtin_bit_cast(s16x8, u); }

DI void dual_softmax(const f32x16& p, float sc, float os, bool hi, f32x16& pos, f32x16& neg) {
  const float L = sc * 1.4426950408889634f;
  f32x16 a = p, b = p;
  for (int i = 12; i < 16; i++) { a[i] = hi ? -INFINITY : p[i]; b[i] = hi ? INFINITY : p[i]; }
  float mx = a[0], mn = b[0];
  for (int i = 1; i < 16; i++) { mx = fmaxf(mx, a[i]); mn = fminf(mn, b[i]); }
  mx = max32(mx); mn = min32(mn);
  float cx = -mx * L, cn = mn * L, s1 = 0.f, s2 = 0.f;
  for (int i = 0; i < 16; i++) {
    pos[i] = __builtin_amdgcn_exp2f(fmaf(a[i], L, cx)); s1 += pos[i];
    neg[i] = __builtin_amdgcn_exp2f(fmaf(b[i], -L, cn)); s2 += neg[i];
  }
  s1 = add32(s1, s1); s2 = add32(s2, s2);
  const float r1 = os * __builtin_amdgcn_rcpf(s1), r2 = os * __builtin_amdgcn_rcpf(s2);
  for (int i = 0; i < 16; i++) { pos[i] *= r1; neg[i] *= r2; }
}

__global__ __launch_bounds__(512, 2) void fused_kernel(const char* __restrict__ x, const char* __restrict__ wqkv, const char* __restrict__ wphi,
                                                       const char* __restrict__ wo, const char* __restrict__ wg, const char* __restrict__ wu,
                                                       const char* __restrict__ wd, char* __restrict__ out, char* ws) {
  extern __shared__ __attribute__((aligned(16))) char smem[];
  const int tid = threadIdx.x, lane = tid & 63, w = __builtin_amdgcn_readfirstlane(tid >> 6);
  const int bx = blockIdx.x, xcd = bx & 7, r = bx >> 3;
  const int th = xcd & 1, m = (xcd >> 1) * 4 + (r & 3), t8 = r >> 2;
  const bool hi = lane >= 32;
  const int g16 = (lane >> 4) & 1, q4 = (lane & 15) >> 2, p4 = lane & 3, l31 = lane & 31, hb = lane >> 5;
  STAMP(0); STAMPC(14);
#define DUMPTK()
#define TA(i)
  TA(0);
  const rsrc_t rs = mkrs(ws, WS_BYTES);
  const int rl = lane >> 3;

  unsigned tag; u32x4 px, pw[3];
  const int lc = bx % 77, cpart = bx / 77;
  {
    asm volatile("buffer_load_dword %0, off, %1, 0 sc1" : "=v"(tag) : "s"(rs) : "memory");
    asm volatile("global_load_dwordx4 %0, %1, %2" : "=v"(px) : "v"(min(lane, 55) * 16), "s"(x + (size_t)(bx * 8 + w) * 896) : "memory");
    const char* cb = lc < 21 ? wqkv + lc * 128 : (lc < 49 ? wg + (lc - 21) * 128 : wu + (lc - 49) * 128);
    const unsigned pitch = lc < 21 ? 2688 : 3584;
    const unsigned vo = rl * pitch + (lane & 7) * 16;
#pragma unroll
    for (int i = 0; i < 3; i++) {
      const int row = min(152 * cpart + 8 * (w + 8 * i), 440);
      asm volatile("global_load_dwordx4 %0, %1, %2" : "=v"(pw[i]) : "v"(vo), "s"(cb + (size_t)row * pitch) : "memory");
    }
  }
  const unsigned swz = (((lane & 7) ^ rmask(16 * w + rl)) << 4);
  const unsigned voffX = (16 * w + rl) * 896 + swz, voffX2 = voffX + 8 * 896 - 1024;
  const rsrc_t rsX = mkrs(x + (size_t)m * 128 * 896, 128 * 896);
  const int rt = xcd * 4 + (r & 3), ct = t8;
  const rsrc_t rsV = mkrs(wd + ct * 128, 1792 * 896 - ct * 128);
  const unsigned ldsw = w * 2048;
  unsigned st0, st1;
#define ITEM_X(c, h) do { if (!(h)) DMA1I(voffX, rsX, ldsw, (7 + ((c) + 2) % 3) * SLOT, (c) * 128, st0); else DMA2(voffX2, rsX, st0, 1024); } while (0)
#define ITEM_V(c, h) do { if (!(h)) DMA1I(voffX, rsV, ldsw, (((c) + 7) % 10) * SLOT, (c) * (128 * 896), st0); else DMA2(voffX2, rsV, st0, 1024); } while (0)

  const int npass = (t8 == 4) ? 2 : 3, fi = th * 5 + t8;
  const int rb = w & 3, role = w >> 2;
  constexpr unsigned WSTR = 64 * 3584;
  const bool isq = t8 == 7;
  const int g = th * 2 + (t8 - 5);
  constexpr unsigned STG = 28672 + 16384;
  unsigned voffW[4];
  rsrc_t rsW;
  unsigned ldsa;
  if (t8 < 5) {
    voffW[0] = (16 * (w & 3) + rl) * 3584 + ((((((lane & 7) >> 1) ^ ((rl >> 1) & 1) ^ (role << 1)) << 1) | (lane & 1)) << 4);
    voffW[1] = voffW[0] + 8 * 3584 - 1024; voffW[2] = voffW[3] = 0;
    rsW = mkrs((role ? wu : wg) + fi * 128, 448 * 3584 - fi * 128);
    ldsa = 0;
  } else {
#pragma unroll
    for (int i = 0; i < 4; i++) {
      const int ci = 64 * (4 * w + i) + lane, wrow = ci / 28, wc = ci % 28;
      voffW[i] = wrow * 2688 + (isq ? 448 * th + 16 * wc : 896 + 224 * g + 16 * wc + (wc >= 14 ? 896 - 224 : 0)) - i * 1024;
    }
    rsW = mkrs(wqkv, w < 7 ? 448 * 2688 : 0);
    ldsa = w < 7 ? w * 4096 : LDS_BYTES;
  }
#define ITEM_W(slot, SOFF, h) do { if (!(h)) DMA1I(voffW[0], rsW, ldsw, (slot) * SLOT, SOFF, st1); else DMA2(voffW[1], rsW, st1, 1024); } while (0)
  if (t8 < 5) {
#pragma unroll
    for (int c = 0; c < 3; c++) { ITEM_X(c, 0); ITEM_X(c, 1); ITEM_W(c, c * WSTR, 0); ITEM_W(c, c * WSTR, 1); }
  } else {
#pragma unroll
    for (int c = 0; c < 2; c++) {
      DMA1I(voffX, rsX, ldsw, c * STG + 28672, c * 128, st0); DMA2(voffX2, rsX, st0, 1024);
      DMA1I(voffW[0], rsW, ldsa, c * STG, c * (64 * 2688), st1); DMA2(voffW[1], rsW, st1, 1024); DMA2(voffW[2], rsW, st1, 2048); DMA2(voffW[3], rsW, st1, 3072);
    }
  }
  TA(1);
  asm volatile("s_waitcnt vmcnt(12)" : "+v"(tag), "+v"(px), "+v"(pw[0]), "+v"(pw[1]), "+v"(pw[2])::"memory");
  TA(2);
  const unsigned ldsm = t8 < 5 ? LDS_MISC_F : LDS_MISC_A;
  {
    tag += 1;
    float e[8];
    for (int i = 0; i < 4; i++) { e[2 * i] = blo(px[i]); e[2 * i + 1] = bhi(px[i]); }
    float s = ((e[0] + e[1]) + (e[2] + e[3])) + ((e[4] + e[5]) + (e[6] + e[7]));
    s = wave_sum(lane < 56 ? s : 0.f);
    const float mu = s * (1.f / 448.f);
    float q = 0.f;
    for (int i = 0; i < 8; i++) { const float d = e[i] - mu; q = fmaf(d, d, q); }
    q = wave_sum(lane < 56 ? q : 0.f);
    const float rstd = __builtin_amdgcn_rsqf(q * (1.f / 448.f) + 1e-5f);
    if (lane == 0) { u32x4 o = {split2(-mu), tag, __float_as_uint(rstd), tag}; st16(rs, WS_STAT + (bx * 8 + w) * 16, o); }
    if (t8 < 5) {
#pragma unroll
      for (int c = 3; c < 6; c++) { ITEM_W(c, c * WSTR, 0); ITEM_W(c, c * WSTR, 1); }
    } else {
      const rsrc_t rsP = mkrs(wphi + (isq ? 8 * th : 4 * g) * 1568, (isq ? 8 : 4) * 1568);
      const unsigned vp = w * 1024 + lane * 16, lp = w * 1024;
      DMA1I(vp, rsP, lp, LDS_PHI, 0, st0); DMA1I(vp + 8192, rsP, lp, LDS_PHI + 8192, 0, st0);
    }
    const bool third = w + 16 < (cpart == 2 ? 18 : 19);
    float c[8];
    for (int i = 0; i < 4; i++) {
      c[2 * i] = blo(pw[0][i]) + blo(pw[1][i]) + (third ? blo(pw[2][i]) : 0.f);
      c[2 * i + 1] = bhi(pw[0][i]) + bhi(pw[1][i]) + (third ? bhi(pw[2][i]) : 0.f);
    }
    for (int i = 0; i < 4; i++) c[i] = add32(c[i], c[i + 4]);
    for (int i = 0; i < 2; i++) c[i] = add16(c[i], c[i + 2]);
    const bool b3 = lane & 8;
    const float cc = (b3 ? c[1] : c[0]) + dpp<0x128>(b3 ? c[0] : c[1]);
    *(LAS float*)(ldsm + w * 256 + ((lane & 7) * 8 + hb * 4 + g16 * 2 + (b3 ? 1 : 0)) * 4) = cc;
  }
  auto prephase2 = [&]() {
    if (w == 7 && cpart < 3) {
      float s = 0.f;
      for (int i = 0; i < 8; i++) s += *(LAS float*)(ldsm + i * 256 + lane * 4);
      u32x2 o = {split2(s), tag};
      __builtin_amdgcn_raw_buffer_store_b64(o, rs, WS_CS + cpart * CS_PART + (lc * 64 + lane) * 8, 0, 16);
    }
  };
  STAMP(10);

  const int m2 = rt >> 1;
  const unsigned vfl = (m2 * 3 * 128) + (lane & 15) * 4;
  f32x16 pa0, pa1;
  float k0, k1, ia2;
  unsigned ho = 0, fl0 = 0;

  if (t8 < 5) {
    const unsigned xrow = 32 * rb + l31;
    unsigned aW[4];
    aW[0] = g16 * 8192 + (8 * hb + q4) * 128 + (((2 * role) ^ ((q4 >> 1) & 1) ^ (g16 << 1)) * 32) + p4 * 8;
    aW[1] = aW[0] ^ 32; aW[2] = aW[0] + 65536; aW[3] = aW[1] + 65536;
    OPAQUE(aW[0]); OPAQUE(aW[1]); OPAQUE(aW[2]); OPAQUE(aW[3]);
    auto wfrag = [&](int n, int t) {
      const int sl = (n >> 2) % 7; const unsigned o = (sl & 3) * SLOT + (n & 3) * 2048;
      const unsigned b = aW[(sl >> 2) * 2 + t];
      return trfrag(b + o, b + o + 512);
    };
    const unsigned vcs = (1344 + g16 * 1792 + 32 * role + (lane & 15)) * 8;
    const unsigned vst = (m * 128 + xrow) * 16;
    s16x8 xf[28], wq[3][2], fb;
    u32x4 stt;
    CsG cg;
    f32x16 acc0 = zero16(), acc1 = zero16();
    {
      unsigned aX[4];
#pragma unroll
      for (int ks = 0; ks < 4; ks++) { aX[ks] = 7 * SLOT + xrow * 128 + (((2 * ks + hb) ^ rmask(xrow)) << 4); OPAQUE(aX[ks]); }
      auto step0 = [&](auto cc) {
        constexpr int c = decltype(cc)::value;
        waitv<(c == 0 ? 14 : c == 1 ? 10 : c == 2 ? 12 : c == 3 ? 8 : c == 4 ? 6 : 13)>();
        BAR();
        TK(c);
        if (c == 0) prephase2();
        xf[4 * c] = *(LAS s16x8*)(aX[0] + ((c + 2) % 3) * SLOT);
        wq[(4 * c) % 3][0] = wfrag(4 * c, 0); wq[(4 * c) % 3][1] = wfrag(4 * c, 1);
        xf[4 * c + 1] = *(LAS s16x8*)(aX[1] + ((c + 2) % 3) * SLOT);
        wq[(4 * c + 1) % 3][0] = wfrag(4 * c + 1, 0); wq[(4 * c + 1) % 3][1] = wfrag(4 * c + 1, 1);
#pragma unroll
        for (int ks = 0; ks < 4; ks++) {
          const int n = 4 * c + ks;
          if (ks < 2) xf[n + 2] = *(LAS s16x8*)(aX[ks + 2] + ((c + 2) % 3) * SLOT);
          if (ks < 2 || c == 6) { wq[(n + 2) % 3][0] = wfrag(n + 2, 0); wq[(n + 2) % 3][1] = wfrag(n + 2, 1); }
          SB();
          acc0 = MFMA(wq[n % 3][0], xf[n], acc0);
          if (c >= 1 && c <= 4 && ks == 0) ITEM_X(c + 2, 0);
          if (c == 1 && ks == 1) ITEM_W(6, 6 * WSTR, 0);
          if (c >= 1 && ks == (c <= 4 ? 2 : 0)) ITEM_W(c - 1, (c - 1) * WSTR + 10 * 128, 0);
          if (c >= 5 && ks == 2) ITEM_V(c - 5, 0);
          SB();
          acc1 = MFMA(wq[n % 3][1], xf[n], acc1);
          if (c >= 1 && c <= 4 && ks == 0) ITEM_X(c + 2, 1);
          if (c == 1 && ks == 1) ITEM_W(6, 6 * WSTR, 1);
          if (c >= 1 && ks == (c <= 4 ? 2 : 0)) ITEM_W(c - 1, (c - 1) * WSTR + 10 * 128, 1);
          if (c >= 5 && ks == 2) ITEM_V(c - 5, 1);
          if (c == 4 && ks == 1) { cs_issue(rs, cg, vcs + fi * 512, vcs + fi * 512 + 128); ALD16(stt, vst, WS_STAT); }
          SB();
        }
      };
      step0(IC<0>{}); step0(IC<1>{}); step0(IC<2>{}); step0(IC<3>{}); step0(IC<4>{}); step0(IC<5>{}); step0(IC<6>{});
      TK(7);
      CS_PIN(cg, "s_waitcnt vmcnt(10)");
      asm volatile("" : "+v"(stt));
      st_check(rs, stt, vst, tag);
      cs_check(rs, cg, vcs + fi * 512, vcs + fi * 512 + 128, tag);
      const float rstd = __uint_as_float(stt[2]);
      const float c0 = -2.f * 0.7978845608028654f * 1.4426950408889634f;
      k0 = c0 * rstd; k1 = c0 * 0.044715f * rstd * rstd * rstd; ia2 = __builtin_amdgcn_rcpf(rstd * rstd);
      fb = fold_b(stt[0], hi);
      pa0 = MFMA(fold_a(cg.a), fb, acc0);
      pa1 = MFMA(fold_a(cg.b), fb, acc1);
      ho = WS_AFRAG + ((m * 4 + rb) * 144 + fi * 4 + 2 * role) * 1024 + lane * 16;
      STAMP(1);
    }
    wq[0][0] = wq[1][0]; wq[0][1] = wq[1][1]; wq[1][0] = wq[2][0]; wq[1][1] = wq[2][1];
#pragma unroll 1
    for (int ps = 1; ps < npass; ps++) {
      const int hl = ps == 2 ? 20 + th * 4 + t8 : 10 + fi;
      const bool last = ps == npass - 1;
      const rsrc_t rsB = last ? rsV : rsW;
      const unsigned voffB = last ? voffX : voffW[0], voffB2 = last ? voffX2 : voffW[1];
      const unsigned sinc = last ? 128 * 896 : WSTR;
      unsigned sB = last ? 2 * 128 * 896 : (20 + th * 4 + t8 - fi) * 128 - WSTR;
      unsigned sA = (hl - fi) * 128; const unsigned sAinc = 6 * WSTR;
      acc0 = zero16(); acc1 = zero16();
      float ge, gr, gu0, gu1, hprev; u32x4 hq;
      auto stepn = [&](auto cc) {
        constexpr int c = decltype(cc)::value;
        if (c == 6) WAITV(2); else WAITV(8);
        BAR();
        TK(8 + c);
        if (c == 6 && tid == 0) publish(ws, WS_HFLAG + (m * 3 + ps - 1) * 128 + fi * 4, tag);
#pragma unroll
        for (int ks = 0; ks < 4; ks++) {
          const int n = 4 * c + ks;
          wq[(n + 2) % 3][0] = wfrag(n + 2, 0); wq[(n + 2) % 3][1] = wfrag(n + 2, 1);
          SB();
          acc0 = MFMA(wq[n % 3][0], xf[n], acc0);
          float z = 0.f, hv = 0.f;
          if (n >= 2 && n < 18) hv = gu0 * gr;
          if (n >= 1 && n < 17) gr = __builtin_amdgcn_rcpf(fmaf(ge, ia2, ia2));
          if (n < 16) { const float G = n < 8 ? pa0[n & 7] : pa1[n & 7]; z = G * fmaf(G * G, k1, k0); }
          if (ks == 0) { if (c == 0) DMA1A(voffW[0], rsW, ldsw, 6 * SLOT, sA, sAinc); else DMA1A(voffB, rsB, ldsw, (c - 1) * SLOT, sB, sinc); }
          SB();
          acc1 = MFMA(wq[n % 3][1], xf[n], acc1);
          gu0 = gu1;
          if (n < 16) { ge = __builtin_amdgcn_exp2f(z); gu1 = n < 8 ? pa0[n & 7] * pa0[(n & 7) + 8] : pa1[n & 7] * pa1[(n & 7) + 8]; }
          if (n >= 2 && n < 18) {
            if (n & 1) hq[((n - 2) >> 1) & 3] = pk2(hprev, hv); else hprev = hv;
            if (((n - 2) & 7) == 7) st16(rs, ho + ((n - 2) >> 3) * 1024, hq);
          }
          if (ks == 2) { if (c == 0) DMA2(voffW[1], rsW, sA, 1024); else DMA2(voffB2, rsB, sB, 1024); }
          if (c == 0 && ks == 3) { if (ps == 1) { ITEM_V(2, 0); ITEM_V(2, 1); } cs_issue(rs, cg, vcs + hl * 512, vcs + hl * 512 + 128); }
          if (c == 4 && ks == 3) ALD4(fl0, vfl, WS_HFLAG);
          SB();
        }
      };
      stepn(IC<0>{}); stepn(IC<1>{}); stepn(IC<2>{}); stepn(IC<3>{}); stepn(IC<4>{}); stepn(IC<5>{}); stepn(IC<6>{});
      wq[0][0] = wq[1][0]; wq[0][1] = wq[1][1]; wq[1][0] = wq[2][0]; wq[1][1] = wq[2][1];
      TK(15);
      CS_PIN(cg, "");
      asm volatile("" : "+v"(fl0));
      cs_check(rs, cg, vcs + hl * 512, vcs + hl * 512 + 128, tag);
      pa0 = MFMA(fold_a(cg.a), fb, acc0);
      pa1 = MFMA(fold_a(cg.b), fb, acc1);
      ho = WS_AFRAG + ((m * 4 + rb) * 144 + hl * 4 + 2 * role) * 1024 + lane * 16;
      STAMP(1 + ps);
    }
    DUMPTK();
  } else {
    const int rp = w & 1, hp = w >> 1;
    const int head0 = isq ? 8 * th + hp : 4 * g + hp;
    const unsigned xr0 = 64 * rp + l31;
    unsigned aX[4];
#pragma unroll
    for (int ks = 0; ks < 4; ks++) aX[ks] = 28672 + xr0 * 128 + (((2 * ks + hb) ^ rmask(xr0)) << 4);
    unsigned aW = (8 * hb + q4) * 448 + 56 * hp + g16 * 32 + p4 * 8;
    const unsigned vca = ((isq ? 0 : 448) + 28 * head0 + l31) * 8, vcb = vca + (isq ? 112 : 448) * 8;
    const unsigned vst = (m * 128 + xr0) * 16;
    CsG cg; u32x4 stt[2];
    f32x16 acc[2][2] = {{zero16(), zero16()}, {zero16(), zero16()}};
    unsigned sx = 128, sw = 64 * 2688, lx = ldsw + 2 * STG, lw = ldsa + 2 * STG, cstg = 0;
    const unsigned sxi = 128, swi = 64 * 2688;
    auto astep = [&](auto more) {
      constexpr bool M = decltype(more)::value;
      OPAQUE(aW); OPAQUE(aX[0]); OPAQUE(aX[1]); OPAQUE(aX[2]); OPAQUE(aX[3]);
      s16x8 x0 = *(LAS s16x8*)(aX[0]);
      s16x8 w0 = trfrag(aW, aW + 1792);
      s16x8 x1 = *(LAS s16x8*)(aX[0] + 4096);
      s16x8 w1 = trfrag(aW + 224, aW + 1792 + 224);
#pragma unroll
      for (int ks = 0; ks < 4; ks++) {
        s16x8 xn0 = x0, xn1 = x1, wn0 = w0, wn1 = w1;
        if (ks < 3) {
          xn0 = *(LAS s16x8*)(aX[ks + 1]);
          wn0 = trfrag(aW + (ks + 1) * 7168, aW + (ks + 1) * 7168 + 1792);
          xn1 = *(LAS s16x8*)(aX[ks + 1] + 4096);
          wn1 = trfrag(aW + (ks + 1) * 7168 + 224, aW + (ks + 1) * 7168 + 1792 + 224);
        }
        SB();
        acc[0][0] = MFMA(w0, x0, acc[0][0]); acc[0][1] = MFMA(w0, x1, acc[0][1]);
        if (M && ks == 0) DMA1A(voffX, rsX, lx, 28672, sx, sxi);
        if (M && ks == 1) DMA1A(voffW[0], rsW, lw, 0, sw, swi);
        if (M && ks == 2) DMA2(voffW[2], rsW, sw, 2048);
        SB();
        acc[1][0] = MFMA(w1, x0, acc[1][0]); acc[1][1] = MFMA(w1, x1, acc[1][1]);
        if (M && ks == 0) DMA2(voffX2, rsX, sx, 1024);
        if (M && ks == 1) DMA2(voffW[1], rsW, sw, 1024);
        if (M && ks == 2) DMA2(voffW[3], rsW, sw, 3072);
        if (ks == 3) {
          lx = ldsw + cstg; lw = ldsa + cstg;
          const unsigned d = cstg == 2 * STG ? -2 * STG : STG;
          cstg += d; aW += d; aX[0] += d; aX[1] += d; aX[2] += d; aX[3] += d;
        }
        SB();
        x0 = xn0; x1 = xn1; w0 = wn0; w1 = wn1;
      }
    };
    TA(3);
    WAITV(8); BAR(); prephase2();
    TA(4);
#pragma unroll 1
    for (int c = 0; c < 5; c++) { astep(std::true_type{}); WAITV(6); BAR(); }
    TA(5);
    cs_issue(rs, cg, vca, vcb); ALD16(stt[0], vst, WS_STAT); ALD16(stt[1], vst, WS_STAT + 512);
    astep(std::false_type{});
    WAITV(8); BAR();
    astep(std::false_type{});
    STAMP(1); TA(6);
    CS_PIN(cg, "s_waitcnt vmcnt(0)");
    asm volatile("" : "+v"(stt[0]), "+v"(stt[1]));
    cs_check(rs, cg, vca, vcb, tag);
    st_check(rs, stt[0], vst, tag); st_check(rs, stt[1], vst + 512, tag);
    float rstd[2];
    {
      const s16x8 fa0 = fold_a(cg.a), fa1 = fold_a(cg.b);
#pragma unroll
      for (int b = 0; b < 2; b++) {
        rstd[b] = __uint_as_float(stt[b][2]);
        const s16x8 fb = fold_b(stt[b][0], hi);
        acc[0][b] = MFMA(fa0, fb, acc[0][b]); acc[1][b] = MFMA(fa1, fb, acc[1][b]);
      }
    }
    s16x8 wf[2][2];
    {
      const unsigned pa = LDS_PHI + hp * 1568 + (4 * hb + q4) * 56 + g16 * 32 + p4 * 8;
#pragma unroll
      for (int u = 0; u < 2; u++) {
        const unsigned pu = pa + (isq ? u * 4 * 1568 : 0);
        wf[u][0] = trfrag(pu, pu + 448);
        u32x4 t = __builtin_bit_cast(u32x4, trfrag(pu + 896, pu + 1344));
        t[2] = hi ? 0u : t[2]; t[3] = hi ? 0u : t[3];
        wf[u][1] = __builtin_bit_cast(s16x8, t);
      }
    }
    if (!isq) {
      TA(7);
      BAR();
      TA(8);
      const unsigned img = 3 * SLOT + w * 6400;
      f32x16 kvp = zero16(), kvn = zero16();
#pragma unroll
      for (int b = 0; b < 2; b++) {
        f32x16 kk = acc[0][b], vv = acc[1][b];
        for (int i = 12; i < 16; i++) kk[i] = hi ? 0.f : kk[i];
        f32x16 pk = MFMA(wf[0][0], pack8(kk, 0), zero16());
        pk = MFMA(wf[0][1], pack8(kk, 1), pk);
        ITEM_V(3 * b, 0); ITEM_V(3 * b, 1);
        f32x16 pos, neg;
        dual_softmax(pk, rstd[b], rstd[b], hi, pos, neg);
        const unsigned ir = split2(__builtin_amdgcn_rcpf(rstd[b]));
        const unsigned wa = img + l31 * 200 + hb * 8;
#pragma unroll
        for (int gq = 0; gq < 4; gq++) {
          u32x2 a = {pk2(pos[4 * gq], pos[4 * gq + 1]), pk2(pos[4 * gq + 2], pos[4 * gq + 3])};
          u32x2 n = {pk2(neg[4 * gq], neg[4 * gq + 1]), pk2(neg[4 * gq + 2], neg[4 * gq + 3])};
          u32x2 v = {pk2(vv[4 * gq], vv[4 * gq + 1]), pk2(vv[4 * gq + 2], vv[4 * gq + 3])};
          if (gq == 3) { v[0] = hi ? ir : v[0]; v[1] = hi ? 0u : v[1]; }
          *(LAS u32x2*)(wa + gq * 16) = a; *(LAS u32x2*)(wa + 64 + gq * 16) = n; *(LAS u32x2*)(wa + 128 + gq * 16) = v;
        }
        ITEM_V(3 * b + 1, 0); ITEM_V(3 * b + 1, 1);
        asm volatile("s_waitcnt lgkmcnt(0)" ::: "memory");
        const unsigned ra = img + (8 * hb + q4) * 200 + g16 * 32 + p4 * 8;
#pragma unroll
        for (int s = 0; s < 2; s++) {
          s16x8 fp = trfrag(ra + s * 3200, ra + s * 3200 + 800);
          s16x8 fn = trfrag(ra + s * 3200 + 64, ra + s * 3200 + 864);
          s16x8 fv = trfrag(ra + s * 3200 + 128, ra + s * 3200 + 928);
          kvp = MFMA(fp, fv, kvp); kvn = MFMA(fn, fv, kvn);
        }
        ITEM_V(3 * b + 2, 0); ITEM_V(3 * b + 2, 1);
        asm volatile("s_waitcnt lgkmcnt(0)" ::: "memory");
        TA(9 + b);
      }
      STAMP(11);
      {
        const f32x16 snd = rp ? kvp : kvn;
        for (int gq = 0; gq < 4; gq++) { f32x4 v = {snd[4 * gq], snd[4 * gq + 1], snd[4 * gq + 2], snd[4 * gq + 3]}; *(LAS f32x4*)(img + gq * 1024 + lane * 16) = v; }
      }
      BAR();
      TA(11);
      {
        const f32x16 kp = rp ? kvn : kvp;
        for (int gq = 0; gq < 4; gq++) {
          f32x4 v = *(LAS f32x4*)(3 * SLOT + (w ^ 1) * 6400 + gq * 1024 + lane * 16);
          for (int i = 0; i < 4; i++) v[i] += kp[4 * gq + i];
          const int slice = hp * 4 + rp * 2 + (gq >> 1);
          st16(rs, WS_KVPART + ((g * 16 + slice) * 16 + m) * 2048 + (gq & 1) * 1024 + lane * 16, __builtin_bit_cast(u32x4, v));
        }
      }
      ALD4(fl0, vfl, WS_HFLAG);
      TA(12);
      asm volatile("s_waitcnt vmcnt(0)" : "+v"(fl0)::"memory");
      TA(13);
      BAR();
      TA(14); DUMPTK();
      if (tid == 0) publish(ws, WS_KVFLAG + g * 128 + m * 4, tag);
      STAMP(2);
      ITEM_V(6, 0); ITEM_V(6, 1); ITEM_V(7, 0); ITEM_V(7, 1); ITEM_V(8, 0); ITEM_V(8, 1);
    } else {
      s16x8 ph[2][2][4];
#pragma unroll
      for (int u = 0; u < 2; u++)
#pragma unroll
        for (int b = 0; b < 2; b++) {
          f32x16 qq = acc[u][b];
          for (int i = 12; i < 16; i++) qq[i] = hi ? 0.f : qq[i];
          f32x16 pq = MFMA(wf[u][0], pack8(qq, 0), zero16());
          pq = MFMA(wf[u][1], pack8(qq, 1), pq);
          f32x16 pos, neg;
          dual_softmax(pq, rstd[b], 1.f, hi, pos, neg);
          ph[u][b][0] = pack8(pos, 0); ph[u][b][1] = pack8(pos, 1); ph[u][b][2] = pack8(neg, 0); ph[u][b][3] = pack8(neg, 1);
          asm volatile("" : "+v"(ph[u][b][0]), "+v"(ph[u][b][1]), "+v"(ph[u][b][2]), "+v"(ph[u][b][3]));
        }
      STAMP(2); TA(7);
      if (w < 4) {
        const int gg = 2 * th + (w >> 1);
        poll(ws, WS_KVFLAG + gg * 128 + (lane & 15) * 4, true, tag);
        STAMP(5); TA(8);
        const unsigned pb = WS_KVPART + (gg * 16 + m) * 16 * 2048 + (w & 1) * 1024 + lane * 16;
        u32x4 pv[16];
#pragma unroll
        for (int i = 0; i < 16; i++) pv[i] = ld16(rs, pb + i * 2048);
        SB();
        f32x4 s0 = __builtin_bit_cast(f32x4, pv[0]);
#pragma unroll
        for (int i = 1; i < 16; i++) s0 += __builtin_bit_cast(f32x4, pv[i]);
        u32x2 o = {pk2(s0[0], s0[1]), pk2(s0[2], s0[3])};
        __builtin_amdgcn_raw_buffer_store_b64(o, rs, WS_KVRED + (gg * 16 + m) * 1024 + lane * 16 + (w & 1) * 8, 0, 16);
        TA(9);
        WAITV(0);
        TA(10);
        if (lane == 0) publish(ws, WS_RDFLAG + gg * 128 + (m * 2 + (w & 1)) * 4, tag);
        STAMP(6);
      }
      poll(ws, WS_RDFLAG + (2 * th + ((lane >> 3) & 1)) * 128 + (hp * 8 + (lane & 7)) * 4, true, tag);
      STAMP(3); TA(11);
      u32x4 kf[2][4];
#pragma unroll
      for (int u = 0; u < 2; u++)
#pragma unroll
        for (int i = 0; i < 4; i++) kf[u][i] = ld16(rs, WS_KVRED + ((head0 + 4 * u) * 4 + i) * 1024 + lane * 16);
      SB();
#pragma unroll
      for (int u = 0; u < 2; u++)
#pragma unroll
        for (int b = 0; b < 2; b++) {
          f32x16 nm = zero16();
          for (int i = 0; i < 4; i++) nm = MFMA(__builtin_bit_cast(s16x8, kf[u][i]), ph[u][b][i], nm);
          const float dh = hi ? nm[12] + nm[13] : 0.f;
          const float rd = __builtin_amdgcn_rcpf(add32(dh, dh) + 1e-6f);
          for (int i = 0; i < 16; i++) nm[i] *= rd;
          for (int i = 12; i < 16; i++) nm[i] = hi ? 0.f : nm[i];
          const unsigned oo = WS_AFRAG + ((m * 4 + 2 * rp + b) * 144 + 112 + (head0 + 4 * u) * 2) * 1024 + lane * 16;
          st16(rs, oo, __builtin_bit_cast(u32x4, pack8(nm, 0)));
          st16(rs, oo + 1024, __builtin_bit_cast(u32x4, pack8(nm, 1)));
        }
      TA(12);
      WAITV(0); TA(13); BAR();
      TA(14); DUMPTK();
      if (tid == 0) publish(ws, WS_OFLAG + m * 128 + th * 4, tag);
      STAMP(4);
      if (bx == 7 * 4 * 8 && w == 0) {
#pragma unroll 1
        for (int i = 0; i < 4; i++) { const int mm = i * 4 + (lane >> 4); poll(ws, WS_HFLAG + mm * 3 * 128 + (lane & 15) * 4, (lane & 15) < 10, tag); }
        poll(ws, WS_OFLAG + (lane & 15) * 128 + ((lane >> 4) & 1) * 4, true, tag);
        if (lane == 0) publish(ws, WS_EPOCH, tag);
      }
      return;
    }
  }

  {
    const int rb2 = w & 1, kq = w >> 1;
    const unsigned ab = ((rt * 2 + rb2) * 144 + 2 * kq) * 1024 + lane * 16;
    const bool a10 = (lane & 15) < 10;
    u32x4 af[36];
#define LDA2(c) asm volatile("s_mov_b32 %2, %5\n\tbuffer_load_dwordx4 %0, %3, %4, %2 offen sc1\n\tbuffer_load_dwordx4 %1, %3, %4, %2 offen offset:1024 sc1" \
    : "=&v"(af[2 * (c)]), "=&v"(af[2 * (c) + 1]), "=&s"(st1) : "v"(ab), "s"(rs), "n"(WS_AFRAG + (c) * 8192) : "memory")
    if (!__all(!a10 || fl0 == tag)) poll(ws, WS_HFLAG + vfl, a10, tag);
    STAMP(5);
    LDA2(0); LDA2(1);
    if (t8 < 5) {
#pragma unroll
      for (int t = 0; t < 2; t++) {
        const f32x16& a = t ? pa1 : pa0;
        float hv[8];
#pragma unroll
        for (int i = 0; i < 8; i++) {
          const float e = __builtin_amdgcn_exp2f(a[i] * fmaf(a[i] * a[i], k1, k0));
          hv[i] = (a[i] * a[i + 8]) * __builtin_amdgcn_rcpf(fmaf(e, ia2, ia2));
        }
        u32x4 o = {pk2(hv[0], hv[1]), pk2(hv[2], hv[3]), pk2(hv[4], hv[5]), pk2(hv[6], hv[7])};
        st16(rs, ho + t * 1024, o);
      }
    }
    unsigned fl;
    ALD4(fl, vfl, WS_HFLAG + 128);
    LDA2(2); LDA2(3);
    const unsigned r0 = 32 * kq + 4 * hb + q4;
    unsigned bV[3][4];
#pragma unroll
    for (int i = 0; i < 12; i++) { bV[i >> 2][i & 3] = ((r0 * 128 + (((2 * g16 + (p4 >> 1)) ^ rmask(r0)) << 4) + (p4 & 1) * 8) ^ ((i & 1) * 64) ^ ((i & 2) * 16)) + (i >> 2) * 65536; OPAQUE(bV[i >> 2][i & 3]); }
    const unsigned voffO = (28 * (w >> 1) + 16 * (w & 1) + rl) * 896 + swz;
    const unsigned voffO2 = voffO + (((w & 1) && rl >= 4) ? 0 : 8 * 896) - 1024;
    const rsrc_t rsO = mkrs(wo + ct * 128, 448 * 896 - ct * 128);
    const size_t go = (size_t)(rt * 64 + (tid >> 3)) * 896 + ct * 128 + (tid & 7) * 16;
    u32x4 xr;
    f32x16 acc0 = zero16(), acc1 = zero16();
    auto nops = [](int s) constexpr { return (s + 9 < 18 ? 2 : 0) + (s + 4 < 18 ? 2 : 0) + ((s == 3 || s == 7) ? 1 : 0) + (s == 14 ? 1 : 0); };
    auto step = [&](auto cc) {
      constexpr int c = decltype(cc)::value;
      constexpr int N = c == 0 ? 4 : (c == 1 ? 4 + nops(0) : (c == 2 ? 2 + nops(0) + nops(1) : (c == 3 ? nops(0) + nops(1) + nops(2) : nops(c - 3) + nops(c - 2) + nops(c - 1))));
      constexpr int N2 = (c == 6 || c == 10) ? 2 + nops(c - 2) + nops(c - 1) : N;
      asm volatile("s_waitcnt vmcnt(%3)" : "+v"(af[2 * c]), "+v"(af[2 * c + 1]), "+v"(fl) : "n"(N2) : "memory");
      if (c <= 12) BAR();
      if (c == 0 && t8 < 5 && tid == 0) publish(ws, WS_HFLAG + (m * 3 + npass - 1) * 128 + fi * 4, tag);
      constexpr int sl = (c + 7) % 10; constexpr unsigned so = (sl & 3) * SLOT;
      s16x8 wv[4];
#pragma unroll
      for (int i = 0; i < 4; i++) wv[i] = trfrag(bV[sl >> 2][i] + so + (i >> 1) * 2048, bV[sl >> 2][i] + so + (i >> 1) * 2048 + 1024);
      SB();
      acc0 = MFMA(wv[0], __builtin_bit_cast(s16x8, af[2 * c]), acc0); acc1 = MFMA(wv[1], __builtin_bit_cast(s16x8, af[2 * c]), acc1);
      if (c + 9 < 14) { ITEM_V(c + 9, 0); ITEM_V(c + 9, 1); }
      else if (c + 9 < 18) { DMA1I(voffO, rsO, ldsw, ((c + 16) % 10) * SLOT, (c + 9 - 14) * (112 * 896), st0); DMA2(voffO2, rsO, st0, 1024); }
      SB();
      acc0 = MFMA(wv[2], __builtin_bit_cast(s16x8, af[2 * c + 1]), acc0); acc1 = MFMA(wv[3], __builtin_bit_cast(s16x8, af[2 * c + 1]), acc1);
      if (c == 1) { if (!__all(!a10 || fl == tag)) poll(ws, WS_HFLAG + 128 + vfl, a10, tag); STAMP(6); }
      if (c == 6) { const bool a8 = a10 && (lane & 15) != 4 && (lane & 15) != 9; if (!__all(!a8 || fl == tag)) poll(ws, WS_HFLAG + 256 + vfl, a8, tag); STAMP(7); }
      if (c == 10) { if (!__all(fl == tag)) poll(ws, WS_OFLAG + m2 * 128 + (lane & 1) * 4, true, tag); STAMP(12); }
      if (c == 3) ALD4(fl, vfl, WS_HFLAG + 256);
      if (c == 7) ALD4(fl, m2 * 128 + (lane & 1) * 4, WS_OFLAG);
      if constexpr (c + 4 < 18) LDA2(c + 4);
      if (c == 14) asm volatile("global_load_dwordx4 %0, %1, off" : "=v"(xr) : "v"(x + go) : "memory");
      SB();
    };
    step(IC<0>{}); step(IC<1>{}); step(IC<2>{}); step(IC<3>{}); step(IC<4>{}); step(IC<5>{}); step(IC<6>{}); step(IC<7>{}); step(IC<8>{});
    step(IC<9>{}); step(IC<10>{}); step(IC<11>{}); step(IC<12>{}); step(IC<13>{}); step(IC<14>{}); step(IC<15>{}); step(IC<16>{}); step(IC<17>{});
    STAMP(8);
    BAR();
    const unsigned wa = (kq * 64 + rb2 * 32 + l31) * 272 + hb * 16;
#pragma unroll
    for (int gq = 0; gq < 4; gq++) {
      f32x4 v0 = {acc0[4 * gq], acc0[4 * gq + 1], acc0[4 * gq + 2], acc0[4 * gq + 3]};
      f32x4 v1 = {acc1[4 * gq], acc1[4 * gq + 1], acc1[4 * gq + 2], acc1[4 * gq + 3]};
      *(LAS f32x4*)(wa + gq * 32) = v0; *(LAS f32x4*)(wa + 128 + gq * 32) = v1;
    }
    BAR();
    const unsigned ra = (tid >> 3) * 272 + (tid & 7) * 32;
    f32x4 s0 = *(LAS f32x4*)ra, s1 = *(LAS f32x4*)(ra + 16);
#pragma unroll
    for (int k = 1; k < 4; k++) { s0 += *(LAS f32x4*)(ra + k * 64 * 272); s1 += *(LAS f32x4*)(ra + k * 64 * 272 + 16); }
    asm volatile("s_waitcnt vmcnt(0)" : "+v"(xr)::"memory");
    u32x4 o;
    o[0] = pk2(s0[0] + blo(xr[0]), s0[1] + bhi(xr[0]));
    o[1] = pk2(s0[2] + blo(xr[1]), s0[3] + bhi(xr[1]));
    o[2] = pk2(s1[0] + blo(xr[2]), s1[1] + bhi(xr[2]));
    o[3] = pk2(s1[2] + blo(xr[3]), s1[3] + bhi(xr[3]));
    *(u32x4*)(out + go) = o;
    STAMP(9); STAMPC(15);
  }
}

extern "C" void kernel_launch(void* const* d_in, const int* in_sizes, int n_in, void* d_out, int out_size, void* d_ws, size_t ws_size, hipStream_t stream) {
  (void)hipFuncSetAttribute((const void*)fused_kernel, hipFuncAttributeMaxDynamicSharedMemorySize, LDS_BYTES);
  fused_kernel<<<256, 512, LDS_BYTES, stream>>>((const char*)d_in[0], (const char*)d_in[1], (const char*)d_in[2], (const char*)d_in[3],
                                                (const char*)d_in[5], (const char*)d_in[6], (const char*)d_in[7], (char*)d_out, (char*)d_ws);
}
```

```cpp
#include <hip/hip_runtime.h>
#include <hip/hip_bf16.h>
#include <stdint.h>
#include <type_traits>
#pragma clang diagnostic ignored "-Wint-to-pointer-cast"
#pragma clang diagnostic ignored "-Warray-bounds"

#define DI __device__ __forceinline__
#define LAS __attribute__((address_space(3)))
#define GAS __attribute__((address_space(1)))
typedef __attribute__((ext_vector_type(8))) short s16x8;
typedef __attribute__((ext_vector_type(4))) short s16x4;
typedef __attribute__((ext_vector_type(16))) float f32x16;
typedef __attribute__((ext_vector_type(4))) float f32x4;
typedef __attribute__((ext_vector_type(2))) float f32x2;
typedef __attribute__((ext_vector_type(4))) unsigned u32x4;
typedef __attribute__((ext_vector_type(2))) unsigned u32x2;
typedef __attribute__((ext_vector_type(2))) __bf16 bf16x2;
typedef GAS unsigned gu32;
typedef __amdgpu_buffer_rsrc_t rsrc_t;
#define RLX_AGENT __ATOMIC_RELAXED, __HIP_MEMORY_SCOPE_AGENT

constexpr unsigned WS_EPOCH = 0;
constexpr unsigned WS_CFLAG = 1024;
constexpr unsigned WS_AFLAG = 16384;
constexpr unsigned WS_STAT = WS_AFLAG + 128 * 128;
constexpr unsigned WS_CS = WS_STAT + 2048 * 16;
constexpr unsigned WS_KVRED = 262144;
constexpr unsigned WS_KVPART = WS_KVRED + 131072;
constexpr unsigned WS_AFRAG = WS_KVPART + (2u << 20);
constexpr unsigned WS_STAMP = WS_AFRAG + 64u * 144u * 1024u;
constexpr unsigned WS_BYTES = WS_STAMP;
static_assert(WS_CFLAG + 96 * 128 <= WS_AFLAG && WS_CS + 4928 * 32 <= WS_KVRED, "ws map");

constexpr int LDS_BYTES = 163840;
constexpr unsigned SLOT = 16384;
constexpr unsigned LDS_MISC = 0, LDS_PHI = 2048;
constexpr int SPIN_CAP = 1 << 14;

#define HOTA(c) (c)
#define HOTV(c) (c)
#define MFMA(a, b, c) __builtin_amdgcn_mfma_f32_32x32x16_bf16((a), (b), (c), 0, 0, 0)
#define WAITV(n) asm volatile("s_waitcnt vmcnt(" #n ")" ::: "memory")
#define BAR() do { asm volatile("s_waitcnt lgkmcnt(0)" ::: "memory"); __builtin_amdgcn_s_barrier(); asm volatile("" ::: "memory"); } while (0)
#define SB() __builtin_amdgcn_sched_barrier(0)
#define OPAQUE(v) asm volatile("" : "+v"(v))
template <int N> DI void waitv() { asm volatile("s_waitcnt vmcnt(%0)" ::"n"(N) : "memory"); }
template <int N> using IC = std::integral_constant<int, N>;
template <int I, int N, class F> DI void sfor(F&& f) { if constexpr (I < N) { f(IC<I>{}); sfor<I + 1, N>(f); } }

#define STAMP(i)
#define STAMPC(i)
#define TK(i)
#define TK2(i)
#define DUMPTK()

#define DMA1I(voff, rsrc, ldsb, LIMM, SOFF, tmp) asm volatile("s_add_u32 m0, %3, %4\n\ts_mov_b32 %0, %5\n\tbuffer_load_dwordx4 %1, %2, %0 offen lds" \
    : "=&s"(tmp) : "v"(voff), "s"(rsrc), "s"(ldsb), "n"(LIMM), "n"(SOFF) : "memory", "scc")
#define DMA1A(voff, rsrc, ldsb, LIMM, soff, sinc) asm volatile("s_add_u32 m0, %3, %4\n\ts_add_u32 %0, %0, %5\n\tbuffer_load_dwordx4 %1, %2, %0 offen lds" \
    : "+s"(soff) : "v"(voff), "s"(rsrc), "s"(ldsb), "n"(LIMM), "s"(sinc) : "memory", "scc")
#define DMA2(voff, rsrc, soff) asm volatile("buffer_load_dwordx4 %0, %1, %2 offen offset:1024 lds" :: "v"(voff), "s"(rsrc), "s"(soff) : "memory")
#define ALD16(dst, voff, soff) asm volatile("buffer_load_dwordx4 %0, %1, %2, %3 offen sc1" : "=v"(dst) : "v"(voff), "s"(rs), "s"(soff) : "memory")
#define ALD4(dst, voff, soff) asm volatile("buffer_load_dword %0, %1, %2, %3 offen sc1" : "=v"(dst) : "v"(voff), "s"(rs), "s"(soff) : "memory")

#define FRESH_LANE() int lane; asm volatile("v_mbcnt_lo_u32_b32 %0, -1, 0\n\tv_mbcnt_hi_u32_b32 %0, -1, %0" : "=v"(lane)); \
  const int g16 = (lane >> 4) & 1, q4 = (lane & 15) >> 2, p4 = lane & 3, l31 = lane & 31, hb = lane >> 5, rl = lane >> 3; \
  (void)g16; (void)q4; (void)p4; (void)l31; (void)hb; (void)rl
DI rsrc_t mkrs(const void* p, unsigned bytes) { return __builtin_amdgcn_make_buffer_rsrc((void*)p, 0, bytes, 0x00020000); }
DI s16x8 trfrag(unsigned a0, unsigned a1) {
  s16x4 lo = __builtin_amdgcn_ds_read_tr16_b64_v4i16((LAS s16x4*)a0);
  s16x4 hi = __builtin_amdgcn_ds_read_tr16_b64_v4i16((LAS s16x4*)a1);
  return __builtin_shufflevector(lo, hi, 0, 1, 2, 3, 4, 5, 6, 7);
}
DI unsigned pk2(float a, float b) { f32x2 p = {a, b}; return __builtin_bit_cast(unsigned, __builtin_convertvector(p, bf16x2)); }
DI float dot2(unsigned a, unsigned b, float c) { return __builtin_amdgcn_fdot2_f32_bf16(__builtin_bit_cast(bf16x2, a), __builtin_bit_cast(bf16x2, b), c, false); }
DI float blo(unsigned u) { return __uint_as_float(u << 16); }
DI float bhi(unsigned u) { return __uint_as_float(u & 0xffff0000u); }
DI float fand(float v, unsigned m) { return __uint_as_float(__float_as_uint(v) & m); }
DI float vmul(float a, float b) { float r = a * b; OPAQUE(r); return r; }
DI float vadd(float a, float b) { float r = a + b; OPAQUE(r); return r; }
DI float max3(float a, float b, float c) { return __builtin_elementwise_maximum(__builtin_elementwise_maximum(a, b), c); }
DI float min3(float a, float b, float c) { return __builtin_elementwise_minimum(__builtin_elementwise_minimum(a, b), c); }
DI float add32(float a, float b) { auto r = __builtin_amdgcn_permlane32_swap(__float_as_uint(a), __float_as_uint(b), false, false); return __uint_as_float(r[0]) + __uint_as_float(r[1]); }
DI float add16(float a, float b) { auto r = __builtin_amdgcn_permlane16_swap(__float_as_uint(a), __float_as_uint(b), false, false); return __uint_as_float(r[0]) + __uint_as_float(r[1]); }
DI void bcast32(float v, float& lo, float& hi) { auto r = __builtin_amdgcn_permlane32_swap(__float_as_uint(v), __float_as_uint(v), false, false); lo = __uint_as_float(r[0]); hi = __uint_as_float(r[1]); }
template <int CTRL> DI float dpp(float x) { return __builtin_bit_cast(float, __builtin_amdgcn_mov_dpp(__builtin_bit_cast(int, x), CTRL, 0xf, 0xf, true)); }
DI float wave_sum(float v) {
  v += dpp<0xB1>(v); v += dpp<0x4E>(v); v += dpp<0x141>(v); v += dpp<0x128>(v);
  v = add16(v, v); return add32(v, v);
}
DI unsigned split2(float v) {
  const unsigned h = pk2(v, 0.f);
  return pk2(__uint_as_float(h << 16), v - __uint_as_float(h << 16));
}
DI u32x4 pack8u(const f32x16& x, int s) {
  u32x4 u = {pk2(x[8 * s], x[8 * s + 1]), pk2(x[8 * s + 2], x[8 * s + 3]), pk2(x[8 * s + 4], x[8 * s + 5]), pk2(x[8 * s + 6], x[8 * s + 7])};
  return u;
}
DI s16x8 asf(u32x4 u) { return __builtin_bit_cast(s16x8, u); }
DI unsigned rmask(unsigned row) { return (((row >> 1) & 1) << 2) | ((row >> 2) & 1) | ((row >> 3) & 2); }
DI f32x16 zero16() { f32x16 z; for (int i = 0; i < 16; i++) z[i] = 0.f; return z; }
DI void st16(rsrc_t rs, unsigned off, u32x4 v) { __builtin_amdgcn_raw_buffer_store_b128(v, rs, off, 0, 16); }
DI void st4(rsrc_t rs, unsigned off, unsigned v) { __builtin_amdgcn_raw_buffer_store_b32(v, rs, off, 0, 16); }
DI u32x4 ld16(rsrc_t rs, unsigned off) { return __builtin_amdgcn_raw_buffer_load_b128(rs, off, 0, 16); }
DI bool seen(unsigned v, unsigned tag, unsigned long long need) { return (__builtin_amdgcn_ballot_w64(v == tag) & need) == need; }
DI void poll(char* ws, unsigned off, unsigned long long need, unsigned tag) {
#pragma unroll 1
  for (int it = 0; it < SPIN_CAP; ++it) {
    const unsigned v = __hip_atomic_load((gu32*)(ws + off), RLX_AGENT);
    if (seen(v, tag, need)) break;
    __builtin_amdgcn_s_sleep(2);
  }
  __builtin_amdgcn_fence(__ATOMIC_ACQUIRE, "wavefront");
}
template <bool ST> DI void g_check(rsrc_t rs, u32x4& a, u32x4& b, u32x4& st, unsigned va, unsigned vb, unsigned vs, unsigned tag, unsigned mlo) {
#pragma unroll 1
  for (int it = 0; it < SPIN_CAP; ++it) {
    unsigned bad = (a[1] ^ tag) | (b[1] ^ tag) | (((a[3] ^ tag) | (b[3] ^ tag)) & mlo);
    if (ST) bad |= (st[1] ^ tag) | (st[3] ^ tag);
    if (__builtin_expect(__builtin_amdgcn_ballot_w64(bad != 0) == 0ull, 1)) break;
    ALD16(a, va, WS_CS); ALD16(b, vb, WS_CS); if (ST) ALD16(st, vs, WS_STAT);
    if (ST) asm volatile("s_waitcnt vmcnt(0)" : "+v"(a), "+v"(b), "+v"(st)::"memory"); else asm volatile("s_waitcnt vmcnt(0)" : "+v"(a), "+v"(b)::"memory");
  }
}

DI unsigned umax4(unsigned m, const u32x4& v) { return max(max(max(m, v[0]), v[1]), max(v[2], v[3])); }
DI bool fresh(unsigned m) { return __builtin_amdgcn_ballot_w64(m == 0xffffffffu) == 0ull; }

struct Sm { float L, cx, cn, mx, mn, s1, s2; f32x16 p, ep, en; };
DI void sm_minmax(Sm& s, float ninf) {
  const f32x16& p = s.p;
  float a = max3(p[0], p[1], p[2]), b = min3(p[0], p[1], p[2]);
  for (int i = 3; i < 11; i += 2) { a = max3(a, p[i], p[i + 1]); b = min3(b, p[i], p[i + 1]); }
  float c = max3(p[12], p[13], p[14]), d = min3(p[12], p[13], p[14]);
  c = max3(c, p[15], p[15]) + ninf; d = min3(d, p[15], p[15]) - ninf;
  s.mx = max3(a, p[11], c); s.mn = min3(b, p[11], d);
}
DI void sm_consts(Sm& s, float sc) {
  auto r = __builtin_amdgcn_permlane32_swap(__float_as_uint(s.mx), __float_as_uint(-s.mn), false, false);
  const float m = max3(__uint_as_float(r[0]), __uint_as_float(r[1]), __uint_as_float(r[1]));
  float mx, nmn; bcast32(m, mx, nmn);
  s.L = sc * 1.4426950408889634f; s.cx = -mx * s.L; s.cn = -nmn * s.L; s.s1 = 0.f; s.s2 = 0.f;
}
DI void sm_exp(Sm& s, int i, float ninf) {
  if (i == 12) { s.cx += ninf; s.cn += ninf; }
  s.ep[i] = __builtin_amdgcn_exp2f(fmaf(s.p[i], s.L, s.cx)); s.en[i] = __builtin_amdgcn_exp2f(fmaf(s.p[i], -s.L, s.cn));
  if (i > 0) { s.s1 = vadd(s.s1, s.ep[i - 1]); s.s2 = vadd(s.s2, s.en[i - 1]); }
}
DI void sm_sum(Sm& s, float os) {
  const float t = add32(vadd(s.s1, s.ep[15]), vadd(s.s2, s.en[15]));
  bcast32(os * __builtin_amdgcn_rcpf(t), s.s1, s.s2);
}
DI u32x4 sm_norm(const Sm& s, int f) {
  const f32x16& e = f < 2 ? s.ep : s.en; const float r = f < 2 ? s.s1 : s.s2; const int o = 8 * (f & 1);
  u32x4 u;
  for (int i = 0; i < 4; i++) u[i] = pk2(vmul(e[o + 2 * i], r), vmul(e[o + 2 * i + 1], r));
  return u;
}

struct P2 {
  static constexpr int a0(int s) { return s <= 2 ? 5 : s == 3 ? 7 : s + 5 < 18 ? s + 5 : 18; }
  static constexpr int vs(int j) { return j < 2 ? j : j == 2 ? 4 : j == 3 ? 5 : j == 4 ? 7 : j == 5 ? 8 : j == 6 ? 10 : 12; }
  static constexpr int vj(int s) { for (int j = 0; j < 8; j++) if (vs(j) == s) return j; return -1; }
  static constexpr int FL1 = 0, N1 = 2, FL2 = 6, N2 = 9;
  static constexpr int LASTBAR = 16;
  static constexpr bool bar(int c) { return c <= 2 || vj(c) >= 0 || (c >= 9 && c <= LASTBAR); }
  static constexpr int na(int s) { return 2 * (a0(s + 1) - a0(s)); }
  static constexpr int nops(int s) { return (vj(s) >= 0 ? 2 : 0) + na(s) + (s == 13 || s == FL1 || s == FL2 ? 1 : 0); }
  static constexpr int upto(int from, int c) { int n = 0; for (int s = from; s < c; s++) n += nops(s); return n; }
  static constexpr int wait(int c) {
    int n = 63;
    if (c >= 2 && c < 5) n = 2 * (4 - c) + upto(0, c);
    for (int s = 0; s < c; s++) if (a0(s) <= c && c < a0(s + 1)) n = 2 * (a0(s + 1) - 1 - c) + upto(s + 1, c);
    if (c == N1 || c == N2) { const int f = upto(c == N1 ? FL1 : FL2, c) - 1; if (f < n) n = f; }
    if (c + 1 >= 10 && c + 1 < 18) { const int s = vs(c - 9), v = na(s) + upto(s + 1, c); if (v < n) n = v; }
    return n > 63 ? 63 : n;
  }
};

__global__ __launch_bounds__(512, 2) void fused_kernel(const char* __restrict__ x, const char* __restrict__ wqkv, const char* __restrict__ wphi,
                                                       const char* __restrict__ wo, const char* __restrict__ wg, const char* __restrict__ wu,
                                                       const char* __restrict__ wd, char* __restrict__ out, char* ws) {
  const int tid = threadIdx.x, lane = tid & 63, w = __builtin_amdgcn_readfirstlane(tid >> 6);
  const int bx = blockIdx.x, xcd = bx & 7, r = bx >> 3;
  const int th = xcd & 1, m = (xcd >> 1) * 4 + (r & 3), t8 = r >> 2;
  const int g16 = (lane >> 4) & 1, q4 = (lane & 15) >> 2, p4 = lane & 3, l31 = lane & 31, hb = lane >> 5, rl = lane >> 3;
  STAMP(0); STAMPC(14); TK(0);
  unsigned st0, st1;
  const unsigned ldsw = w * 2048;
  const unsigned swz = (((lane & 7) ^ rmask(16 * w + rl)) << 4);
  const unsigned voffX = (16 * w + rl) * 896 + swz, voffX2 = voffX + 8 * 896 - 1024;
  const rsrc_t rsX = mkrs(x + (size_t)m * 128 * 896, 128 * 896);
#define ITEM_X(c, h) do { if (!(h)) DMA1I(voffX, rsX, ldsw, (7 + ((c) + 2) % 3) * SLOT, (c) * 128, st0); else DMA2(voffX2, rsX, st0); } while (0)
#define ITEM_V(c, h) do { if (!(h)) DMA1I(voffX, rsV, ldsw, (((c) + 7) % 10) * SLOT, HOTV(c) * (128 * 896), st0); else DMA2(voffX2, rsV, st0); } while (0)
  ITEM_X(0, 0); ITEM_X(0, 1);

  const bool ffn = t8 < 5, isq = t8 == 7;
  const int fi = th * 5 + t8, g = th * 2 + (t8 - 5);
  const int rb = w & 3, role = w >> 2;
  unsigned voffW, voffW2, wstr, wnext;
  rsrc_t rsW;
  if (ffn) {
    voffW = (16 * (w & 3) + rl) * 3584 + ((((((lane & 7) >> 1) ^ ((rl >> 1) & 1) ^ (role << 1)) << 1) | (lane & 1)) << 4);
    voffW2 = voffW + 8 * 3584 - 1024;
    rsW = mkrs((role ? wu : wg) + fi * 128, 448 * 3584 - fi * 128);
    wstr = 64 * 3584; wnext = 10 * 128;
  } else {
    const int c0 = 128 * w + lane, c1 = c0 + 64;
    const int r0 = c0 / 28, k0 = c0 - 28 * r0, r1 = c1 / 28, k1 = c1 - 28 * r1;
    voffW = r0 * 2688 + k0 * 16 + (k0 >= 14 ? 32 * 2688 - 224 : 0);
    voffW2 = r1 * 2688 + k1 * 16 + (k1 >= 14 ? 32 * 2688 - 224 : 0) - 1024;
    const int co = isq ? 448 * th : 896 + 224 * g;
    rsW = mkrs(wqkv + co, w < 7 ? 448 * 2688 - co : 0);
    wstr = 64 * 2688; wnext = isq ? 224 : 896;
  }
  DMA1I(voffW, rsW, ldsw, 0, 0, st1); DMA2(voffW2, rsW, st1);

  const rsrc_t rs = mkrs(ws, WS_BYTES);
  unsigned tag;
  const int lc = bx % 77, cpart = bx / 77;
  const int rt = xcd * 4 + (r & 3), ct = t8;
  const rsrc_t rsV = mkrs(wd + ct * 128, isq ? 0 : 1792 * 896 - ct * 128);
  const unsigned mlo = lane < 32 ? 0xffffffffu : 0u;
  const unsigned vp = w * 1024 + lane * 16, lp = w * 1024;

  const int rbg = m * 4 + rb;
  const int rb2 = w & 1, kq = w >> 1;
#define VFL (((rt * 2 + rb2) * 2 + (kq & 1)) * 128 + l31 * 4)
#define AB (((rt * 2 + rb2) * 144 + 2 * kq) * 1024 + lane * 16)
  u32x4 af[36];
#define LDA2(c) asm volatile("s_mov_b32 %2, %5\n\tbuffer_load_dwordx4 %0, %3, %4, %2 offen sc1\n\tbuffer_load_dwordx4 %1, %3, %4, %2 offen offset:1024 sc1" \
    : "=&v"(af[2 * (c)]), "=&v"(af[2 * (c) + 1]), "=&s"(st1) : "v"(AB), "s"(rs), "n"(WS_AFRAG + HOTA(c) * 8192) : "memory")
#define PIN4(c) "+v"(af[2 * (c)]), "+v"(af[2 * (c) + 1]), "+v"(af[2 * (c) + 2]), "+v"(af[2 * (c) + 3])
#define PIN8(c) "+v"(af[2 * (c)]), "+v"(af[2 * (c) + 1]), "+v"(af[2 * (c) + 2]), "+v"(af[2 * (c) + 3]), "+v"(af[2 * (c) + 4]), "+v"(af[2 * (c) + 5]), "+v"(af[2 * (c) + 6]), "+v"(af[2 * (c) + 7])
#define NEED(mask) do { if (!seen(fl, tag, mask)) poll(ws, WS_AFLAG + VFL, mask, tag); } while (0)
  f32x16 pa0, pa1;
  float k0, k1, ia2;
  unsigned fl = 0;

  const unsigned xrow = 32 * rb + l31;
  const unsigned vst = (m * 128 + xrow) * 16;
  s16x8 xf[28], wq[3][2], fb, wf[2][2];
  u32x4 stt, ca, cb;
  unsigned aW[4];
  auto fold = [&](u32x4 c) { const unsigned c2 = c[2] & mlo; u32x4 u = {c[0], c2, c[0], c2}; return asf(u); };
  auto make_fb = [&]() { const unsigned a = stt[0] & 0xffffu, b = stt[0] >> 16; u32x4 u = {a | (a << 16), a | (a << 16), b | (b << 16), b | (b << 16)}; fb = asf(u); };

  auto wfrag = [&](auto att, int n, int t) {
    const int sl = (n >> 2) % 7;
    if constexpr (decltype(att)::value) {
      const unsigned o = (sl & 3) * SLOT + (n & 1) * 7168 + ((n >> 1) & 1) * 224 + t * 56, b = aW[sl >> 2];
      return trfrag(b + o, b + o + 1792);
    } else {
      const unsigned o = (sl & 3) * SLOT + (n & 3) * 2048, b = aW[(sl >> 2) * 2 + t];
      return trfrag(b + o, b + o + 512);
    }
  };
  auto pass0 = [&](auto att, unsigned vca, unsigned vcb) {
    constexpr bool A = decltype(att)::value;
    constexpr unsigned WS_ = A ? 64 * 2688 : 64 * 3584;
#define ITEM_W(c, h) do { if (!(h)) DMA1I(voffW, rsW, ldsw, (c) * SLOT, (c) * WS_, st1); else DMA2(voffW2, rsW, st1); } while (0)
#define ITEM_N(c, h) do { if (!(h)) { st1 = (c) * WS_; DMA1A(voffW, rsW, ldsw, (c) * SLOT, st1, wnext); } else DMA2(voffW2, rsW, st1); } while (0)
    u32x4 px, pw[3];
    {
      asm volatile("buffer_load_dword %0, off, %1, 0 sc1" : "=v"(tag) : "s"(rs) : "memory");
      asm volatile("global_load_dwordx4 %0, %1, %2" : "=v"(px) : "v"(min(lane, 55) * 16), "s"(x + (size_t)(bx * 8 + w) * 896) : "memory");
      const char* cb = lc < 21 ? wqkv + lc * 128 : (lc < 49 ? wg + (lc - 21) * 128 : wu + (lc - 49) * 128);
      const unsigned pitch = lc < 21 ? 2688 : 3584;
      const unsigned vo = rl * pitch + (lane & 7) * 16;
#pragma unroll
      for (int i = 0; i < 3; i++) {
        const int row = min(152 * cpart + 8 * (w + 8 * i), 440);
        asm volatile("global_load_dwordx4 %0, %1, %2" : "=v"(pw[i]) : "v"(vo), "s"(cb + (size_t)row * pitch) : "memory");
      }
    }
    ITEM_X(1, 0); ITEM_X(1, 1);
    ITEM_W(1, 0); ITEM_W(1, 1);
    TK(1);
    unsigned aX[4];
#pragma unroll
    for (int ks = 0; ks < 4; ks++) { aX[ks] = 7 * SLOT + xrow * 128 + (((2 * ks + hb) ^ rmask(xrow)) << 4); OPAQUE(aX[ks]); }
    auto xread = [&](int n) { xf[n] = *(LAS s16x8*)(aX[n & 3] + (((n >> 2) + 2) % 3) * SLOT); };
    f32x16 acc0 = zero16(), acc1 = zero16();
    const unsigned m56 = lane < 56 ? 0xffffffffu : 0u;
    float cs[8], s = 0.f, q = 0.f; unsigned pfd;
    WAITV(9);
    BAR();
    TK(4);
    xread(0); wq[0][1] = wfrag(att, 0, 1); wq[0][0] = wfrag(att, 0, 0);
    xread(1); wq[1][1] = wfrag(att, 1, 1); wq[1][0] = wfrag(att, 1, 0);
    ITEM_X(2, 0); ITEM_X(2, 1);
    auto pre = [&](int p) {
      if (p == 4) { tag += 1; for (int i = 0; i < 4; i++) px[i] &= m56; }
      if (p == 5) { for (int i = 0; i < 4; i++) { s = dot2(px[i], 0x3f803f80u, s); q = dot2(px[i], px[i], q); } OPAQUE(s); OPAQUE(q); }
      if (p == 6) { s = add32(s, q); s += dpp<0xB1>(s); OPAQUE(s); }
      if (p == 7) { s += dpp<0x4E>(s); s += dpp<0x141>(s); OPAQUE(s); }
      if (p == 8) { s += dpp<0x128>(s); s = add16(s, s); OPAQUE(s); }
      if (p == 9) { bcast32(s * (1.f / 448.f), s, q); q = fmaxf(fmaf(-s, s, q), 0.f) + 1e-5f; OPAQUE(s); OPAQUE(q); }
      if (p == 10) {
        u32x4 o = {split2(-s), tag, __float_as_uint(__builtin_amdgcn_rsqf(q)), tag};
        st16(rs, lane == 0 ? WS_STAT + (bx * 8 + w) * 16 : 0xfffffff0u, o);
      }
      if (p >= 12 && p < 20) {
        const int i = (p - 12) >> 1;
        if (!(p & 1)) { pw[2][i] &= (w + 16 < (cpart == 2 ? 18 : 19) ? 0xffffffffu : 0u); cs[2 * i] = vadd(vadd(blo(pw[0][i]), blo(pw[1][i])), blo(pw[2][i])); }
        else cs[2 * i + 1] = vadd(vadd(bhi(pw[0][i]), bhi(pw[1][i])), bhi(pw[2][i]));
      }
      if (p == 20 || p == 21) for (int i = 2 * (p - 20); i < 2 * (p - 19); i++) { cs[i] = add32(cs[i], cs[i + 4]); OPAQUE(cs[i]); }
      if (p == 22) for (int i = 0; i < 2; i++) { cs[i] = add16(cs[i], cs[i + 2]); OPAQUE(cs[i]); }
      if (p == 23) for (int i = 0; i < 2; i++) { cs[i] += dpp<0x128>(cs[i]); OPAQUE(cs[i]); }
      if (p == 24) {
        *(LAS f32x2*)(LDS_MISC + w * 256 + ((lane & 7) * 8 + hb * 4 + g16 * 2) * 4) = f32x2{cs[0], cs[1]};
        if (A) *(LAS u32x2*)(LDS_PHI + 4 * 1568 + lane * 8) = u32x2{0u, 0u};
      }
    };
    sfor<0, 28>([&](auto nn) {
      constexpr int n = decltype(nn)::value;
      constexpr int c = (n + 2) >> 2, j = (n + 2) & 3;
      if (j == 0) {
        if (c == 1) asm volatile("s_waitcnt vmcnt(6)" : "+v"(tag), "+v"(px), "+v"(pw[0]), "+v"(pw[1]), "+v"(pw[2])::"memory");
        if (c == 2 || c == 3) WAITV(7); if (c == 4) WAITV(6); if (c == 5 || c == 6) WAITV(9); if (c == 7) asm volatile("s_waitcnt vmcnt(4)" : "+v"(pfd)::"memory");
        BAR();
        TK(4 + c);
      }
      if (A && n == 15) {
        const unsigned pa = LDS_PHI + 2 * role * 1568 + (4 * hb + q4) * 56 + g16 * 32 + p4 * 8;
        for (int u = 0; u < 2; u++) { wf[u][0] = trfrag(pa + u * 1568, pa + u * 1568 + 448); wf[u][1] = trfrag(pa + u * 1568 + 896, pa + u * 1568 + 1344); }
      }
      if (n == 14 && w == 7 && cpart < 3) {
        float t = 0.f;
        for (int i = 0; i < 8; i++) t += *(LAS float*)(LDS_MISC + i * 256 + lane * 4);
        u32x2 o = {split2(t), tag};
        __builtin_amdgcn_raw_buffer_store_b64(o, rs, WS_CS + (lc * 64 + lane) * 32 + cpart * 8, 0, 16);
      }
#define P0_ISSUE(h) do { \
      if (n < 2) ITEM_W(2 + n, h); \
      if (c >= 1 && c <= 4 && j == 0) ITEM_X(c + 2, h); \
      if (c >= 1 && c <= 3 && j == 2) ITEM_W(c + 3, h); \
      if (c == 4 && j >= 2) ITEM_N(j - 1, h); \
      if (c == 5 && j == 0) ITEM_N(0, h); \
      if (c == 5 && j == 2) ITEM_N(3, h); \
      if (c == 6 && j == 0) ITEM_N(4, h); } while (0)
      SB();
      if (n == 27) pa0 = MFMA(wq[n % 3][0], xf[n], acc0); else acc0 = MFMA(wq[n % 3][0], xf[n], acc0);
      SB();
      if (n + 2 < 28) xread(n + 2);
      wq[(n + 2) % 3][1] = wfrag(att, n + 2, 1);
      pre(2 * n);
      P0_ISSUE(0);
      if (n == 5) {
        const rsrc_t rsP = mkrs(wphi + (isq ? 8 * th : 4 * g) * 1568, A ? 4 * 1568 : 0);
        DMA1I(vp, rsP, lp, A ? LDS_PHI : LDS_BYTES, 0, st0);
      }
      if (n == 15) {
        const int u = bx * 8 + w;
        const rsrc_t rsF = u < 1568 ? mkrs(wd + u * 1024, 1024) : mkrs(wo + (u - 1568) * 1024, u < 1960 ? 1024 : 0);
        asm volatile("buffer_load_dword %0, %1, %2, 0 offen" : "=v"(pfd) : "v"((lane & 7) * 128), "s"(rsF) : "memory");
      }
      if (n == 26) ALD16(ca, vca, WS_CS);
      if (n == 27) ALD16(stt, vst, WS_STAT);
      SB();
      if (n == 27) pa1 = MFMA(wq[n % 3][1], xf[n], acc1); else acc1 = MFMA(wq[n % 3][1], xf[n], acc1);
      SB();
      wq[(n + 2) % 3][0] = wfrag(att, n + 2, 0);
      pre(2 * n + 1);
      P0_ISSUE(1);
      if (n == 26) ALD16(cb, vcb, WS_CS);
      SB();
    });
    wq[0][0] = wq[1][0]; wq[0][1] = wq[1][1]; wq[1][0] = wq[2][0]; wq[1][1] = wq[2][1];
    STAMP(1); TK(12);
  };

  if (ffn) {
    FRESH_LANE();
    aW[0] = g16 * 8192 + (8 * hb + q4) * 128 + (((2 * role) ^ ((q4 >> 1) & 1) ^ (g16 << 1)) * 32) + p4 * 8;
    aW[1] = aW[0] ^ 32; aW[2] = aW[0] + 65536; aW[3] = aW[1] + 65536;
    OPAQUE(aW[0]); OPAQUE(aW[1]); OPAQUE(aW[2]); OPAQUE(aW[3]);
    const unsigned vcs = (1344 + g16 * 1792 + 32 * role + (lane & 15)) * 32 + hb * 16;
    pass0(std::false_type{}, vcs + fi * 2048, vcs + fi * 2048 + 512);
    const int hl2 = 20 + th * 4 + t8;
    const unsigned hb0 = WS_AFRAG + (rbg * 144 + fi * 4 + 2 * role) * 1024 + lane * 16;
    const unsigned pf = lane == 0 ? WS_AFLAG + (rbg * 2 + role) * 128 + fi * 4 : 0xffffff00u;
    const bool two = t8 == 4;
    auto passn = [&](auto first) {
      constexpr bool F = decltype(first)::value, L = !F;
      const rsrc_t rsB = two ? rsV : rsW;
      unsigned voffB = voffW, voffB2 = voffW2;
      if (F && two) { voffB = voffX; voffB2 = voffX2; OPAQUE(voffB); }
      const unsigned sinc = two ? 128 * 896 : 64 * 3584;
      const unsigned ho = F ? hb0 : hb0 + 10 * 4096, vcp = vcs + (F ? fi : 10 + fi) * 2048, vcn = vcs + (F ? 10 + fi : hl2) * 2048;
      unsigned sA = (F ? 10 : hl2 - fi) * 128 + 4 * 64 * 3584, sB = two ? 2 * 128 * 896 : (hl2 - fi) * 128 - 64 * 3584;
      const unsigned c2 = two ? 0xf0000000u : hb0 + (hl2 - fi) * 4096;
      f32x16 acc0 = zero16(), acc1 = zero16();
      f32x16 qa0, qa1;
      float gt[2], gu[2], hprev; u32x4 hq;
      auto geglu = [&](int hs) {
#pragma unroll
        for (int e = 0; e < 16; e++) {
          const int d = hs - ((F ? 12 : 4) + 12 * e / 5);
          const float G = e < 8 ? qa0[e & 7] : qa1[e & 7], U = e < 8 ? qa0[(e & 7) + 8] : qa1[(e & 7) + 8];
          if (d == 0) gt[e & 1] = vmul(G, fmaf(G * G, k1, k0));
          if (d == 1) { gt[e & 1] = __builtin_amdgcn_exp2f(gt[e & 1]); gu[e & 1] = vmul(G, U); }
          if (d == 2) gt[e & 1] = __builtin_amdgcn_rcpf(fmaf(gt[e & 1], ia2, ia2));
          if (d == 3) {
            if (e & 1) hq[(e >> 1) & 3] = pk2(hprev, gu[1] * gt[1]); else hprev = vmul(gu[0], gt[0]);
            if ((e & 7) == 7) st16(rs, ho + (e >> 3) * 1024, hq);
          }
        }
      };
      sfor<0, 28>([&](auto nn) {
        constexpr int n = decltype(nn)::value;
        if (n == 10) { if (!F) { WAITV(12); st4(rs, pf, tag); } BAR(); TK(13); }
        if (n == 18) { asm volatile("s_waitcnt vmcnt(8)" : "+v"(ca), "+v"(cb)::"memory"); BAR(); TK(14); }
        if (n == 26) { if (F) WAITV(7); BAR(); TK(15); }
        if (n == (F ? 4 : 0)) {
          if (F) asm volatile("s_waitcnt vmcnt(5)" : "+v"(ca), "+v"(cb), "+v"(stt)::"memory");
          g_check<F>(rs, ca, cb, stt, vcp, vcp + 512, vst, tag, mlo);
        }
        SB();
        if (n == 27) pa0 = MFMA(wq[n % 3][0], xf[n], acc0); else acc0 = MFMA(wq[n % 3][0], xf[n], acc0);
        SB();
        if (!L || n + 2 < 28) wq[(n + 2) % 3][1] = wfrag(std::false_type{}, n + 2, 1);
        if (F && n == 4) make_fb();
        if (n == (F ? 5 : 1)) qa0 = MFMA(fold(ca), fb, pa0);
        if (n == 0 || n == 2) DMA1A(voffW, rsW, ldsw, (5 + n / 2) * SLOT, sA, wstr);
        if (n == 1 || n == 3) DMA2(voffW2, rsW, sA);
        if (L && n >= 4 && n < 16) ITEM_V((n - 4) / 2, n & 1);
        if (L && (n == 18 || n == 19)) ITEM_V(6, n & 1);
        if (L && (n == 21 || n == 22)) ITEM_V(7, !(n & 1));
        if (L && n >= 26) ITEM_V(n - 18, 0);
        if (!L && (n == 10 || n == 12 || n == 14)) DMA1A(voffB, rsB, ldsw, ((n - 10) / 2) * SLOT, sB, sinc);
        if (!L && (n == 18 || n == 21)) DMA1A(voffB, rsB, ldsw, (3 + (n - 18) / 3) * SLOT, sB, sinc);
        if (!L && (n == 11 || n == 13 || n == 15 || n == 19 || n == 22)) DMA2(voffB2, rsB, sB);
        geglu(2 * n);
        SB();
        if (n == 27) pa1 = MFMA(wq[n % 3][1], xf[n], acc1); else acc1 = MFMA(wq[n % 3][1], xf[n], acc1);
        SB();
        if (!L || n + 2 < 28) wq[(n + 2) % 3][0] = wfrag(std::false_type{}, n + 2, 0);
        if (F && n == 4) {
          const float rstd = __uint_as_float(stt[2]);
          const float c0 = -2.f * 0.7978845608028654f * 1.4426950408889634f;
          k0 = vmul(c0, rstd); k1 = vmul(c0 * 0.044715f * rstd * rstd, rstd); ia2 = __builtin_amdgcn_rcpf(rstd * rstd);
        }
        if (n == (F ? 5 : 1)) qa1 = MFMA(fold(cb), fb, pa1);
        if (F && (n == 0 || n == 17)) st16(rs, c2 + (n & 1) * 1024, u32x4{~0u, ~0u, ~0u, ~0u});
        if (!F && n == 20) ALD4(fl, VFL, WS_AFLAG);
        if (n == 8) ALD16(ca, vcn, WS_CS);
        if (n == 9) ALD16(cb, vcn + 512, WS_CS);
        if (L && n >= 26) ITEM_V(n - 18, 1);
        geglu(2 * n + 1);
        SB();
      });
      if (!L) { wq[0][0] = wq[1][0]; wq[0][1] = wq[1][1]; wq[1][0] = wq[2][0]; wq[1][1] = wq[2][1]; }
      STAMP(F ? 2 : 3); TK(16);
    };
    passn(std::true_type{});
    if (two) { ITEM_V(0, 0); ITEM_V(0, 1); ITEM_V(1, 0); ITEM_V(1, 1); ITEM_V(2, 0); ITEM_V(2, 1); ITEM_V(8, 0); ITEM_V(8, 1); ITEM_V(9, 0); ITEM_V(9, 1); WAITV(10); st4(rs, pf, tag); fl = ~tag; }
    int t8o = t8; asm volatile("" : "+s"(t8o));
    if (t8o != 4) passn(std::false_type{});
    {
      const unsigned vcn = vcs + (two ? 10 + fi : hl2) * 2048, hs = hb0 + (two ? 10 : hl2 - fi) * 4096;
      TK2(0);
      g_check<false>(rs, ca, cb, stt, vcn, vcn + 512, vst, tag, mlo);
      pa0 = MFMA(fold(ca), fb, pa0); pa1 = MFMA(fold(cb), fb, pa1);
      asm volatile("s_waitcnt vmcnt(7)" : "+v"(fl)::"memory");
      const bool early = seen(fl, tag, 0x3ffull);
      u32x4 o[2];
#pragma unroll
      for (int t = 0; t < 2; t++) {
        const f32x16& a = t ? pa1 : pa0;
        float hv[8];
        if (t == 0) { LDA2(0); LDA2(1); } else { TK2(1); WAITV(9); TK2(2); st4(rs, two ? 0xffffff00u : pf + 40, tag); }
#pragma unroll
        for (int i = 0; i < 8; i++) {
          const float e = __builtin_amdgcn_exp2f(a[i] * fmaf(a[i] * a[i], k1, k0));
          hv[i] = (a[i] * a[i + 8]) * __builtin_amdgcn_rcpf(fmaf(e, ia2, ia2));
        }
        o[t] = u32x4{pk2(hv[0], hv[1]), pk2(hv[2], hv[3]), pk2(hv[4], hv[5]), pk2(hv[6], hv[7])};
        OPAQUE(o[t]); SB();
      }
      st16(rs, hs, o[0]); st16(rs, hs + 1024, o[1]);
      TK2(3);
      asm volatile("s_waitcnt vmcnt(3)" : PIN4(0)::"memory");
      TK2(4);
      if (!early) {
        WAITV(0); st4(rs, two ? pf + 40 : 0xffffff00u, tag);
        poll(ws, WS_AFLAG + VFL, 0x3ffull, tag); LDA2(0); LDA2(1);
        asm volatile("s_waitcnt vmcnt(0)" : PIN4(0)::"memory");
      }
      if (two) BAR();
    }
  } else {
    aW[0] = (8 * hb + q4) * 448 + 112 * role + g16 * 32 + p4 * 8; aW[1] = aW[0] + 65536;
    OPAQUE(aW[0]); OPAQUE(aW[1]);
    const int head0 = (isq ? 8 * th : 4 * g) + 2 * role;
    const unsigned vcs = ((isq ? 0 : 448) + 28 * head0 + l31) * 32 + hb * 16;
    pass0(std::true_type{}, vcs, vcs + 28 * 32);
    float rstd, os;
    const float ninf = __uint_as_float(~mlo & 0xff800000u);
    u32x4 ph[2][4];
    Sm sm;
    {
      unsigned sA = wnext + 4 * 64 * 2688;
      const unsigned vca = vcs + wnext * 16;
      const unsigned oc = isq ? WS_AFRAG + (rbg * 144 + 112 + head0 * 2) * 1024 + lane * 16 : 0xf0000000u;
      f32x16 acc0 = zero16(), acc1 = zero16();
      f32x16 qa0, qa1;
      u32x4 kf[2][2];
      f32x16 p1;
      sfor<0, 28>([&](auto nn) {
        constexpr int n = decltype(nn)::value;
        if (n == 10) { BAR(); TK(13); }
        if (n == 18) {
          asm volatile("s_waitcnt vmcnt(2)" : "+v"(ca), "+v"(cb)::"memory"); BAR(); TK(14);
          st4(rs, tid == 0 ? WS_CFLAG + ((isq ? 4 + th : g) * 16 + m) * 128 : 0xf0000000u, tag);
        }
        if (n == 26) { BAR(); TK(15); }
        if (n == 6 || n == 11) {
          FRESH_LANE();
          const unsigned co = isq ? (lane < 16 && n == 6 ? WS_KVRED + ((2 * th + (w >> 2)) * 16 + m) * 1024 + (w & 3) * 256 + lane * 16 : 0xf0000000u)
                                  : WS_KVPART + ((g * 8 + w) * 16 + m) * 2048 + (n & 1) * 1024 + lane * 16;
          st16(rs, co, u32x4{~0u, ~0u, ~0u, ~0u});
        }
        if (n == 4) {
          asm volatile("s_waitcnt vmcnt(4)" : "+v"(ca), "+v"(cb), "+v"(stt)::"memory");
          g_check<true>(rs, ca, cb, stt, vcs, vcs + 28 * 32, vst, tag, mlo);
        }
        if (n == 19) g_check<false>(rs, ca, cb, stt, vca, vca + 28 * 32, vst, tag, mlo);
        SB();
        if (n == 27) pa0 = MFMA(wq[n % 3][0], xf[n], acc0); else acc0 = MFMA(wq[n % 3][0], xf[n], acc0);
        SB();
        if (n + 2 < 28) wq[(n + 2) % 3][1] = wfrag(std::true_type{}, n + 2, 1);
        if (n == 4) { make_fb(); rstd = __uint_as_float(stt[2]); os = isq ? 1.f : rstd; }
        if (n == 5) qa0 = MFMA(fold(ca), fb, pa0);
        if (n == 7 || n == 8) kf[n - 7][0] = pack8u(n == 8 ? qa1 : qa0, 0);
        if (n == 8) sm.p = MFMA(wf[0][0], asf(kf[0][0]), zero16());
        if (n == 18) p1 = MFMA(wf[1][0], asf(kf[1][0]), zero16());
        if (n == 10) sm_minmax(sm, ninf);
        if (n == 11) sm_consts(sm, rstd);
        if (n >= 12 && n < 20) sm_exp(sm, 2 * (n - 12), ninf);
        if (n == 20) sm_sum(sm, os);
        if (n >= 21 && n < 25) ph[0][n - 21] = sm_norm(sm, n - 21);
        if (n == 25) sm_consts(sm, rstd);
        if (n >= 26) sm_exp(sm, 2 * (n - 26), ninf);
        if (n == 0 || n == 2) DMA1A(voffW, rsW, ldsw, (5 + n / 2) * SLOT, sA, wstr);
        if (n == 1 || n == 3) DMA2(voffW2, rsW, sA);
        if (n >= 4 && n < 10) ITEM_V((n - 4) / 2, n & 1);
        if (n == 10) {
          const rsrc_t rsP = mkrs(wphi + (8 * th + 4) * 1568, isq ? 4 * 1568 : 0);
          FRESH_LANE();
          DMA1I(lp + lane * 16, rsP, lp, 0, 0, st0);
        }
        if (n == 20) acc0 = MFMA(fold(ca), fb, acc0);
        SB();
        if (n == 27) pa1 = MFMA(wq[n % 3][1], xf[n], acc1); else acc1 = MFMA(wq[n % 3][1], xf[n], acc1);
        SB();
        if (n + 2 < 28) wq[(n + 2) % 3][0] = wfrag(std::true_type{}, n + 2, 0);
        if (n == 5) qa1 = MFMA(fold(cb), fb, pa1);
        if (n >= 5 && n % 3 == 2) { constexpr int i = (n - 5) / 3; __builtin_amdgcn_raw_buffer_store_b128(u32x4{~0u, ~0u, ~0u, ~0u}, rs, oc, (i & 3) * 1024 + (i >> 2) * 8192, 16); }
        if (n == 8) ALD16(ca, vca, WS_CS);
        if (n == 9) ALD16(cb, vca + 28 * 32, WS_CS);
        if (n == 7 || n == 8) { kf[n - 7][1] = pack8u(n == 8 ? qa1 : qa0, 1); kf[n - 7][1][2] &= mlo; kf[n - 7][1][3] &= mlo; }
        if (n == 8) sm.p = MFMA(wf[0][1], asf(kf[0][1]), sm.p);
        if (n == 18) p1 = MFMA(wf[1][1], asf(kf[1][1]), p1);
        if (n >= 12 && n < 20) sm_exp(sm, 2 * (n - 12) + 1, ninf);
        if (n == 20) { sm.p = p1; sm_minmax(sm, ninf); }
        if (n >= 26) sm_exp(sm, 2 * (n - 26) + 1, ninf);
        if (n == 20) acc1 = MFMA(fold(cb), fb, acc1);
        SB();
      });
      for (int i = 4; i < 16; i++) sm_exp(sm, i, ninf);
      sm_sum(sm, os);
    }
    STAMP(2); TK(16);
    FRESH_LANE();
    const unsigned xrow = 32 * rb + l31;
    if (!isq) {
      const unsigned ir = split2(__builtin_amdgcn_rcpf(rstd)) & ~mlo;
      const unsigned wa = (2 * role * 128 + xrow) * 200 + hb * 8;
#pragma unroll
      for (int u = 0; u < 2; u++) {
        if (u) for (int f = 0; f < 4; f++) ph[1][f] = sm_norm(sm, f);
#pragma unroll
        for (int f = 0; f < 6; f++) {
          u32x4 v = f < 4 ? ph[u][f] : pack8u(u ? pa1 : pa0, f - 4);
          if (f == 5) { v[2] = (v[2] & mlo) | ir; v[3] &= mlo; }
          *(LAS u32x2*)(wa + u * 25600 + f * 32) = u32x2{v[0], v[1]}; *(LAS u32x2*)(wa + u * 25600 + f * 32 + 16) = u32x2{v[2], v[3]};
        }
      }
      BAR();
      TK(17);
      unsigned ra = (w >> 1) * 25600 + (8 * hb + q4) * 200 + g16 * 32 + p4 * 8, rp = ra + (w & 1) * 64;
      OPAQUE(ra); OPAQUE(rp);
      f32x16 kv = zero16();
#pragma unroll
      for (int s = 0; s < 8; s++) kv = MFMA(trfrag(rp + s * 3200, rp + s * 3200 + 800), trfrag(ra + 128 + s * 3200, ra + 128 + s * 3200 + 800), kv);
      ALD4(fl, VFL, WS_AFLAG);
      st16(rs, WS_KVPART + ((g * 8 + w) * 16 + m) * 2048 + lane * 16, pack8u(kv, 0));
      st16(rs, WS_KVPART + ((g * 8 + w) * 16 + m) * 2048 + 1024 + lane * 16, pack8u(kv, 1));
      STAMP(3); TK(18);
      BAR();
      asm volatile("s_waitcnt vmcnt(2)" : "+v"(fl)::"memory");
      NEED(0x3ffull);
      LDA2(0); LDA2(1);
      sfor<3, 10>([&](auto cc) { ITEM_V(decltype(cc)::value, 0); ITEM_V(decltype(cc)::value, 1); });
      asm volatile("s_waitcnt vmcnt(14)" : PIN4(0)::"memory");
      TK(19);
    } else {
      u32x4 ph2[2][4];
      const unsigned vcf = (lane < 32 ? 2 * th * 16 + lane : (4 + th) * 16 + (lane & 15)) * 128;
      ALD4(fl, vcf, WS_CFLAG);
      for (int f = 0; f < 4; f++) ph[1][f] = sm_norm(sm, f);
      {
        const unsigned pa = 2 * role * 1568 + (4 * hb + q4) * 56 + g16 * 32 + p4 * 8;
#pragma unroll
        for (int u = 0; u < 2; u++) {
          u32x4 k1 = pack8u(u ? pa1 : pa0, 1); k1[2] &= mlo; k1[3] &= mlo;
          sm.p = MFMA(trfrag(pa + u * 1568, pa + u * 1568 + 448), asf(pack8u(u ? pa1 : pa0, 0)), zero16());
          sm.p = MFMA(trfrag(pa + u * 1568 + 896, pa + u * 1568 + 1344), asf(k1), sm.p);
          sm_minmax(sm, ninf); sm_consts(sm, rstd);
          for (int i = 0; i < 16; i++) sm_exp(sm, i, ninf);
          sm_sum(sm, 1.f);
          for (int f = 0; f < 4; f++) { ph2[u][f] = sm_norm(sm, f); OPAQUE(ph2[u][f]); }
        }
      }
      STAMP(3); TK(17);
      asm volatile("s_waitcnt vmcnt(0)" : "+v"(fl)::"memory");
      st4(rs, l31 == 0 ? WS_AFLAG + (rbg * 2 + hb) * 128 + (28 + th * 2 + role) * 4 : 0xf0000000u, tag);
      if (!seen(fl, tag, ~0ull)) poll(ws, WS_CFLAG + vcf, ~0ull, tag);
      {
        const int gg = 2 * th + (w >> 2);
        const unsigned pb = WS_KVPART + ((gg * 8 + (m >> 1)) * 16 + (lane >> 4)) * 2048 + (m & 1) * 1024 + ((w & 3) * 16 + (lane & 15)) * 16;
        u32x4 pv[4];
#pragma unroll 1
        for (int it = 0; it < SPIN_CAP; ++it) {
          unsigned mx = 0;
#pragma unroll
          for (int i = 0; i < 4; i++) pv[i] = ld16(rs, pb + i * 8192);
#pragma unroll
          for (int i = 0; i < 4; i++) mx = umax4(mx, pv[i]);
          if (fresh(mx)) break;
          __builtin_amdgcn_s_sleep(1);
        }
        STAMP(5); TK(18);
        float t[8];
#pragma unroll
        for (int j = 0; j < 4; j++) {
          t[2 * j] = (blo(pv[0][j]) + blo(pv[1][j])) + (blo(pv[2][j]) + blo(pv[3][j]));
          t[2 * j + 1] = (bhi(pv[0][j]) + bhi(pv[1][j])) + (bhi(pv[2][j]) + bhi(pv[3][j]));
        }
        for (int j = 0; j < 8; j++) { t[j] = add32(t[j], t[j]); t[j] = add16(t[j], t[j]); }
        st16(rs, lane < 16 ? WS_KVRED + (gg * 16 + m) * 1024 + ((w & 3) * 16 + lane) * 16 : 0xf0000000u, u32x4{pk2(t[0], t[1]), pk2(t[2], t[3]), pk2(t[4], t[5]), pk2(t[6], t[7])});
        STAMP(6); TK(19);
      }
      {
        u32x4 gv[4];
        __builtin_amdgcn_s_sleep(12);
#pragma unroll 1
        for (int it = 0; it < SPIN_CAP; ++it) {
          unsigned mx = 0;
#pragma unroll
          for (int i = 0; i < 4; i++) gv[i] = ld16(rs, WS_KVRED + ((8 * th + w) * 4 + i) * 1024 + lane * 16);
#pragma unroll
          for (int i = 0; i < 4; i++) mx = umax4(mx, gv[i]);
          if (fresh(mx)) break;
          __builtin_amdgcn_s_sleep(4);
        }
#pragma unroll
        for (int i = 0; i < 4; i++) *(LAS u32x4*)(SLOT + (w * 4 + i) * 1024 + lane * 16) = gv[i];
      }
      BAR();
      STAMP(7); TK(20);
      u32x4 kf[4][4];
#pragma unroll
      for (int u = 0; u < 4; u++)
#pragma unroll
        for (int i = 0; i < 4; i++) kf[u][i] = *(LAS u32x4*)(SLOT + ((2 * role + (u & 1) + 4 * (u >> 1)) * 4 + i) * 1024 + lane * 16);
#pragma unroll
      for (int u = 0; u < 4; u++) {
        f32x16 nm = zero16();
        for (int i = 0; i < 4; i++) nm = MFMA(asf(kf[u][i]), asf(u < 2 ? ph[u & 1][i] : ph2[u & 1][i]), nm);
        float dl, dh; bcast32(nm[12] + nm[13], dl, dh);
        const float rd = __builtin_amdgcn_rcpf(dh + 1e-6f);
        for (int i = 0; i < 16; i++) nm[i] *= rd;
        u32x4 o1 = pack8u(nm, 1); o1[2] &= mlo; o1[3] &= mlo;
        const unsigned oo = WS_AFRAG + (rbg * 144 + 112 + (head0 + (u & 1) + 4 * (u >> 1)) * 2) * 1024 + lane * 16;
        st16(rs, oo, pack8u(nm, 0));
        st16(rs, oo + 1024, o1);
      }
      STAMP(4); TK(21); DUMPTK();
      if (bx == 7 * 4 * 8 && w == 0) {
#pragma unroll 1
        for (int i = 0; i < 8; i++) poll(ws, WS_AFLAG + (2 * i + hb) * 8 * 128 + l31 * 4, 0x500003ff500003ffull, tag);
        poll(ws, WS_CFLAG + lane * 128, ~0ull, tag);
        st4(rs, WS_EPOCH, tag);
      }
      return;
    }
  }

  {
    FRESH_LANE();
    const int tid = w * 64 + lane;
    const unsigned swz = (((lane & 7) ^ rmask(16 * w + rl)) << 4);
    STAMP(5);
    LDA2(2); LDA2(3); LDA2(4);
    const unsigned r0 = 32 * kq + 4 * hb + q4;
    unsigned bV[3][4];
#pragma unroll
    for (int i = 0; i < 12; i++) { bV[i >> 2][i & 3] = ((r0 * 128 + (((2 * g16 + (p4 >> 1)) ^ rmask(r0)) << 4) + (p4 & 1) * 8) ^ ((i & 1) * 64) ^ ((i & 2) * 16)) + (i >> 2) * 65536; OPAQUE(bV[i >> 2][i & 3]); }
    const unsigned voffO = (28 * (w >> 1) + 16 * (w & 1) + rl) * 896 + swz;
    const unsigned voffO2 = voffO + (((w & 1) && rl >= 4) ? 0 : 8 * 896) - 1024;
    const rsrc_t rsO = mkrs(wo + ct * 128, 448 * 896 - ct * 128);
    const size_t go = (size_t)(rt * 64 + (tid >> 3)) * 896 + ct * 128 + (tid & 7) * 16;
    u32x4 xr;
    f32x16 acc0 = zero16(), acc1 = zero16();
    s16x8 wv[2][4];
    auto vread = [&](int c) {
      const int sl = (c + 7) % 10; const unsigned so = (sl & 3) * SLOT;
      for (int i = 0; i < 4; i++) wv[c & 1][i] = trfrag(bV[sl >> 2][i] + so + (i >> 1) * 2048, bV[sl >> 2][i] + so + (i >> 1) * 2048 + 1024);
    };
    vread(0);
    TK(20);
    sfor<0, 18>([&](auto cc) {
      constexpr int c = decltype(cc)::value;
      asm volatile("s_waitcnt vmcnt(%3)" : "+v"(af[2 * c]), "+v"(af[2 * c + 1]), "+v"(fl) : "n"(P2::wait(c)) : "memory");
      if constexpr (c >= 10) {
        constexpr int c0 = c < 14 ? 10 : 14;
#pragma unroll 1
        for (int it = 0; it < SPIN_CAP; ++it) {
          if (__builtin_expect(fresh(umax4(umax4(0u, af[2 * c]), af[2 * c + 1])), 1)) break;
          WAITV(0);
          __builtin_amdgcn_s_sleep(4);
          LDA2(c); if (c + 1 < c0 + 4) LDA2(c0 + 3); if (c + 2 < c0 + 4) LDA2(c0 + 2); if (c + 3 < c0 + 4) LDA2(c0 + 1);
          asm volatile("s_waitcnt vmcnt(0)" : PIN8(c0)::"memory");
        }
        if (c == 10) { STAMP(7); TK(21); } if (c == 14) { STAMP(12); TK(22); }
      }
      if (P2::bar(c)) BAR();
      TK2(5 + c);
      if (c + 1 < 18) vread(c + 1);
      SB();
      acc0 = MFMA(wv[c & 1][0], asf(af[2 * c]), acc0); acc1 = MFMA(wv[c & 1][1], asf(af[2 * c]), acc1);
      if (c == P2::FL1 || c == P2::FL2) ALD4(fl, VFL, WS_AFLAG);
      constexpr int j = P2::vj(c);
      if (j >= 0 && j < 4) { ITEM_V(j + 10, 0); ITEM_V(j + 10, 1); }
      if (j >= 4) { DMA1I(voffO, rsO, ldsw, ((j + 7) % 10) * SLOT, (j - 4) * (112 * 896), st0); DMA2(voffO2, rsO, st0); }
      SB();
      acc0 = MFMA(wv[c & 1][2], asf(af[2 * c + 1]), acc0); acc1 = MFMA(wv[c & 1][3], asf(af[2 * c + 1]), acc1);
      if (c == P2::N1) { NEED(0xffc00ull); STAMP(6); }
      if (c == P2::N2) NEED(0xf0000000ull);
      if constexpr (P2::a0(c) < P2::a0(c + 1)) LDA2(P2::a0(c));
      if constexpr (P2::a0(c) + 1 < P2::a0(c + 1)) LDA2(P2::a0(c) + 1);
      if constexpr (P2::a0(c) + 2 < P2::a0(c + 1)) LDA2(P2::a0(c) + 2);
      if constexpr (P2::a0(c) + 3 < P2::a0(c + 1)) LDA2(P2::a0(c) + 3);
      static_assert(P2::a0(c + 1) - P2::a0(c) <= 4, "schedule");
      if (c == 13) asm volatile("global_load_dwordx4 %0, %1, off" : "=v"(xr) : "v"(x + go) : "memory");
      SB();
    });
    STAMP(8);
    BAR();
    const unsigned wa = (kq * 64 + rb2 * 32 + l31) * 272 + hb * 16;
#pragma unroll
    for (int gq = 0; gq < 4; gq++) {
      f32x4 v0 = {acc0[4 * gq], acc0[4 * gq + 1], acc0[4 * gq + 2], acc0[4 * gq + 3]};
      f32x4 v1 = {acc1[4 * gq], acc1[4 * gq + 1], acc1[4 * gq + 2], acc1[4 * gq + 3]};
      *(LAS f32x4*)(wa + gq * 32) = v0; *(LAS f32x4*)(wa + 128 + gq * 32) = v1;
    }
    BAR();
    const unsigned ra = (tid >> 3) * 272 + (tid & 7) * 32;
    f32x4 s0 = *(LAS f32x4*)ra, s1 = *(LAS f32x4*)(ra + 16);
#pragma unroll
    for (int k = 1; k < 4; k++) { s0 += *(LAS f32x4*)(ra + k * 64 * 272); s1 += *(LAS f32x4*)(ra + k * 64 * 272 + 16); }
    asm volatile("s_waitcnt vmcnt(0)" : "+v"(xr)::"memory");
    u32x4 o;
    o[0] = pk2(s0[0] + blo(xr[0]), s0[1] + bhi(xr[0]));
    o[1] = pk2(s0[2] + blo(xr[1]), s0[3] + bhi(xr[1]));
    o[2] = pk2(s1[0] + blo(xr[2]), s1[1] + bhi(xr[2]));
    o[3] = pk2(s1[2] + blo(xr[3]), s1[3] + bhi(xr[3]));
    *(u32x4*)(out + go) = o;
    STAMP(9); STAMPC(15); TK(23); TK2(23); DUMPTK();
  }
}

extern "C" void kernel_launch(void* const* d_in, const int* in_sizes, int n_in, void* d_out, int out_size, void* d_ws, size_t ws_size, hipStream_t stream) {
  (void)hipFuncSetAttribute((const void*)fused_kernel, hipFuncAttributeMaxDynamicSharedMemorySize, LDS_BYTES);
  fused_kernel<<<256, 512, LDS_BYTES, stream>>>((const char*)d_in[0], (const char*)d_in[1], (const char*)d_in[2], (const char*)d_in[3],
                                                (const char*)d_in[5], (const char*)d_in[6], (const char*)d_in[7], (char*)d_out, (char*)d_ws);
}
```

```cpp
#include <hip/hip_runtime.h>
#include <hip/hip_bf16.h>
#include <stdint.h>
#include <type_traits>
#pragma clang diagnostic ignored "-Wint-to-pointer-cast"
#pragma clang diagnostic ignored "-Warray-bounds"

#define DI __device__ __forceinline__
#define LAS __attribute__((address_space(3)))
#define GAS __attribute__((address_space(1)))
typedef __attribute__((ext_vector_type(8))) short s16x8;
typedef __attribute__((ext_vector_type(4))) short s16x4;
typedef __attribute__((ext_vector_type(16))) float f32x16;
typedef __attribute__((ext_vector_type(4))) float f32x4;
typedef __attribute__((ext_vector_type(2))) float f32x2;
typedef __attribute__((ext_vector_type(4))) unsigned u32x4;
typedef __attribute__((ext_vector_type(2))) unsigned u32x2;
typedef __attribute__((ext_vector_type(2))) __bf16 bf16x2;
typedef GAS unsigned gu32;
typedef __amdgpu_buffer_rsrc_t rsrc_t;
#define RLX_AGENT __ATOMIC_RELAXED, __HIP_MEMORY_SCOPE_AGENT

constexpr unsigned WS_EPOCH = 0;
constexpr unsigned WS_CFLAG = 1024;
constexpr unsigned WS_AFLAG = 16384;
constexpr unsigned WS_STAT = WS_AFLAG + 128 * 128;
constexpr unsigned WS_CS = WS_STAT + 2048 * 16;
constexpr unsigned WS_KVRED = 262144;
constexpr unsigned WS_KVPART = WS_KVRED + 131072;
constexpr unsigned WS_AFRAG = WS_KVPART + (2u << 20);
constexpr unsigned WS_STAMP = WS_AFRAG + 64u * 144u * 1024u;
constexpr unsigned WS_BYTES = WS_STAMP;
static_assert(WS_CFLAG + 96 * 128 <= WS_AFLAG && WS_CS + 4928 * 32 <= WS_KVRED, "ws map");

constexpr int LDS_BYTES = 163840;
constexpr unsigned SLOT = 16384;
constexpr unsigned LDS_MISC = 0, LDS_PHI = 2048;
constexpr unsigned CODE_LINES = 296;
constexpr int SPIN_CAP = 1 << 14;

#define HOTA(c) (c)
#define HOTV(c) (c)
#define MFMA(a, b, c) __builtin_amdgcn_mfma_f32_32x32x16_bf16((a), (b), (c), 0, 0, 0)
#define WAITV(n) asm volatile("s_waitcnt vmcnt(" #n ")" ::: "memory")
#define BAR() do { asm volatile("s_waitcnt lgkmcnt(0)" ::: "memory"); __builtin_amdgcn_s_barrier(); asm volatile("" ::: "memory"); } while (0)
#define SB() __builtin_amdgcn_sched_barrier(0)
#define OPAQUE(v) asm volatile("" : "+v"(v))
template <int N> DI void waitv() { asm volatile("s_waitcnt vmcnt(%0)" ::"n"(N) : "memory"); }
template <int N> using IC = std::integral_constant<int, N>;
template <int I, int N, class F> DI void sfor(F&& f) { if constexpr (I < N) { f(IC<I>{}); sfor<I + 1, N>(f); } }

#define STAMP(i)
#define STAMPC(i)
#define TK(i)
#define TK2(i)
#define DUMPTK()

#define DMA1I(voff, rsrc, ldsb, LIMM, SOFF, tmp) asm volatile("s_add_u32 m0, %3, %4\n\ts_mov_b32 %0, %5\n\tbuffer_load_dwordx4 %1, %2, %0 offen lds" \
    : "=&s"(tmp) : "v"(voff), "s"(rsrc), "s"(ldsb), "n"(LIMM), "n"(SOFF) : "memory", "scc")
#define DMA1A(voff, rsrc, ldsb, LIMM, soff, sinc) asm volatile("s_add_u32 m0, %3, %4\n\ts_add_u32 %0, %0, %5\n\tbuffer_load_dwordx4 %1, %2, %0 offen lds" \
    : "+s"(soff) : "v"(voff), "s"(rsrc), "s"(ldsb), "n"(LIMM), "s"(sinc) : "memory", "scc")
#define DMA2(voff, rsrc, soff) asm volatile("buffer_load_dwordx4 %0, %1, %2 offen offset:1024 lds" :: "v"(voff), "s"(rsrc), "s"(soff) : "memory")
#define ALD16(dst, voff, soff) asm volatile("buffer_load_dwordx4 %0, %1, %2, %3 offen sc1" : "=v"(dst) : "v"(voff), "s"(rs), "s"(soff) : "memory")
#define ALD4(dst, voff, soff) asm volatile("buffer_load_dword %0, %1, %2, %3 offen sc1" : "=v"(dst) : "v"(voff), "s"(rs), "s"(soff) : "memory")

#define FRESH_LANE() int lane; asm volatile("v_mbcnt_lo_u32_b32 %0, -1, 0\n\tv_mbcnt_hi_u32_b32 %0, -1, %0" : "=v"(lane)); \
  const int g16 = (lane >> 4) & 1, q4 = (lane & 15) >> 2, p4 = lane & 3, l31 = lane & 31, hb = lane >> 5, rl = lane >> 3; \
  (void)g16; (void)q4; (void)p4; (void)l31; (void)hb; (void)rl
DI rsrc_t mkrs(const void* p, unsigned bytes) { return __builtin_amdgcn_make_buffer_rsrc((void*)p, 0, bytes, 0x00020000); }
DI s16x8 trfrag(unsigned a0, unsigned a1) {
  s16x4 lo = __builtin_amdgcn_ds_read_tr16_b64_v4i16((LAS s16x4*)a0);
  s16x4 hi = __builtin_amdgcn_ds_read_tr16_b64_v4i16((LAS s16x4*)a1);
  return __builtin_shufflevector(lo, hi, 0, 1, 2, 3, 4, 5, 6, 7);
}
DI unsigned pk2(float a, float b) { f32x2 p = {a, b}; return __builtin_bit_cast(unsigned, __builtin_convertvector(p, bf16x2)); }
DI float dot2(unsigned a, unsigned b, float c) { return __builtin_amdgcn_fdot2_f32_bf16(__builtin_bit_cast(bf16x2, a), __builtin_bit_cast(bf16x2, b), c, false); }
DI float blo(unsigned u) { return __uint_as_float(u << 16); }
DI float bhi(unsigned u) { return __uint_as_float(u & 0xffff0000u); }
DI float fand(float v, unsigned m) { return __uint_as_float(__float_as_uint(v) & m); }
DI float vmul(float a, float b) { float r = a * b; OPAQUE(r); return r; }
DI float vadd(float a, float b) { float r = a + b; OPAQUE(r); return r; }
DI float max3(float a, float b, float c) { return __builtin_elementwise_maximum(__builtin_elementwise_maximum(a, b), c); }
DI float min3(float a, float b, float c) { return __builtin_elementwise_minimum(__builtin_elementwise_minimum(a, b), c); }
DI float add32(float a, float b) { auto r = __builtin_amdgcn_permlane32_swap(__float_as_uint(a), __float_as_uint(b), false, false); return __uint_as_float(r[0]) + __uint_as_float(r[1]); }
DI float add16(float a, float b) { auto r = __builtin_amdgcn_permlane16_swap(__float_as_uint(a), __float_as_uint(b), false, false); return __uint_as_float(r[0]) + __uint_as_float(r[1]); }
DI void bcast32(float v, float& lo, float& hi) { auto r = __builtin_amdgcn_permlane32_swap(__float_as_uint(v), __float_as_uint(v), false, false); lo = __uint_as_float(r[0]); hi = __uint_as_float(r[1]); }
template <int CTRL> DI float dpp(float x) { return __builtin_bit_cast(float, __builtin_amdgcn_mov_dpp(__builtin_bit_cast(int, x), CTRL, 0xf, 0xf, true)); }
DI float wave_sum(float v) {
  v += dpp<0xB1>(v); v += dpp<0x4E>(v); v += dpp<0x141>(v); v += dpp<0x128>(v);
  v = add16(v, v); return add32(v, v);
}
DI unsigned split2(float v) {
  const unsigned h = pk2(v, 0.f);
  return pk2(__uint_as_float(h << 16), v - __uint_as_float(h << 16));
}
DI u32x4 pack8u(const f32x16& x, int s) {
  u32x4 u = {pk2(x[8 * s], x[8 * s + 1]), pk2(x[8 * s + 2], x[8 * s + 3]), pk2(x[8 * s + 4], x[8 * s + 5]), pk2(x[8 * s + 6], x[8 * s + 7])};
  return u;
}
DI s16x8 asf(u32x4 u) { return __builtin_bit_cast(s16x8, u); }
DI unsigned rmask(unsigned row) { return (((row >> 1) & 1) << 2) | ((row >> 2) & 1) | ((row >> 3) & 2); }
DI f32x16 zero16() { f32x16 z; for (int i = 0; i < 16; i++) z[i] = 0.f; return z; }
DI void st16(rsrc_t rs, unsigned off, u32x4 v) { __builtin_amdgcn_raw_buffer_store_b128(v, rs, off, 0, 16); }
DI void st4(rsrc_t rs, unsigned off, unsigned v) { __builtin_amdgcn_raw_buffer_store_b32(v, rs, off, 0, 16); }
DI u32x4 ld16(rsrc_t rs, unsigned off) { return __builtin_amdgcn_raw_buffer_load_b128(rs, off, 0, 16); }
DI bool seen(unsigned v, unsigned tag, unsigned long long need) { return (__builtin_amdgcn_ballot_w64(v == tag) & need) == need; }
DI void poll(char* ws, unsigned off, unsigned long long need, unsigned tag) {
#pragma unroll 1
  for (int it = 0; it < SPIN_CAP; ++it) {
    const unsigned v = __hip_atomic_load((gu32*)(ws + off), RLX_AGENT);
    if (seen(v, tag, need)) break;
    __builtin_amdgcn_s_sleep(2);
  }
  __builtin_amdgcn_fence(__ATOMIC_ACQUIRE, "wavefront");
}
template <bool ST> DI void g_check(rsrc_t rs, u32x4& a, u32x4& b, u32x4& st, unsigned va, unsigned vb, unsigned vs, unsigned tag, unsigned mlo) {
#pragma unroll 1
  for (int it = 0; it < SPIN_CAP; ++it) {
    unsigned bad = (a[1] ^ tag) | (b[1] ^ tag) | (((a[3] ^ tag) | (b[3] ^ tag)) & mlo);
    if (ST) bad |= (st[1] ^ tag) | (st[3] ^ tag);
    if (__builtin_expect(__builtin_amdgcn_ballot_w64(bad != 0) == 0ull, 1)) break;
    ALD16(a, va, WS_CS); ALD16(b, vb, WS_CS); if (ST) ALD16(st, vs, WS_STAT);
    if (ST) asm volatile("s_waitcnt vmcnt(0)" : "+v"(a), "+v"(b), "+v"(st)::"memory"); else asm volatile("s_waitcnt vmcnt(0)" : "+v"(a), "+v"(b)::"memory");
  }
}

DI unsigned umax4(unsigned m, const u32x4& v) { return max(max(max(m, v[0]), v[1]), max(v[2], v[3])); }
DI bool fresh(unsigned m) { return __builtin_amdgcn_ballot_w64(m == 0xffffffffu) == 0ull; }

struct Sm { float L, cx, cn, mx, mn, s1, s2; f32x16 p, ep, en; };
DI void sm_minmax(Sm& s, float ninf) {
  const f32x16& p = s.p;
  float a = max3(p[0], p[1], p[2]), b = min3(p[0], p[1], p[2]);
  for (int i = 3; i < 11; i += 2) { a = max3(a, p[i], p[i + 1]); b = min3(b, p[i], p[i + 1]); }
  float c = max3(p[12], p[13], p[14]), d = min3(p[12], p[13], p[14]);
  c = max3(c, p[15], p[15]) + ninf; d = min3(d, p[15], p[15]) - ninf;
  s.mx = max3(a, p[11], c); s.mn = min3(b, p[11], d);
}
DI void sm_consts(Sm& s, float sc) {
  auto r = __builtin_amdgcn_permlane32_swap(__float_as_uint(s.mx), __float_as_uint(-s.mn), false, false);
  const float m = max3(__uint_as_float(r[0]), __uint_as_float(r[1]), __uint_as_float(r[1]));
  float mx, nmn; bcast32(m, mx, nmn);
  s.L = sc * 1.4426950408889634f; s.cx = -mx * s.L; s.cn = -nmn * s.L; s.s1 = 0.f; s.s2 = 0.f;
}
DI void sm_exp(Sm& s, int i, float ninf) {
  if (i == 12) { s.cx += ninf; s.cn += ninf; }
  s.ep[i] = __builtin_amdgcn_exp2f(fmaf(s.p[i], s.L, s.cx)); s.en[i] = __builtin_amdgcn_exp2f(fmaf(s.p[i], -s.L, s.cn));
  if (i > 0) { s.s1 = vadd(s.s1, s.ep[i - 1]); s.s2 = vadd(s.s2, s.en[i - 1]); }
}
DI void sm_sum(Sm& s, float os) {
  const float t = add32(vadd(s.s1, s.ep[15]), vadd(s.s2, s.en[15]));
  bcast32(os * __builtin_amdgcn_rcpf(t), s.s1, s.s2);
}
DI u32x4 sm_norm(const Sm& s, int f) {
  const f32x16& e = f < 2 ? s.ep : s.en; const float r = f < 2 ? s.s1 : s.s2; const int o = 8 * (f & 1);
  u32x4 u;
  for (int i = 0; i < 4; i++) u[i] = pk2(vmul(e[o + 2 * i], r), vmul(e[o + 2 * i + 1], r));
  return u;
}

struct P2 {
  static constexpr int a0(int s) { return s <= 2 ? 5 : s == 3 ? 7 : s + 5 < 18 ? s + 5 : 18; }
  static constexpr int vs(int j) { return j + 1; }
  static constexpr int vj(int s) { for (int j = 0; j < 8; j++) if (vs(j) == s) return j; return -1; }
  static constexpr int FL1 = 0, N1 = 2, FL2 = 6, N2 = 9;
  static constexpr bool bar(int c) { return c == 1 || c == 3 || c == 5 || c == 7 || c == 12; }
  static constexpr int vneed(int c) { return c == 1 ? 3 : c == 3 ? 9 : c == 7 ? 12 : c == 12 ? 17 : -1; }
  static constexpr int na(int s) { return 2 * (a0(s + 1) - a0(s)); }
  static constexpr int nops(int s) { return (vj(s) >= 0 ? 2 : 0) + na(s) + (s == 13 || s == FL1 || s == FL2 ? 1 : 0); }
  static constexpr int upto(int from, int c) { int n = 0; for (int s = from; s < c; s++) n += nops(s); return n; }
  static constexpr int wait(int c) {
    int n = 63;
    if (c >= 2 && c < 5) n = 2 * (4 - c) + upto(0, c);
    for (int s = 0; s < c; s++) if (a0(s) <= c && c < a0(s + 1)) n = 2 * (a0(s + 1) - 1 - c) + upto(s + 1, c);
    if (c == N1 || c == N2) { const int f = upto(c == N1 ? FL1 : FL2, c) - 1; if (f < n) n = f; }
    if (vneed(c) >= 10) { const int s = vs(vneed(c) - 10), v = na(s) + upto(s + 1, c); if (v < n) n = v; }
    else if (vneed(c) >= 0) { const int v = 2 * (9 - vneed(c)) + 6 + upto(0, c); if (v < n) n = v; }
    return n > 63 ? 63 : n;
  }
};

__global__ __launch_bounds__(512, 2) void fused_kernel(const char* __restrict__ x, const char* __restrict__ wqkv, const char* __restrict__ wphi,
                                                       const char* __restrict__ wo, const char* __restrict__ wg, const char* __restrict__ wu,
                                                       const char* __restrict__ wd, char* __restrict__ out, char* ws) {
  const int tid = threadIdx.x, lane = tid & 63, w = __builtin_amdgcn_readfirstlane(tid >> 6);
  const int bx = blockIdx.x, xcd = bx & 7, r = bx >> 3;
  const int th = xcd & 1, m = (xcd >> 1) * 4 + (r & 3), t8 = r >> 2;
  const int g16 = (lane >> 4) & 1, q4 = (lane & 15) >> 2, p4 = lane & 3, l31 = lane & 31, hb = lane >> 5, rl = lane >> 3;
  STAMP(0); STAMPC(14); TK(0);
  unsigned cpf;
  {
    unsigned long long pc; asm volatile("s_getpc_b64 %0" : "=s"(pc));
    const unsigned line = r * 8 + w + (lane & 1) * 256;
    asm volatile("global_load_dword %0, %1, %2" : "=v"(cpf) : "v"(min(line, CODE_LINES) * 128), "s"(pc & ~127ull) : "memory");
  }
  unsigned st0, st1;
  const unsigned ldsw = w * 2048;
  const unsigned swz = (((lane & 7) ^ rmask(16 * w + rl)) << 4);
  const unsigned voffX = (16 * w + rl) * 896 + swz, voffX2 = voffX + 8 * 896 - 1024;
  const rsrc_t rsX = mkrs(x + (size_t)m * 128 * 896, 128 * 896);
#define ITEM_X(c, h) do { if (!(h)) DMA1I(voffX, rsX, ldsw, (7 + ((c) + 2) % 3) * SLOT, (c) * 128, st0); else DMA2(voffX2, rsX, st0); } while (0)
#define ITEM_V(c, h) do { if (!(h)) DMA1I(voffX, rsV, ldsw, (((c) + 7) % 10) * SLOT, HOTV(c) * (128 * 896), st0); else DMA2(voffX2, rsV, st0); } while (0)
  ITEM_X(0, 0); ITEM_X(0, 1);

  const bool ffn = t8 < 5, isq = t8 == 7;
  const int fi = th * 5 + t8, g = th * 2 + (t8 - 5);
  const int rb = w & 3, role = w >> 2;
  unsigned voffW, voffW2, wstr, wnext;
  rsrc_t rsW;
  if (ffn) {
    voffW = (16 * (w & 3) + rl) * 3584 + ((((((lane & 7) >> 1) ^ ((rl >> 1) & 1) ^ (role << 1)) << 1) | (lane & 1)) << 4);
    voffW2 = voffW + 8 * 3584 - 1024;
    rsW = mkrs((role ? wu : wg) + fi * 128, 448 * 3584 - fi * 128);
    wstr = 64 * 3584; wnext = 10 * 128;
  } else {
    const int c0 = 128 * w + lane, c1 = c0 + 64;
    const int r0 = c0 / 28, k0 = c0 - 28 * r0, r1 = c1 / 28, k1 = c1 - 28 * r1;
    voffW = r0 * 2688 + k0 * 16 + (k0 >= 14 ? 32 * 2688 - 224 : 0);
    voffW2 = r1 * 2688 + k1 * 16 + (k1 >= 14 ? 32 * 2688 - 224 : 0) - 1024;
    const int co = isq ? 448 * th : 896 + 224 * g;
    rsW = mkrs(wqkv + co, w < 7 ? 448 * 2688 - co : 0);
    wstr = 64 * 2688; wnext = isq ? 224 : 896;
  }
  DMA1I(voffW, rsW, ldsw, 0, 0, st1); DMA2(voffW2, rsW, st1);

  const rsrc_t rs = mkrs(ws, WS_BYTES);
  unsigned tag;
  const int lc = bx % 77, cpart = bx / 77;
  const int rt = xcd * 4 + (r & 3), ct = t8;
  const rsrc_t rsV = mkrs(wd + ct * 128, isq ? 0 : 1792 * 896 - ct * 128);
  const unsigned mlo = lane < 32 ? 0xffffffffu : 0u;
  const unsigned vp = w * 1024 + lane * 16, lp = w * 1024;

  const int rbg = m * 4 + rb;
  const int rb2 = w & 1, kq = w >> 1;
#define VFL (((rt * 2 + rb2) * 2 + (kq & 1)) * 128 + l31 * 4)
#define AB (((rt * 2 + rb2) * 144 + 2 * kq) * 1024 + lane * 16)
  u32x4 af[36];
#define LDA2(c) asm volatile("s_mov_b32 %2, %5\n\tbuffer_load_dwordx4 %0, %3, %4, %2 offen sc1\n\tbuffer_load_dwordx4 %1, %3, %4, %2 offen offset:1024 sc1" \
    : "=&v"(af[2 * (c)]), "=&v"(af[2 * (c) + 1]), "=&s"(st1) : "v"(AB), "s"(rs), "n"(WS_AFRAG + HOTA(c) * 8192) : "memory")
#define PIN4(c) "+v"(af[2 * (c)]), "+v"(af[2 * (c) + 1]), "+v"(af[2 * (c) + 2]), "+v"(af[2 * (c) + 3])
#define PIN8(c) "+v"(af[2 * (c)]), "+v"(af[2 * (c) + 1]), "+v"(af[2 * (c) + 2]), "+v"(af[2 * (c) + 3]), "+v"(af[2 * (c) + 4]), "+v"(af[2 * (c) + 5]), "+v"(af[2 * (c) + 6]), "+v"(af[2 * (c) + 7])
#define NEED(mask) do { if (!seen(fl, tag, mask)) poll(ws, WS_AFLAG + VFL, mask, tag); } while (0)
  f32x16 pa0, pa1;
  float k0, k1, ia2;
  unsigned fl = 0;

  const unsigned xrow = 32 * rb + l31;
  const unsigned vst = (m * 128 + xrow) * 16;
  s16x8 xf[28], wq[3][2], fb, wf[2][2];
  u32x4 stt, ca, cb;
  unsigned aW[4];
  auto fold = [&](u32x4 c) { const unsigned c2 = c[2] & mlo; u32x4 u = {c[0], c2, c[0], c2}; return asf(u); };
  auto make_fb = [&]() { const unsigned a = stt[0] & 0xffffu, b = stt[0] >> 16; u32x4 u = {a | (a << 16), a | (a << 16), b | (b << 16), b | (b << 16)}; fb = asf(u); };

  auto wfrag = [&](auto att, int n, int t) {
    const int sl = (n >> 2) % 7;
    if constexpr (decltype(att)::value) {
      const unsigned o = (sl & 3) * SLOT + (n & 1) * 7168 + ((n >> 1) & 1) * 224 + t * 56, b = aW[sl >> 2];
      return trfrag(b + o, b + o + 1792);
    } else {
      const unsigned o = (sl & 3) * SLOT + (n & 3) * 2048, b = aW[(sl >> 2) * 2 + t];
      return trfrag(b + o, b + o + 512);
    }
  };
  auto pass0 = [&](auto att, unsigned vca, unsigned vcb) {
    constexpr bool A = decltype(att)::value;
    constexpr unsigned WS_ = A ? 64 * 2688 : 64 * 3584;
#define ITEM_W(c, h) do { if (!(h)) DMA1I(voffW, rsW, ldsw, (c) * SLOT, (c) * WS_, st1); else DMA2(voffW2, rsW, st1); } while (0)
#define ITEM_N(c, h) do { if (!(h)) { st1 = (c) * WS_; DMA1A(voffW, rsW, ldsw, (c) * SLOT, st1, wnext); } else DMA2(voffW2, rsW, st1); } while (0)
    u32x4 px, pw[3];
    {
      asm volatile("buffer_load_dword %0, off, %1, 0 sc1" : "=v"(tag) : "s"(rs) : "memory");
      asm volatile("global_load_dwordx4 %0, %1, %2" : "=v"(px) : "v"(min(lane, 55) * 16), "s"(x + (size_t)(bx * 8 + w) * 896) : "memory");
      const char* cb = lc < 21 ? wqkv + lc * 128 : (lc < 49 ? wg + (lc - 21) * 128 : wu + (lc - 49) * 128);
      const unsigned pitch = lc < 21 ? 2688 : 3584;
      const unsigned vo = rl * pitch + (lane & 7) * 16;
#pragma unroll
      for (int i = 0; i < 3; i++) {
        const int row = min(152 * cpart + 8 * (w + 8 * i), 440);
        asm volatile("global_load_dwordx4 %0, %1, %2" : "=v"(pw[i]) : "v"(vo), "s"(cb + (size_t)row * pitch) : "memory");
      }
    }
    ITEM_X(1, 0); ITEM_X(1, 1);
    ITEM_W(1, 0); ITEM_W(1, 1);
    TK(1);
    unsigned aX[4];
#pragma unroll
    for (int ks = 0; ks < 4; ks++) { aX[ks] = 7 * SLOT + xrow * 128 + (((2 * ks + hb) ^ rmask(xrow)) << 4); OPAQUE(aX[ks]); }
    auto xread = [&](int n) { xf[n] = *(LAS s16x8*)(aX[n & 3] + (((n >> 2) + 2) % 3) * SLOT); };
    f32x16 acc0 = zero16(), acc1 = zero16();
    const unsigned m56 = lane < 56 ? 0xffffffffu : 0u;
    float cs[8], s = 0.f, q = 0.f; unsigned pfd;
    asm volatile("s_waitcnt vmcnt(9)" :: "v"(cpf) : "memory");
    BAR();
    TK(4);
    xread(0); wq[0][1] = wfrag(att, 0, 1); wq[0][0] = wfrag(att, 0, 0);
    xread(1); wq[1][1] = wfrag(att, 1, 1); wq[1][0] = wfrag(att, 1, 0);
    ITEM_X(2, 0); ITEM_X(2, 1);
    auto pre = [&](int p) {
      if (p == 4) { tag += 1; for (int i = 0; i < 4; i++) px[i] &= m56; }
      if (p == 5) { for (int i = 0; i < 4; i++) { s = dot2(px[i], 0x3f803f80u, s); q = dot2(px[i], px[i], q); } OPAQUE(s); OPAQUE(q); }
      if (p == 6) { s = add32(s, q); s += dpp<0xB1>(s); OPAQUE(s); }
      if (p == 7) { s += dpp<0x4E>(s); s += dpp<0x141>(s); OPAQUE(s); }
      if (p == 8) { s += dpp<0x128>(s); s = add16(s, s); OPAQUE(s); }
      if (p == 9) { bcast32(s * (1.f / 448.f), s, q); q = fmaxf(fmaf(-s, s, q), 0.f) + 1e-5f; OPAQUE(s); OPAQUE(q); }
      if (p == 10) {
        u32x4 o = {split2(-s), tag, __float_as_uint(__builtin_amdgcn_rsqf(q)), tag};
        st16(rs, lane == 0 ? WS_STAT + (bx * 8 + w) * 16 : 0xfffffff0u, o);
      }
      if (p >= 12 && p < 20) {
        const int i = (p - 12) >> 1;
        if (!(p & 1)) { pw[2][i] &= (w + 16 < (cpart == 2 ? 18 : 19) ? 0xffffffffu : 0u); cs[2 * i] = vadd(vadd(blo(pw[0][i]), blo(pw[1][i])), blo(pw[2][i])); }
        else cs[2 * i + 1] = vadd(vadd(bhi(pw[0][i]), bhi(pw[1][i])), bhi(pw[2][i]));
      }
      if (p == 20 || p == 21) for (int i = 2 * (p - 20); i < 2 * (p - 19); i++) { cs[i] = add32(cs[i], cs[i + 4]); OPAQUE(cs[i]); }
      if (p == 22) for (int i = 0; i < 2; i++) { cs[i] = add16(cs[i], cs[i + 2]); OPAQUE(cs[i]); }
      if (p == 23) for (int i = 0; i < 2; i++) { cs[i] += dpp<0x128>(cs[i]); OPAQUE(cs[i]); }
      if (p == 24) {
        *(LAS f32x2*)(LDS_MISC + w * 256 + ((lane & 7) * 8 + hb * 4 + g16 * 2) * 4) = f32x2{cs[0], cs[1]};
        if (A) *(LAS u32x2*)(LDS_PHI + 4 * 1568 + lane * 8) = u32x2{0u, 0u};
      }
    };
    sfor<0, 28>([&](auto nn) {
      constexpr int n = decltype(nn)::value;
      constexpr int c = (n + 2) >> 2, j = (n + 2) & 3;
      if (j == 0) {
        if (c == 1) asm volatile("s_waitcnt vmcnt(6)" : "+v"(tag), "+v"(px), "+v"(pw[0]), "+v"(pw[1]), "+v"(pw[2])::"memory");
        if (c == 2 || c == 3) WAITV(7); if (c == 4) WAITV(6); if (c == 5 || c == 6) WAITV(9); if (c == 7) asm volatile("s_waitcnt vmcnt(4)" :: "v"(pfd) : "memory");
        BAR();
        TK(4 + c);
      }
      if (A && n == 15) {
        const unsigned pa = LDS_PHI + 2 * role * 1568 + (4 * hb + q4) * 56 + g16 * 32 + p4 * 8;
        for (int u = 0; u < 2; u++) { wf[u][0] = trfrag(pa + u * 1568, pa + u * 1568 + 448); wf[u][1] = trfrag(pa + u * 1568 + 896, pa + u * 1568 + 1344); }
      }
      if (n == 14 && w == 7 && cpart < 3) {
        float t = 0.f;
        for (int i = 0; i < 8; i++) t += *(LAS float*)(LDS_MISC + i * 256 + lane * 4);
        u32x2 o = {split2(t), tag};
        __builtin_amdgcn_raw_buffer_store_b64(o, rs, WS_CS + (lc * 64 + lane) * 32 + cpart * 8, 0, 16);
      }
#define P0_ISSUE(h) do { \
      if (n < 2) ITEM_W(2 + n, h); \
      if (c >= 1 && c <= 4 && j == 0) ITEM_X(c + 2, h); \
      if (c >= 1 && c <= 3 && j == 2) ITEM_W(c + 3, h); \
      if (c == 4 && j >= 2) ITEM_N(j - 1, h); \
      if (c == 5 && j == 0) ITEM_N(0, h); \
      if (c == 5 && j == 2) ITEM_N(3, h); \
      if (c == 6 && j == 0) ITEM_N(4, h); } while (0)
      SB();
      if (n == 27) pa0 = MFMA(wq[n % 3][0], xf[n], acc0); else acc0 = MFMA(wq[n % 3][0], xf[n], acc0);
      SB();
      if (n + 2 < 28) xread(n + 2);
      wq[(n + 2) % 3][1] = wfrag(att, n + 2, 1);
      pre(2 * n);
      P0_ISSUE(0);
      if (n == 5) {
        const rsrc_t rsP = mkrs(wphi + (isq ? 8 * th : 4 * g) * 1568, A ? 4 * 1568 : 0);
        DMA1I(vp, rsP, lp, A ? LDS_PHI : LDS_BYTES, 0, st0);
      }
      if (n == 15) {
        const int u = bx * 8 + w;
        const rsrc_t rsF = u < 1568 ? mkrs(wd + u * 1024, 1024) : mkrs(wo + (u - 1568) * 1024, u < 1960 ? 1024 : 0);
        asm volatile("buffer_load_dword %0, %1, %2, 0 offen" : "=v"(pfd) : "v"((lane & 7) * 128), "s"(rsF) : "memory");
      }
      if (n == 26) ALD16(ca, vca, WS_CS);
      if (n == 27) ALD16(stt, vst, WS_STAT);
      SB();
      if (n == 27) pa1 = MFMA(wq[n % 3][1], xf[n], acc1); else acc1 = MFMA(wq[n % 3][1], xf[n], acc1);
      SB();
      wq[(n + 2) % 3][0] = wfrag(att, n + 2, 0);
      pre(2 * n + 1);
      P0_ISSUE(1);
      if (n == 26) ALD16(cb, vcb, WS_CS);
      SB();
    });
    wq[0][0] = wq[1][0]; wq[0][1] = wq[1][1]; wq[1][0] = wq[2][0]; wq[1][1] = wq[2][1];
    STAMP(1); TK(12);
  };

  if (ffn) {
    FRESH_LANE();
    aW[0] = g16 * 8192 + (8 * hb + q4) * 128 + (((2 * role) ^ ((q4 >> 1) & 1) ^ (g16 << 1)) * 32) + p4 * 8;
    aW[1] = aW[0] ^ 32; aW[2] = aW[0] + 65536; aW[3] = aW[1] + 65536;
    OPAQUE(aW[0]); OPAQUE(aW[1]); OPAQUE(aW[2]); OPAQUE(aW[3]);
    const unsigned vcs = (1344 + g16 * 1792 + 32 * role + (lane & 15)) * 32 + hb * 16;
    pass0(std::false_type{}, vcs + fi * 2048, vcs + fi * 2048 + 512);
    const int hl2 = 20 + th * 4 + t8;
    const unsigned hb0 = WS_AFRAG + (rbg * 144 + fi * 4 + 2 * role) * 1024 + lane * 16;
    const unsigned pf = lane == 0 ? WS_AFLAG + (rbg * 2 + role) * 128 + fi * 4 : 0xffffff00u;
    const bool two = t8 == 4;
    auto passn = [&](auto first) {
      constexpr bool F = decltype(first)::value, L = !F;
      const rsrc_t rsB = two ? rsV : rsW;
      unsigned voffB = voffW, voffB2 = voffW2;
      if (F && two) { voffB = voffX; voffB2 = voffX2; OPAQUE(voffB); }
      const unsigned sinc = two ? 128 * 896 : 64 * 3584;
      const unsigned ho = F ? hb0 : hb0 + 10 * 4096, vcp = vcs + (F ? fi : 10 + fi) * 2048, vcn = vcs + (F ? 10 + fi : hl2) * 2048;
      unsigned sA = (F ? 10 : hl2 - fi) * 128 + 4 * 64 * 3584, sB = two ? 2 * 128 * 896 : (hl2 - fi) * 128 - 64 * 3584;
      const unsigned c2 = two ? 0xf0000000u : hb0 + (hl2 - fi) * 4096;
      f32x16 acc0 = zero16(), acc1 = zero16();
      f32x16 qa0, qa1;
      float gt[2], gu[2], hprev; u32x4 hq;
      auto geglu = [&](int hs) {
#pragma unroll
        for (int e = 0; e < 16; e++) {
          const int d = hs - ((F ? 12 : 4) + 12 * e / 5);
          const float G = e < 8 ? qa0[e & 7] : qa1[e & 7], U = e < 8 ? qa0[(e & 7) + 8] : qa1[(e & 7) + 8];
          if (d == 0) gt[e & 1] = vmul(G, fmaf(G * G, k1, k0));
          if (d == 1) { gt[e & 1] = __builtin_amdgcn_exp2f(gt[e & 1]); gu[e & 1] = vmul(G, U); }
          if (d == 2) gt[e & 1] = __builtin_amdgcn_rcpf(fmaf(gt[e & 1], ia2, ia2));
          if (d == 3) {
            if (e & 1) hq[(e >> 1) & 3] = pk2(hprev, gu[1] * gt[1]); else hprev = vmul(gu[0], gt[0]);
            if ((e & 7) == 7) st16(rs, ho + (e >> 3) * 1024, hq);
          }
        }
      };
      sfor<0, 28>([&](auto nn) {
        constexpr int n = decltype(nn)::value;
        if (n == 10) { if (!F) { WAITV(12); st4(rs, pf, tag); } BAR(); TK(13); }
        if (n == 18) { asm volatile("s_waitcnt vmcnt(8)" : "+v"(ca), "+v"(cb)::"memory"); BAR(); TK(14); }
        if (n == 26) { if (F) WAITV(7); BAR(); TK(15); }
        if (n == (F ? 4 : 0)) {
          if (F) asm volatile("s_waitcnt vmcnt(5)" : "+v"(ca), "+v"(cb), "+v"(stt)::"memory");
          g_check<F>(rs, ca, cb, stt, vcp, vcp + 512, vst, tag, mlo);
        }
        SB();
        if (n == 27) pa0 = MFMA(wq[n % 3][0], xf[n], acc0); else acc0 = MFMA(wq[n % 3][0], xf[n], acc0);
        SB();
        if (!L || n + 2 < 28) wq[(n + 2) % 3][1] = wfrag(std::false_type{}, n + 2, 1);
        if (F && n == 4) make_fb();
        if (n == (F ? 5 : 1)) qa0 = MFMA(fold(ca), fb, pa0);
        if (n == 0 || n == 2) DMA1A(voffW, rsW, ldsw, (5 + n / 2) * SLOT, sA, wstr);
        if (n == 1 || n == 3) DMA2(voffW2, rsW, sA);
        if (L && n >= 4 && n < 16) ITEM_V((n - 4) / 2, n & 1);
        if (L && (n == 18 || n == 19)) ITEM_V(6, n & 1);
        if (L && (n == 21 || n == 22)) ITEM_V(7, !(n & 1));
        if (L && n >= 26) ITEM_V(n - 18, 0);
        if (!L && (n == 10 || n == 12 || n == 14)) DMA1A(voffB, rsB, ldsw, ((n - 10) / 2) * SLOT, sB, sinc);
        if (!L && (n == 18 || n == 21)) DMA1A(voffB, rsB, ldsw, (3 + (n - 18) / 3) * SLOT, sB, sinc);
        if (!L && (n == 11 || n == 13 || n == 15 || n == 19 || n == 22)) DMA2(voffB2, rsB, sB);
        geglu(2 * n);
        SB();
        if (n == 27) pa1 = MFMA(wq[n % 3][1], xf[n], acc1); else acc1 = MFMA(wq[n % 3][1], xf[n], acc1);
        SB();
        if (!L || n + 2 < 28) wq[(n + 2) % 3][0] = wfrag(std::false_type{}, n + 2, 0);
        if (F && n == 4) {
          const float rstd = __uint_as_float(stt[2]);
          const float c0 = -2.f * 0.7978845608028654f * 1.4426950408889634f;
          k0 = vmul(c0, rstd); k1 = vmul(c0 * 0.044715f * rstd * rstd, rstd); ia2 = __builtin_amdgcn_rcpf(rstd * rstd);
        }
        if (n == (F ? 5 : 1)) qa1 = MFMA(fold(cb), fb, pa1);
        if (F && (n == 0 || n == 17)) st16(rs, c2 + (n & 1) * 1024, u32x4{~0u, ~0u, ~0u, ~0u});
        if (!F && n == 20) ALD4(fl, VFL, WS_AFLAG);
        if (n == 8) ALD16(ca, vcn, WS_CS);
        if (n == 9) ALD16(cb, vcn + 512, WS_CS);
        if (L && n >= 26) ITEM_V(n - 18, 1);
        geglu(2 * n + 1);
        SB();
      });
      if (!L) { wq[0][0] = wq[1][0]; wq[0][1] = wq[1][1]; wq[1][0] = wq[2][0]; wq[1][1] = wq[2][1]; }
      STAMP(F ? 2 : 3); TK(16);
    };
    passn(std::true_type{});
    if (two) { ITEM_V(0, 0); ITEM_V(0, 1); ITEM_V(1, 0); ITEM_V(1, 1); ITEM_V(2, 0); ITEM_V(2, 1); ITEM_V(8, 0); ITEM_V(8, 1); ITEM_V(9, 0); ITEM_V(9, 1); WAITV(10); st4(rs, pf, tag); fl = ~tag; }
    int t8o = t8; asm volatile("" : "+s"(t8o));
    if (t8o != 4) passn(std::false_type{});
    {
      const unsigned vcn = vcs + (two ? 10 + fi : hl2) * 2048, hs = hb0 + (two ? 10 : hl2 - fi) * 4096;
      TK2(0);
      g_check<false>(rs, ca, cb, stt, vcn, vcn + 512, vst, tag, mlo);
      pa0 = MFMA(fold(ca), fb, pa0); pa1 = MFMA(fold(cb), fb, pa1);
      asm volatile("s_waitcnt vmcnt(7)" : "+v"(fl)::"memory");
      const bool early = seen(fl, tag, 0x3ffull);
      u32x4 o[2];
#pragma unroll
      for (int t = 0; t < 2; t++) {
        const f32x16& a = t ? pa1 : pa0;
        float hv[8];
        if (t == 0) { LDA2(0); LDA2(1); } else { TK2(1); WAITV(9); TK2(2); st4(rs, two ? 0xffffff00u : pf + 40, tag); }
#pragma unroll
        for (int i = 0; i < 8; i++) {
          const float e = __builtin_amdgcn_exp2f(a[i] * fmaf(a[i] * a[i], k1, k0));
          hv[i] = (a[i] * a[i + 8]) * __builtin_amdgcn_rcpf(fmaf(e, ia2, ia2));
        }
        o[t] = u32x4{pk2(hv[0], hv[1]), pk2(hv[2], hv[3]), pk2(hv[4], hv[5]), pk2(hv[6], hv[7])};
        OPAQUE(o[t]); SB();
      }
      st16(rs, hs, o[0]); st16(rs, hs + 1024, o[1]);
      TK2(3);
      asm volatile("s_waitcnt vmcnt(3)" : PIN4(0)::"memory");
      TK2(4);
      if (!early) {
        WAITV(0); st4(rs, two ? pf + 40 : 0xffffff00u, tag);
        poll(ws, WS_AFLAG + VFL, 0x3ffull, tag); LDA2(0); LDA2(1);
        asm volatile("s_waitcnt vmcnt(0)" : PIN4(0)::"memory");
      }
      if (two) BAR();
    }
  } else {
    aW[0] = (8 * hb + q4) * 448 + 112 * role + g16 * 32 + p4 * 8; aW[1] = aW[0] + 65536;
    OPAQUE(aW[0]); OPAQUE(aW[1]);
    const int head0 = (isq ? 8 * th : 4 * g) + 2 * role;
    const unsigned vcs = ((isq ? 0 : 448) + 28 * head0 + l31) * 32 + hb * 16;
    pass0(std::true_type{}, vcs, vcs + 28 * 32);
    float rstd, os;
    const float ninf = __uint_as_float(~mlo & 0xff800000u);
    u32x4 ph[2][4];
    Sm sm;
    {
      unsigned sA = wnext + 4 * 64 * 2688;
      const unsigned vca = vcs + wnext * 16;
      const unsigned oc = isq ? WS_AFRAG + (rbg * 144 + 112 + head0 * 2) * 1024 + lane * 16 : 0xf0000000u;
      f32x16 acc0 = zero16(), acc1 = zero16();
      f32x16 qa0, qa1;
      u32x4 kf[2][2];
      f32x16 p1;
      sfor<0, 28>([&](auto nn) {
        constexpr int n = decltype(nn)::value;
        if (n == 10) { BAR(); TK(13); }
        if (n == 18) {
          asm volatile("s_waitcnt vmcnt(2)" : "+v"(ca), "+v"(cb)::"memory"); BAR(); TK(14);
          st4(rs, tid == 0 ? WS_CFLAG + ((isq ? 4 + th : g) * 16 + m) * 128 : 0xf0000000u, tag);
        }
        if (n == 26) { BAR(); TK(15); }
        if (n == 6 || n == 11) {
          FRESH_LANE();
          const unsigned co = isq ? (lane < 16 && n == 6 ? WS_KVRED + ((2 * th + (w >> 2)) * 16 + m) * 1024 + (w & 3) * 256 + lane * 16 : 0xf0000000u)
                                  : WS_KVPART + ((g * 8 + w) * 16 + m) * 2048 + (n & 1) * 1024 + lane * 16;
          st16(rs, co, u32x4{~0u, ~0u, ~0u, ~0u});
        }
        if (n == 4) {
          asm volatile("s_waitcnt vmcnt(4)" : "+v"(ca), "+v"(cb), "+v"(stt)::"memory");
          g_check<true>(rs, ca, cb, stt, vcs, vcs + 28 * 32, vst, tag, mlo);
        }
        if (n == 19) g_check<false>(rs, ca, cb, stt, vca, vca + 28 * 32, vst, tag, mlo);
        SB();
        if (n == 27) pa0 = MFMA(wq[n % 3][0], xf[n], acc0); else acc0 = MFMA(wq[n % 3][0], xf[n], acc0);
        SB();
        if (n + 2 < 28) wq[(n + 2) % 3][1] = wfrag(std::true_type{}, n + 2, 1);
        if (n == 4) { make_fb(); rstd = __uint_as_float(stt[2]); os = isq ? 1.f : rstd; }
        if (n == 5) qa0 = MFMA(fold(ca), fb, pa0);
        if (n == 7 || n == 8) kf[n - 7][0] = pack8u(n == 8 ? qa1 : qa0, 0);
        if (n == 8) sm.p = MFMA(wf[0][0], asf(kf[0][0]), zero16());
        if (n == 18) p1 = MFMA(wf[1][0], asf(kf[1][0]), zero16());
        if (n == 10) sm_minmax(sm, ninf);
        if (n == 11) sm_consts(sm, rstd);
        if (n >= 12 && n < 20) sm_exp(sm, 2 * (n - 12), ninf);
        if (n == 20) sm_sum(sm, os);
        if (n >= 21 && n < 25) ph[0][n - 21] = sm_norm(sm, n - 21);
        if (n == 25) sm_consts(sm, rstd);
        if (n >= 26) sm_exp(sm, 2 * (n - 26), ninf);
        if (n == 0 || n == 2) DMA1A(voffW, rsW, ldsw, (5 + n / 2) * SLOT, sA, wstr);
        if (n == 1 || n == 3) DMA2(voffW2, rsW, sA);
        if (n >= 4 && n < 10) ITEM_V((n - 4) / 2, n & 1);
        if (n == 10) {
          const rsrc_t rsP = mkrs(wphi + (8 * th + 4) * 1568, isq ? 4 * 1568 : 0);
          FRESH_LANE();
          DMA1I(lp + lane * 16, rsP, lp, 0, 0, st0);
        }
        if (n == 20) acc0 = MFMA(fold(ca), fb, acc0);
        SB();
        if (n == 27) pa1 = MFMA(wq[n % 3][1], xf[n], acc1); else acc1 = MFMA(wq[n % 3][1], xf[n], acc1);
        SB();
        if (n + 2 < 28) wq[(n + 2) % 3][0] = wfrag(std::true_type{}, n + 2, 0);
        if (n == 5) qa1 = MFMA(fold(cb), fb, pa1);
        if (n >= 5 && n % 3 == 2) { constexpr int i = (n - 5) / 3; __builtin_amdgcn_raw_buffer_store_b128(u32x4{~0u, ~0u, ~0u, ~0u}, rs, oc, (i & 3) * 1024 + (i >> 2) * 8192, 16); }
        if (n == 8) ALD16(ca, vca, WS_CS);
        if (n == 9) ALD16(cb, vca + 28 * 32, WS_CS);
        if (n == 7 || n == 8) { kf[n - 7][1] = pack8u(n == 8 ? qa1 : qa0, 1); kf[n - 7][1][2] &= mlo; kf[n - 7][1][3] &= mlo; }
        if (n == 8) sm.p = MFMA(wf[0][1], asf(kf[0][1]), sm.p);
        if (n == 18) p1 = MFMA(wf[1][1], asf(kf[1][1]), p1);
        if (n >= 12 && n < 20) sm_exp(sm, 2 * (n - 12) + 1, ninf);
        if (n == 20) { sm.p = p1; sm_minmax(sm, ninf); }
        if (n >= 26) sm_exp(sm, 2 * (n - 26) + 1, ninf);
        if (n == 20) acc1 = MFMA(fold(cb), fb, acc1);
        SB();
      });
      for (int i = 4; i < 16; i++) sm_exp(sm, i, ninf);
      sm_sum(sm, os);
    }
    STAMP(2); TK(16);
    FRESH_LANE();
    const unsigned xrow = 32 * rb + l31;
    if (!isq) {
      const unsigned ir = split2(__builtin_amdgcn_rcpf(rstd)) & ~mlo;
      const unsigned wa = (2 * role * 128 + xrow) * 200 + hb * 8;
#pragma unroll
      for (int u = 0; u < 2; u++) {
        if (u) for (int f = 0; f < 4; f++) ph[1][f] = sm_norm(sm, f);
#pragma unroll
        for (int f = 0; f < 6; f++) {
          u32x4 v = f < 4 ? ph[u][f] : pack8u(u ? pa1 : pa0, f - 4);
          if (f == 5) { v[2] = (v[2] & mlo) | ir; v[3] &= mlo; }
          *(LAS u32x2*)(wa + u * 25600 + f * 32) = u32x2{v[0], v[1]}; *(LAS u32x2*)(wa + u * 25600 + f * 32 + 16) = u32x2{v[2], v[3]};
        }
      }
      BAR();
      TK(17);
      unsigned ra = (w >> 1) * 25600 + (8 * hb + q4) * 200 + g16 * 32 + p4 * 8, rp = ra + (w & 1) * 64;
      OPAQUE(ra); OPAQUE(rp);
      f32x16 kv = zero16();
#pragma unroll
      for (int s = 0; s < 8; s++) kv = MFMA(trfrag(rp + s * 3200, rp + s * 3200 + 800), trfrag(ra + 128 + s * 3200, ra + 128 + s * 3200 + 800), kv);
      ALD4(fl, VFL, WS_AFLAG);
      st16(rs, WS_KVPART + ((g * 8 + w) * 16 + m) * 2048 + lane * 16, pack8u(kv, 0));
      st16(rs, WS_KVPART + ((g * 8 + w) * 16 + m) * 2048 + 1024 + lane * 16, pack8u(kv, 1));
      STAMP(3); TK(18);
      BAR();
      asm volatile("s_waitcnt vmcnt(2)" : "+v"(fl)::"memory");
      NEED(0x3ffull);
      LDA2(0); LDA2(1);
      sfor<3, 10>([&](auto cc) { ITEM_V(decltype(cc)::value, 0); ITEM_V(decltype(cc)::value, 1); });
      asm volatile("s_waitcnt vmcnt(14)" : PIN4(0)::"memory");
      TK(19);
    } else {
      u32x4 ph2[2][4];
      const unsigned vcf = (lane < 32 ? 2 * th * 16 + lane : (4 + th) * 16 + (lane & 15)) * 128;
      ALD4(fl, vcf, WS_CFLAG);
      for (int f = 0; f < 4; f++) ph[1][f] = sm_norm(sm, f);
      {
        const unsigned pa = 2 * role * 1568 + (4 * hb + q4) * 56 + g16 * 32 + p4 * 8;
#pragma unroll
        for (int u = 0; u < 2; u++) {
          u32x4 k1 = pack8u(u ? pa1 : pa0, 1); k1[2] &= mlo; k1[3] &= mlo;
          sm.p = MFMA(trfrag(pa + u * 1568, pa + u * 1568 + 448), asf(pack8u(u ? pa1 : pa0, 0)), zero16());
          sm.p = MFMA(trfrag(pa + u * 1568 + 896, pa + u * 1568 + 1344), asf(k1), sm.p);
          sm_minmax(sm, ninf); sm_consts(sm, rstd);
          for (int i = 0; i < 16; i++) sm_exp(sm, i, ninf);
          sm_sum(sm, 1.f);
          for (int f = 0; f < 4; f++) { ph2[u][f] = sm_norm(sm, f); OPAQUE(ph2[u][f]); }
        }
      }
      STAMP(3); TK(17);
      asm volatile("s_waitcnt vmcnt(0)" : "+v"(fl)::"memory");
      st4(rs, l31 == 0 ? WS_AFLAG + (rbg * 2 + hb) * 128 + (28 + th * 2 + role) * 4 : 0xf0000000u, tag);
      if (!seen(fl, tag, ~0ull)) poll(ws, WS_CFLAG + vcf, ~0ull, tag);
      {
        const int gg = 2 * th + (w >> 2);
        const unsigned pb = WS_KVPART + ((gg * 8 + (m >> 1)) * 16 + (lane >> 4)) * 2048 + (m & 1) * 1024 + ((w & 3) * 16 + (lane & 15)) * 16;
        u32x4 pv[4];
#pragma unroll 1
        for (int it = 0; it < SPIN_CAP; ++it) {
          unsigned mx = 0;
#pragma unroll
          for (int i = 0; i < 4; i++) pv[i] = ld16(rs, pb + i * 8192);
#pragma unroll
          for (int i = 0; i < 4; i++) mx = umax4(mx, pv[i]);
          if (fresh(mx)) break;
          __builtin_amdgcn_s_sleep(1);
        }
        STAMP(5); TK(18);
        float t[8];
#pragma unroll
        for (int j = 0; j < 4; j++) {
          t[2 * j] = (blo(pv[0][j]) + blo(pv[1][j])) + (blo(pv[2][j]) + blo(pv[3][j]));
          t[2 * j + 1] = (bhi(pv[0][j]) + bhi(pv[1][j])) + (bhi(pv[2][j]) + bhi(pv[3][j]));
        }
        for (int j = 0; j < 8; j++) { t[j] = add32(t[j], t[j]); t[j] = add16(t[j], t[j]); }
        st16(rs, lane < 16 ? WS_KVRED + (gg * 16 + m) * 1024 + ((w & 3) * 16 + lane) * 16 : 0xf0000000u, u32x4{pk2(t[0], t[1]), pk2(t[2], t[3]), pk2(t[4], t[5]), pk2(t[6], t[7])});
        STAMP(6); TK(19);
      }
      {
        u32x4 gv[4];
        __builtin_amdgcn_s_sleep(12);
#pragma unroll 1
        for (int it = 0; it < SPIN_CAP; ++it) {
          unsigned mx = 0;
#pragma unroll
          for (int i = 0; i < 4; i++) gv[i] = ld16(rs, WS_KVRED + ((8 * th + w) * 4 + i) * 1024 + lane * 16);
#pragma unroll
          for (int i = 0; i < 4; i++) mx = umax4(mx, gv[i]);
          if (fresh(mx)) break;
          __builtin_amdgcn_s_sleep(4);
        }
#pragma unroll
        for (int i = 0; i < 4; i++) *(LAS u32x4*)(SLOT + (w * 4 + i) * 1024 + lane * 16) = gv[i];
      }
      BAR();
      STAMP(7); TK(20);
      u32x4 kf[4][4];
#pragma unroll
      for (int u = 0; u < 4; u++)
#pragma unroll
        for (int i = 0; i < 4; i++) kf[u][i] = *(LAS u32x4*)(SLOT + ((2 * role + (u & 1) + 4 * (u >> 1)) * 4 + i) * 1024 + lane * 16);
#pragma unroll
      for (int u = 0; u < 4; u++) {
        f32x16 nm = zero16();
        for (int i = 0; i < 4; i++) nm = MFMA(asf(kf[u][i]), asf(u < 2 ? ph[u & 1][i] : ph2[u & 1][i]), nm);
        float dl, dh; bcast32(nm[12] + nm[13], dl, dh);
        const float rd = __builtin_amdgcn_rcpf(dh + 1e-6f);
        for (int i = 0; i < 16; i++) nm[i] *= rd;
        u32x4 o1 = pack8u(nm, 1); o1[2] &= mlo; o1[3] &= mlo;
        const unsigned oo = WS_AFRAG + (rbg * 144 + 112 + (head0 + (u & 1) + 4 * (u >> 1)) * 2) * 1024 + lane * 16;
        st16(rs, oo, pack8u(nm, 0));
        st16(rs, oo + 1024, o1);
      }
      STAMP(4); TK(21); DUMPTK();
      if (bx == 7 * 4 * 8 && w == 0) {
#pragma unroll 1
        for (int i = 0; i < 8; i++) poll(ws, WS_AFLAG + (2 * i + hb) * 8 * 128 + l31 * 4, 0x500003ff500003ffull, tag);
        poll(ws, WS_CFLAG + lane * 128, ~0ull, tag);
        st4(rs, WS_EPOCH, tag);
      }
      return;
    }
  }

  {
    FRESH_LANE();
    const int tid = w * 64 + lane;
    const unsigned swz = (((lane & 7) ^ rmask(16 * w + rl)) << 4);
    STAMP(5);
    LDA2(2); LDA2(3); LDA2(4);
    const unsigned r0 = 32 * kq + 4 * hb + q4;
    unsigned bV[3][4];
#pragma unroll
    for (int i = 0; i < 12; i++) { bV[i >> 2][i & 3] = ((r0 * 128 + (((2 * g16 + (p4 >> 1)) ^ rmask(r0)) << 4) + (p4 & 1) * 8) ^ ((i & 1) * 64) ^ ((i & 2) * 16)) + (i >> 2) * 65536; OPAQUE(bV[i >> 2][i & 3]); }
    const unsigned voffO = (28 * (w >> 1) + 16 * (w & 1) + rl) * 896 + swz;
    const unsigned voffO2 = voffO + (((w & 1) && rl >= 4) ? 0 : 8 * 896) - 1024;
    const rsrc_t rsO = mkrs(wo + ct * 128, 448 * 896 - ct * 128);
    const size_t go = (size_t)(rt * 64 + (tid >> 3)) * 896 + ct * 128 + (tid & 7) * 16;
    u32x4 xr;
    f32x16 acc0 = zero16(), acc1 = zero16();
    s16x8 wv[2][4];
    auto vread = [&](int c) {
      const int sl = (c + 7) % 10; const unsigned so = (sl & 3) * SLOT;
      for (int i = 0; i < 4; i++) wv[c & 1][i] = trfrag(bV[sl >> 2][i] + so + (i >> 1) * 2048, bV[sl >> 2][i] + so + (i >> 1) * 2048 + 1024);
    };
    vread(0);
    TK(20);
    sfor<0, 18>([&](auto cc) {
      constexpr int c = decltype(cc)::value;
      asm volatile("s_waitcnt vmcnt(%3)" : "+v"(af[2 * c]), "+v"(af[2 * c + 1]), "+v"(fl) : "n"(P2::wait(c)) : "memory");
      if constexpr (c >= 10) {
        constexpr int c0 = c < 14 ? 10 : 14;
#pragma unroll 1
        for (int it = 0; it < SPIN_CAP; ++it) {
          if (__builtin_expect(fresh(umax4(umax4(0u, af[2 * c]), af[2 * c + 1])), 1)) break;
          WAITV(0);
          __builtin_amdgcn_s_sleep(4);
          LDA2(c); if (c + 1 < c0 + 4) LDA2(c0 + 3); if (c + 2 < c0 + 4) LDA2(c0 + 2); if (c + 3 < c0 + 4) LDA2(c0 + 1);
          asm volatile("s_waitcnt vmcnt(0)" : PIN8(c0)::"memory");
        }
        if (c == 10) { STAMP(7); TK(21); } if (c == 14) { STAMP(12); TK(22); }
      }
      if (P2::bar(c)) BAR();
      TK2(5 + c);
      if (c + 1 < 18) vread(c + 1);
      SB();
      acc0 = MFMA(wv[c & 1][0], asf(af[2 * c]), acc0); acc1 = MFMA(wv[c & 1][1], asf(af[2 * c]), acc1);
      if (c == P2::FL1 || c == P2::FL2) ALD4(fl, VFL, WS_AFLAG);
      constexpr int j = P2::vj(c);
      if (j >= 0 && j < 4) { ITEM_V(j + 10, 0); ITEM_V(j + 10, 1); }
      if (j >= 4) { DMA1I(voffO, rsO, ldsw, ((j + 7) % 10) * SLOT, (j - 4) * (112 * 896), st0); DMA2(voffO2, rsO, st0); }
      SB();
      acc0 = MFMA(wv[c & 1][2], asf(af[2 * c + 1]), acc0); acc1 = MFMA(wv[c & 1][3], asf(af[2 * c + 1]), acc1);
      if (c == P2::N1) { NEED(0xffc00ull); STAMP(6); }
      if (c == P2::N2) NEED(0xf0000000ull);
      if constexpr (P2::a0(c) < P2::a0(c + 1)) LDA2(P2::a0(c));
      if constexpr (P2::a0(c) + 1 < P2::a0(c + 1)) LDA2(P2::a0(c) + 1);
      if constexpr (P2::a0(c) + 2 < P2::a0(c + 1)) LDA2(P2::a0(c) + 2);
      if constexpr (P2::a0(c) + 3 < P2::a0(c + 1)) LDA2(P2::a0(c) + 3);
      static_assert(P2::a0(c + 1) - P2::a0(c) <= 4, "schedule");
      if (c == 13) asm volatile("global_load_dwordx4 %0, %1, off" : "=v"(xr) : "v"(x + go) : "memory");
      SB();
    });
    STAMP(8);
    const unsigned wa = 5 * SLOT + (kq * 64 + rb2 * 32 + l31) * 272 + hb * 16;
#pragma unroll
    for (int gq = 0; gq < 4; gq++) {
      f32x4 v0 = {acc0[4 * gq], acc0[4 * gq + 1], acc0[4 * gq + 2], acc0[4 * gq + 3]};
      f32x4 v1 = {acc1[4 * gq], acc1[4 * gq + 1], acc1[4 * gq + 2], acc1[4 * gq + 3]};
      *(LAS f32x4*)(wa + gq * 32) = v0; *(LAS f32x4*)(wa + 128 + gq * 32) = v1;
    }
    BAR();
    const unsigned ra = 5 * SLOT + (tid >> 3) * 272 + (tid & 7) * 32;
    f32x4 s0 = *(LAS f32x4*)ra, s1 = *(LAS f32x4*)(ra + 16);
#pragma unroll
    for (int k = 1; k < 4; k++) { s0 += *(LAS f32x4*)(ra + k * 64 * 272); s1 += *(LAS f32x4*)(ra + k * 64 * 272 + 16); }
    asm volatile("s_waitcnt vmcnt(0)" : "+v"(xr)::"memory");
    u32x4 o;
    o[0] = pk2(s0[0] + blo(xr[0]), s0[1] + bhi(xr[0]));
    o[1] = pk2(s0[2] + blo(xr[1]), s0[3] + bhi(xr[1]));
    o[2] = pk2(s1[0] + blo(xr[2]), s1[1] + bhi(xr[2]));
    o[3] = pk2(s1[2] + blo(xr[3]), s1[3] + bhi(xr[3]));
    *(u32x4*)(out + go) = o;
    STAMP(9); STAMPC(15); TK(23); TK2(23); DUMPTK();
  }
}

extern "C" void kernel_launch(void* const* d_in, const int* in_sizes, int n_in, void* d_out, int out_size, void* d_ws, size_t ws_size, hipStream_t stream) {
  (void)hipFuncSetAttribute((const void*)fused_kernel, hipFuncAttributeMaxDynamicSharedMemorySize, LDS_BYTES);
  fused_kernel<<<256, 512, LDS_BYTES, stream>>>((const char*)d_in[0], (const char*)d_in[1], (const char*)d_in[2], (const char*)d_in[3],
                                                (const char*)d_in[5], (const char*)d_in[6], (const char*)d_in[7], (char*)d_out, (char*)d_ws);
}
```

```cpp
#include <hip/hip_runtime.h>
#include <hip/hip_bf16.h>
#include <stdint.h>
#include <type_traits>
#pragma clang diagnostic ignored "-Wint-to-pointer-cast"
#pragma clang diagnostic ignored "-Warray-bounds"

#define DI __device__ __forceinline__
#define LAS __attribute__((address_space(3)))
#define GAS __attribute__((address_space(1)))
typedef __attribute__((ext_vector_type(8))) short s16x8;
typedef __attribute__((ext_vector_type(4))) short s16x4;
typedef __attribute__((ext_vector_type(16))) float f32x16;
typedef __attribute__((ext_vector_type(4))) float f32x4;
typedef __attribute__((ext_vector_type(2))) float f32x2;
typedef __attribute__((ext_vector_type(4))) unsigned u32x4;
typedef __attribute__((ext_vector_type(2))) unsigned u32x2;
typedef __attribute__((ext_vector_type(2))) __bf16 bf16x2;
typedef GAS unsigned gu32;
typedef __amdgpu_buffer_rsrc_t rsrc_t;
#define RLX_AGENT __ATOMIC_RELAXED, __HIP_MEMORY_SCOPE_AGENT

constexpr unsigned WS_EPOCH = 0;
constexpr unsigned WS_CFLAG = 1024;
constexpr unsigned WS_AFLAG = 16384;
constexpr unsigned WS_STAT = WS_AFLAG + 128 * 128;
constexpr unsigned WS_CS = WS_STAT + 2048 * 16;
constexpr unsigned WS_KVRED = 262144;
constexpr unsigned WS_KVPART = WS_KVRED + 131072;
constexpr unsigned WS_AFRAG = WS_KVPART + (2u << 20);
constexpr unsigned WS_STAMP = WS_AFRAG + 64u * 144u * 1024u;
constexpr unsigned WS_BYTES = WS_STAMP;
static_assert(WS_CFLAG + 96 * 128 <= WS_AFLAG && WS_CS + 4928 * 32 <= WS_KVRED, "ws map");

constexpr int LDS_BYTES = 163840;
constexpr unsigned SLOT = 16384;
constexpr unsigned LDS_MISC = 0, LDS_PHI = 2048;
constexpr unsigned CODE_LINES = 296;
constexpr int SPIN_CAP = 1 << 14;

#define HOTA(c) (c)
#define HOTV(c) (c)
#define MFMA(a, b, c) __builtin_amdgcn_mfma_f32_32x32x16_bf16((a), (b), (c), 0, 0, 0)
#define WAITV(n) asm volatile("s_waitcnt vmcnt(" #n ")" ::: "memory")
#define BAR() do { asm volatile("s_waitcnt lgkmcnt(0)" ::: "memory"); __builtin_amdgcn_s_barrier(); asm volatile("" ::: "memory"); } while (0)
#define SB() __builtin_amdgcn_sched_barrier(0)
#define OPAQUE(v) asm volatile("" : "+v"(v))
template <int N> DI void waitv() { asm volatile("s_waitcnt vmcnt(%0)" ::"n"(N) : "memory"); }
template <int N> using IC = std::integral_constant<int, N>;
template <int I, int N, class F> DI void sfor(F&& f) { if constexpr (I < N) { f(IC<I>{}); sfor<I + 1, N>(f); } }

#define STAMP(i)
#define STAMPC(i)
#define TK(i)
#define TK2(i)
#define DUMPTK()

#define DMA1I(voff, rsrc, ldsb, LIMM, SOFF, tmp) asm volatile("s_add_u32 m0, %3, %4\n\ts_mov_b32 %0, %5\n\tbuffer_load_dwordx4 %1, %2, %0 offen lds" \
    : "=&s"(tmp) : "v"(voff), "s"(rsrc), "s"(ldsb), "n"(LIMM), "n"(SOFF) : "memory", "scc")
#define DMA1A(voff, rsrc, ldsb, LIMM, soff, sinc) asm volatile("s_add_u32 m0, %3, %4\n\ts_add_u32 %0, %0, %5\n\tbuffer_load_dwordx4 %1, %2, %0 offen lds" \
    : "+s"(soff) : "v"(voff), "s"(rsrc), "s"(ldsb), "n"(LIMM), "s"(sinc) : "memory", "scc")
#define DMA2(voff, rsrc, soff) asm volatile("buffer_load_dwordx4 %0, %1, %2 offen offset:1024 lds" :: "v"(voff), "s"(rsrc), "s"(soff) : "memory")
#define ALD16(dst, voff, soff) asm volatile("buffer_load_dwordx4 %0, %1, %2, %3 offen sc1" : "=v"(dst) : "v"(voff), "s"(rs), "s"(soff) : "memory")
#define ALD4(dst, voff, soff) asm volatile("buffer_load_dword %0, %1, %2, %3 offen sc1" : "=v"(dst) : "v"(voff), "s"(rs), "s"(soff) : "memory")

#define FRESH_LANE() int lane; asm volatile("v_mbcnt_lo_u32_b32 %0, -1, 0\n\tv_mbcnt_hi_u32_b32 %0, -1, %0" : "=v"(lane)); \
  const int g16 = (lane >> 4) & 1, q4 = (lane & 15) >> 2, p4 = lane & 3, l31 = lane & 31, hb = lane >> 5, rl = lane >> 3; \
  (void)g16; (void)q4; (void)p4; (void)l31; (void)hb; (void)rl
DI rsrc_t mkrs(const void* p, unsigned bytes) { return __builtin_amdgcn_make_buffer_rsrc((void*)p, 0, bytes, 0x00020000); }
DI s16x8 trfrag(unsigned a0, unsigned a1) {
  s16x4 lo = __builtin_amdgcn_ds_read_tr16_b64_v4i16((LAS s16x4*)a0);
  s16x4 hi = __builtin_amdgcn_ds_read_tr16_b64_v4i16((LAS s16x4*)a1);
  return __builtin_shufflevector(lo, hi, 0, 1, 2, 3, 4, 5, 6, 7);
}
DI unsigned pk2(float a, float b) { f32x2 p = {a, b}; return __builtin_bit_cast(unsigned, __builtin_convertvector(p, bf16x2)); }
DI float dot2(unsigned a, unsigned b, float c) { return __builtin_amdgcn_fdot2_f32_bf16(__builtin_bit_cast(bf16x2, a), __builtin_bit_cast(bf16x2, b), c, false); }
DI float blo(unsigned u) { return __uint_as_float(u << 16); }
DI float bhi(unsigned u) { return __uint_as_float(u & 0xffff0000u); }
DI float fand(float v, unsigned m) { return __uint_as_float(__float_as_uint(v) & m); }
DI float vmul(float a, float b) { float r = a * b; OPAQUE(r); return r; }
DI float vadd(float a, float b) { float r = a + b; OPAQUE(r); return r; }
DI float max3(float a, float b, float c) { return __builtin_elementwise_maximum(__builtin_elementwise_maximum(a, b), c); }
DI float min3(float a, float b, float c) { return __builtin_elementwise_minimum(__builtin_elementwise_minimum(a, b), c); }
DI float add32(float a, float b) { auto r = __builtin_amdgcn_permlane32_swap(__float_as_uint(a), __float_as_uint(b), false, false); return __uint_as_float(r[0]) + __uint_as_float(r[1]); }
DI float add16(float a, float b) { auto r = __builtin_amdgcn_permlane16_swap(__float_as_uint(a), __float_as_uint(b), false, false); return __uint_as_float(r[0]) + __uint_as_float(r[1]); }
DI void bcast32(float v, float& lo, float& hi) { auto r = __builtin_amdgcn_permlane32_swap(__float_as_uint(v), __float_as_uint(v), false, false); lo = __uint_as_float(r[0]); hi = __uint_as_float(r[1]); }
template <int CTRL> DI float dpp(float x) { return __builtin_bit_cast(float, __builtin_amdgcn_mov_dpp(__builtin_bit_cast(int, x), CTRL, 0xf, 0xf, true)); }
DI float wave_sum(float v) {
  v += dpp<0xB1>(v); v += dpp<0x4E>(v); v += dpp<0x141>(v); v += dpp<0x128>(v);
  v = add16(v, v); return add32(v, v);
}
DI unsigned split2(float v) {
  const unsigned h = pk2(v, 0.f);
  return pk2(__uint_as_float(h << 16), v - __uint_as_float(h << 16));
}
DI u32x4 pack8u(const f32x16& x, int s) {
  u32x4 u = {pk2(x[8 * s], x[8 * s + 1]), pk2(x[8 * s + 2], x[8 * s + 3]), pk2(x[8 * s + 4], x[8 * s + 5]), pk2(x[8 * s + 6], x[8 * s + 7])};
  return u;
}
DI s16x8 asf(u32x4 u) { return __builtin_bit_cast(s16x8, u); }
DI unsigned rmask(unsigned row) { return (((row >> 1) & 1) << 2) | ((row >> 2) & 1) | ((row >> 3) & 2); }
DI f32x16 zero16() { f32x16 z; for (int i = 0; i < 16; i++) z[i] = 0.f; return z; }
DI void st16(rsrc_t rs, unsigned off, u32x4 v) { __builtin_amdgcn_raw_buffer_store_b128(v, rs, off, 0, 16); }
DI void st4(rsrc_t rs, unsigned off, unsigned v) { __builtin_amdgcn_raw_buffer_store_b32(v, rs, off, 0, 16); }
DI u32x4 ld16(rsrc_t rs, unsigned off) { return __builtin_amdgcn_raw_buffer_load_b128(rs, off, 0, 16); }
DI bool seen(unsigned v, unsigned tag, unsigned long long need) { return (__builtin_amdgcn_ballot_w64(v == tag) & need) == need; }
DI void poll(char* ws, unsigned off, unsigned long long need, unsigned tag) {
#pragma unroll 1
  for (int it = 0; it < SPIN_CAP; ++it) {
    const unsigned v = __hip_atomic_load((gu32*)(ws + off), RLX_AGENT);
    if (seen(v, tag, need)) break;
    __builtin_amdgcn_s_sleep(2);
  }
  __builtin_amdgcn_fence(__ATOMIC_ACQUIRE, "wavefront");
}
template <bool ST> DI void g_check(rsrc_t rs, u32x4& a, u32x4& b, u32x4& st, unsigned va, unsigned vb, unsigned vs, unsigned tag, unsigned mlo) {
#pragma unroll 1
  for (int it = 0; it < SPIN_CAP; ++it) {
    unsigned bad = (a[1] ^ tag) | (b[1] ^ tag) | (((a[3] ^ tag) | (b[3] ^ tag)) & mlo);
    if (ST) bad |= (st[1] ^ tag) | (st[3] ^ tag);
    if (__builtin_expect(__builtin_amdgcn_ballot_w64(bad != 0) == 0ull, 1)) break;
    ALD16(a, va, WS_CS); ALD16(b, vb, WS_CS); if (ST) ALD16(st, vs, WS_STAT);
    if (ST) asm volatile("s_waitcnt vmcnt(0)" : "+v"(a), "+v"(b), "+v"(st)::"memory"); else asm volatile("s_waitcnt vmcnt(0)" : "+v"(a), "+v"(b)::"memory");
  }
}

DI unsigned umax4(unsigned m, const u32x4& v) { return max(max(max(m, v[0]), v[1]), max(v[2], v[3])); }
DI bool fresh(unsigned m) { return __builtin_amdgcn_ballot_w64(m == 0xffffffffu) == 0ull; }

struct Sm { float L, cx, cn, mx, mn, s1, s2; f32x16 p, ep, en; };
DI void sm_minmax(Sm& s, float ninf) {
  const f32x16& p = s.p;
  float a = max3(p[0], p[1], p[2]), b = min3(p[0], p[1], p[2]);
  for (int i = 3; i < 11; i += 2) { a = max3(a, p[i], p[i + 1]); b = min3(b, p[i], p[i + 1]); }
  float c = max3(p[12], p[13], p[14]), d = min3(p[12], p[13], p[14]);
  c = max3(c, p[15], p[15]) + ninf; d = min3(d, p[15], p[15]) - ninf;
  s.mx = max3(a, p[11], c); s.mn = min3(b, p[11], d);
}
DI void sm_consts(Sm& s, float sc) {
  auto r = __builtin_amdgcn_permlane32_swap(__float_as_uint(s.mx), __float_as_uint(-s.mn), false, false);
  const float m = max3(__uint_as_float(r[0]), __uint_as_float(r[1]), __uint_as_float(r[1]));
  float mx, nmn; bcast32(m, mx, nmn);
  s.L = sc * 1.4426950408889634f; s.cx = -mx * s.L; s.cn = -nmn * s.L; s.s1 = 0.f; s.s2 = 0.f;
}
DI void sm_exp(Sm& s, int i, float ninf) {
  if (i == 12) { s.cx += ninf; s.cn += ninf; }
  s.ep[i] = __builtin_amdgcn_exp2f(fmaf(s.p[i], s.L, s.cx)); s.en[i] = __builtin_amdgcn_exp2f(fmaf(s.p[i], -s.L, s.cn));
  if (i > 0) { s.s1 = vadd(s.s1, s.ep[i - 1]); s.s2 = vadd(s.s2, s.en[i - 1]); }
}
DI void sm_sum(Sm& s, float os) {
  const float t = add32(vadd(s.s1, s.ep[15]), vadd(s.s2, s.en[15]));
  bcast32(os * __builtin_amdgcn_rcpf(t), s.s1, s.s2);
}
DI u32x4 sm_norm(const Sm& s, int f) {
  const f32x16& e = f < 2 ? s.ep : s.en; const float r = f < 2 ? s.s1 : s.s2; const int o = 8 * (f & 1);
  u32x4 u;
  for (int i = 0; i < 4; i++) u[i] = pk2(vmul(e[o + 2 * i], r), vmul(e[o + 2 * i + 1], r));
  return u;
}

struct P2 {
  static constexpr int a0(int s) { return s <= 2 ? 5 : s == 3 ? 7 : s + 5 < 18 ? s + 5 : 18; }
  static constexpr int vs(int j) { return j + 1; }
  static constexpr int vj(int s) { for (int j = 0; j < 8; j++) if (vs(j) == s) return j; return -1; }
  static constexpr int FL1 = 0, N1 = 2, FL2 = 6, N2 = 9;
  static constexpr bool bar(int c) { return c == 1 || c == 3 || c == 5 || c == 7 || c == 12; }
  static constexpr int vneed(int c) { return c == 1 ? 3 : c == 3 ? 9 : c == 7 ? 12 : c == 12 ? 17 : -1; }
  static constexpr int na(int s) { return 2 * (a0(s + 1) - a0(s)); }
  static constexpr int nops(int s) { return (vj(s) >= 0 ? 2 : 0) + na(s) + (s == 13 || s == FL1 || s == FL2 ? 1 : 0); }
  static constexpr int upto(int from, int c) { int n = 0; for (int s = from; s < c; s++) n += nops(s); return n; }
  static constexpr int wait(int c) {
    int n = 63;
    if (c >= 2 && c < 5) n = 2 * (4 - c) + upto(0, c);
    for (int s = 0; s < c; s++) if (a0(s) <= c && c < a0(s + 1)) n = 2 * (a0(s + 1) - 1 - c) + upto(s + 1, c);
    if (c == N1 || c == N2) { const int f = upto(c == N1 ? FL1 : FL2, c) - 1; if (f < n) n = f; }
    if (vneed(c) >= 10) { const int s = vs(vneed(c) - 10), v = na(s) + upto(s + 1, c); if (v < n) n = v; }
    else if (vneed(c) >= 0) { const int v = 2 * (9 - vneed(c)) + 6 + upto(0, c); if (v < n) n = v; }
    return n > 63 ? 63 : n;
  }
};

__global__ __launch_bounds__(512, 2) void fused_kernel(const char* __restrict__ x, const char* __restrict__ wqkv, const char* __restrict__ wphi,
                                                       const char* __restrict__ wo, const char* __restrict__ wg, const char* __restrict__ wu,
                                                       const char* __restrict__ wd, char* __restrict__ out, char* ws) {
  const int tid = threadIdx.x, lane = tid & 63, w = __builtin_amdgcn_readfirstlane(tid >> 6);
  const int bx = blockIdx.x, xcd = bx & 7, r = bx >> 3;
  const int th = xcd & 1, m = (xcd >> 1) * 4 + (r & 3), t8 = r >> 2;
  const int g16 = (lane >> 4) & 1, q4 = (lane & 15) >> 2, p4 = lane & 3, l31 = lane & 31, hb = lane >> 5, rl = lane >> 3;
  STAMP(0); STAMPC(14); TK(0);
  unsigned cpf;
  {
    unsigned long long pc; asm volatile("s_getpc_b64 %0" : "=s"(pc));
    const unsigned line = r * 8 + w + (lane & 1) * 256;
    asm volatile("global_load_dword %0, %1, %2" : "=v"(cpf) : "v"(min(line, CODE_LINES) * 128), "s"(pc & ~127ull) : "memory");
  }
  unsigned st0, st1;
  const unsigned ldsw = w * 2048;
  const unsigned swz = (((lane & 7) ^ rmask(16 * w + rl)) << 4);
  const unsigned voffX = (16 * w + rl) * 896 + swz, voffX2 = voffX + 8 * 896 - 1024;
  const rsrc_t rsX = mkrs(x + (size_t)m * 128 * 896, 128 * 896);
#define ITEM_X(c, h) do { if (!(h)) DMA1I(voffX, rsX, ldsw, (7 + ((c) + 2) % 3) * SLOT, (c) * 128, st0); else DMA2(voffX2, rsX, st0); } while (0)
#define ITEM_V(c, h) do { if (!(h)) DMA1I(voffX, rsV, ldsw, (((c) + 7) % 10) * SLOT, HOTV(c) * (128 * 896), st0); else DMA2(voffX2, rsV, st0); } while (0)
  ITEM_X(0, 0); ITEM_X(0, 1);

  const bool ffn = t8 < 5, isq = t8 == 7;
  const int fi = th * 5 + t8, g = th * 2 + (t8 - 5);
  const int rb = w & 3, role = w >> 2;
  unsigned voffW, voffW2, wstr, wnext;
  rsrc_t rsW;
  if (ffn) {
    voffW = (16 * (w & 3) + rl) * 3584 + ((((((lane & 7) >> 1) ^ ((rl >> 1) & 1) ^ (role << 1)) << 1) | (lane & 1)) << 4);
    voffW2 = voffW + 8 * 3584 - 1024;
    rsW = mkrs((role ? wu : wg) + fi * 128, 448 * 3584 - fi * 128);
    wstr = 64 * 3584; wnext = 10 * 128;
  } else {
    const int c0 = 128 * w + lane, c1 = c0 + 64;
    const int r0 = c0 / 28, k0 = c0 - 28 * r0, r1 = c1 / 28, k1 = c1 - 28 * r1;
    voffW = r0 * 2688 + k0 * 16 + (k0 >= 14 ? 32 * 2688 - 224 : 0);
    voffW2 = r1 * 2688 + k1 * 16 + (k1 >= 14 ? 32 * 2688 - 224 : 0) - 1024;
    const int co = isq ? 448 * th : 896 + 224 * g;
    rsW = mkrs(wqkv + co, w < 7 ? 448 * 2688 - co : 0);
    wstr = 64 * 2688; wnext = isq ? 224 : 896;
  }
  DMA1I(voffW, rsW, ldsw, 0, 0, st1); DMA2(voffW2, rsW, st1);

  const rsrc_t rs = mkrs(ws, WS_BYTES);
  unsigned tpf;
  asm volatile("global_load_dword %0, %1, off" : "=v"(tpf) : "v"((lane & 1) ? ws + (size_t)(r * 8 + w) * (WS_BYTES / 256 & ~127u) : out + (size_t)(r * 8 + w) * 7168) : "memory");
  unsigned tag;
  const int lc = bx % 77, cpart = bx / 77;
  const int rt = xcd * 4 + (r & 3), ct = t8;
  const rsrc_t rsV = mkrs(wd + ct * 128, isq ? 0 : 1792 * 896 - ct * 128);
  const unsigned mlo = lane < 32 ? 0xffffffffu : 0u;
  const unsigned vp = w * 1024 + lane * 16, lp = w * 1024;

  const int rbg = m * 4 + rb;
  const int rb2 = w & 1, kq = w >> 1;
#define VFL (((rt * 2 + rb2) * 2 + (kq & 1)) * 128 + l31 * 4)
#define AB (((rt * 2 + rb2) * 144 + 2 * kq) * 1024 + lane * 16)
  u32x4 af[36];
#define LDA2(c) asm volatile("s_mov_b32 %2, %5\n\tbuffer_load_dwordx4 %0, %3, %4, %2 offen sc1\n\tbuffer_load_dwordx4 %1, %3, %4, %2 offen offset:1024 sc1" \
    : "=&v"(af[2 * (c)]), "=&v"(af[2 * (c) + 1]), "=&s"(st1) : "v"(AB), "s"(rs), "n"(WS_AFRAG + HOTA(c) * 8192) : "memory")
#define PIN4(c) "+v"(af[2 * (c)]), "+v"(af[2 * (c) + 1]), "+v"(af[2 * (c) + 2]), "+v"(af[2 * (c) + 3])
#define PIN8(c) "+v"(af[2 * (c)]), "+v"(af[2 * (c) + 1]), "+v"(af[2 * (c) + 2]), "+v"(af[2 * (c) + 3]), "+v"(af[2 * (c) + 4]), "+v"(af[2 * (c) + 5]), "+v"(af[2 * (c) + 6]), "+v"(af[2 * (c) + 7])
#define NEED(mask) do { if (!seen(fl, tag, mask)) poll(ws, WS_AFLAG + VFL, mask, tag); } while (0)
  f32x16 pa0, pa1;
  float k0, k1, ia2;
  unsigned fl = 0;

  const unsigned xrow = 32 * rb + l31;
  const unsigned vst = (m * 128 + xrow) * 16;
  s16x8 xf[28], wq[3][2], fb, wf[2][2];
  u32x4 stt, ca, cb;
  unsigned aW[4];
  auto fold = [&](u32x4 c) { const unsigned c2 = c[2] & mlo; u32x4 u = {c[0], c2, c[0], c2}; return asf(u); };
  auto make_fb = [&]() { const unsigned a = stt[0] & 0xffffu, b = stt[0] >> 16; u32x4 u = {a | (a << 16), a | (a << 16), b | (b << 16), b | (b << 16)}; fb = asf(u); };

  auto wfrag = [&](auto att, int n, int t) {
    const int sl = (n >> 2) % 7;
    if constexpr (decltype(att)::value) {
      const unsigned o = (sl & 3) * SLOT + (n & 1) * 7168 + ((n >> 1) & 1) * 224 + t * 56, b = aW[sl >> 2];
      return trfrag(b + o, b + o + 1792);
    } else {
      const unsigned o = (sl & 3) * SLOT + (n & 3) * 2048, b = aW[(sl >> 2) * 2 + t];
      return trfrag(b + o, b + o + 512);
    }
  };
  auto pass0 = [&](auto att, unsigned vca, unsigned vcb) {
    constexpr bool A = decltype(att)::value;
    constexpr unsigned WS_ = A ? 64 * 2688 : 64 * 3584;
#define ITEM_W(c, h) do { if (!(h)) DMA1I(voffW, rsW, ldsw, (c) * SLOT, (c) * WS_, st1); else DMA2(voffW2, rsW, st1); } while (0)
#define ITEM_N(c, h) do { if (!(h)) { st1 = (c) * WS_; DMA1A(voffW, rsW, ldsw, (c) * SLOT, st1, wnext); } else DMA2(voffW2, rsW, st1); } while (0)
    u32x4 px, pw[3];
    {
      asm volatile("buffer_load_dword %0, off, %1, 0 sc1" : "=v"(tag) : "s"(rs) : "memory");
      asm volatile("global_load_dwordx4 %0, %1, %2" : "=v"(px) : "v"(min(lane, 55) * 16), "s"(x + (size_t)(bx * 8 + w) * 896) : "memory");
      const char* cb = lc < 21 ? wqkv + lc * 128 : (lc < 49 ? wg + (lc - 21) * 128 : wu + (lc - 49) * 128);
      const unsigned pitch = lc < 21 ? 2688 : 3584;
      const unsigned vo = rl * pitch + (lane & 7) * 16;
#pragma unroll
      for (int i = 0; i < 3; i++) {
        const int row = min(152 * cpart + 8 * (w + 8 * i), 440);
        asm volatile("global_load_dwordx4 %0, %1, %2" : "=v"(pw[i]) : "v"(vo), "s"(cb + (size_t)row * pitch) : "memory");
      }
    }
    ITEM_X(1, 0); ITEM_X(1, 1);
    ITEM_W(1, 0); ITEM_W(1, 1);
    TK(1);
    unsigned aX[4];
#pragma unroll
    for (int ks = 0; ks < 4; ks++) { aX[ks] = 7 * SLOT + xrow * 128 + (((2 * ks + hb) ^ rmask(xrow)) << 4); OPAQUE(aX[ks]); }
    auto xread = [&](int n) { xf[n] = *(LAS s16x8*)(aX[n & 3] + (((n >> 2) + 2) % 3) * SLOT); };
    f32x16 acc0 = zero16(), acc1 = zero16();
    const unsigned m56 = lane < 56 ? 0xffffffffu : 0u;
    float cs[8], s = 0.f, q = 0.f; unsigned pfd;
    asm volatile("s_waitcnt vmcnt(10)" :: "v"(cpf) : "memory");
    BAR();
    TK(4);
    xread(0); wq[0][1] = wfrag(att, 0, 1); wq[0][0] = wfrag(att, 0, 0);
    xread(1); wq[1][1] = wfrag(att, 1, 1); wq[1][0] = wfrag(att, 1, 0);
    ITEM_X(2, 0); ITEM_X(2, 1);
    auto pre = [&](int p) {
      if (p == 4) { tag += 1; for (int i = 0; i < 4; i++) px[i] &= m56; }
      if (p == 5) { for (int i = 0; i < 4; i++) { s = dot2(px[i], 0x3f803f80u, s); q = dot2(px[i], px[i], q); } OPAQUE(s); OPAQUE(q); }
      if (p == 6) { s = add32(s, q); s += dpp<0xB1>(s); OPAQUE(s); }
      if (p == 7) { s += dpp<0x4E>(s); s += dpp<0x141>(s); OPAQUE(s); }
      if (p == 8) { s += dpp<0x128>(s); s = add16(s, s); OPAQUE(s); }
      if (p == 9) { bcast32(s * (1.f / 448.f), s, q); q = fmaxf(fmaf(-s, s, q), 0.f) + 1e-5f; OPAQUE(s); OPAQUE(q); }
      if (p == 10) {
        u32x4 o = {split2(-s), tag, __float_as_uint(__builtin_amdgcn_rsqf(q)), tag};
        st16(rs, lane == 0 ? WS_STAT + (bx * 8 + w) * 16 : 0xfffffff0u, o);
      }
      if (p >= 12 && p < 20) {
        const int i = (p - 12) >> 1;
        if (!(p & 1)) { pw[2][i] &= (w + 16 < (cpart == 2 ? 18 : 19) ? 0xffffffffu : 0u); cs[2 * i] = vadd(vadd(blo(pw[0][i]), blo(pw[1][i])), blo(pw[2][i])); }
        else cs[2 * i + 1] = vadd(vadd(bhi(pw[0][i]), bhi(pw[1][i])), bhi(pw[2][i]));
      }
      if (p == 20 || p == 21) for (int i = 2 * (p - 20); i < 2 * (p - 19); i++) { cs[i] = add32(cs[i], cs[i + 4]); OPAQUE(cs[i]); }
      if (p == 22) for (int i = 0; i < 2; i++) { cs[i] = add16(cs[i], cs[i + 2]); OPAQUE(cs[i]); }
      if (p == 23) for (int i = 0; i < 2; i++) { cs[i] += dpp<0x128>(cs[i]); OPAQUE(cs[i]); }
      if (p == 24) {
        *(LAS f32x2*)(LDS_MISC + w * 256 + ((lane & 7) * 8 + hb * 4 + g16 * 2) * 4) = f32x2{cs[0], cs[1]};
        if (A) *(LAS u32x2*)(LDS_PHI + 4 * 1568 + lane * 8) = u32x2{0u, 0u};
      }
    };
    sfor<0, 28>([&](auto nn) {
      constexpr int n = decltype(nn)::value;
      constexpr int c = (n + 2) >> 2, j = (n + 2) & 3;
      if (j == 0) {
        if (c == 1) asm volatile("s_waitcnt vmcnt(6)" : "+v"(tag), "+v"(px), "+v"(pw[0]), "+v"(pw[1]), "+v"(pw[2]) : "v"(tpf) : "memory");
        if (c == 2 || c == 3) WAITV(7); if (c == 4) WAITV(6); if (c == 5 || c == 6) WAITV(9); if (c == 7) asm volatile("s_waitcnt vmcnt(4)" :: "v"(pfd) : "memory");
        BAR();
        TK(4 + c);
      }
      if (A && n == 15) {
        const unsigned pa = LDS_PHI + 2 * role * 1568 + (4 * hb + q4) * 56 + g16 * 32 + p4 * 8;
        for (int u = 0; u < 2; u++) { wf[u][0] = trfrag(pa + u * 1568, pa + u * 1568 + 448); wf[u][1] = trfrag(pa + u * 1568 + 896, pa + u * 1568 + 1344); }
      }
      if (n == 14 && w == 7 && cpart < 3) {
        float t = 0.f;
        for (int i = 0; i < 8; i++) t += *(LAS float*)(LDS_MISC + i * 256 + lane * 4);
        u32x2 o = {split2(t), tag};
        __builtin_amdgcn_raw_buffer_store_b64(o, rs, WS_CS + (lc * 64 + lane) * 32 + cpart * 8, 0, 16);
      }
#define P0_ISSUE(h) do { \
      if (n < 2) ITEM_W(2 + n, h); \
      if (c >= 1 && c <= 4 && j == 0) ITEM_X(c + 2, h); \
      if (c >= 1 && c <= 3 && j == 2) ITEM_W(c + 3, h); \
      if (c == 4 && j >= 2) ITEM_N(j - 1, h); \
      if (c == 5 && j == 0) ITEM_N(0, h); \
      if (c == 5 && j == 2) ITEM_N(3, h); \
      if (c == 6 && j == 0) ITEM_N(4, h); } while (0)
      SB();
      if (n == 27) pa0 = MFMA(wq[n % 3][0], xf[n], acc0); else acc0 = MFMA(wq[n % 3][0], xf[n], acc0);
      SB();
      if (n + 2 < 28) xread(n + 2);
      wq[(n + 2) % 3][1] = wfrag(att, n + 2, 1);
      pre(2 * n);
      P0_ISSUE(0);
      if (n == 5) {
        const rsrc_t rsP = mkrs(wphi + (isq ? 8 * th : 4 * g) * 1568, A ? 4 * 1568 : 0);
        DMA1I(vp, rsP, lp, A ? LDS_PHI : LDS_BYTES, 0, st0);
      }
      if (n == 15) {
        const int u = bx * 8 + w;
        const rsrc_t rsF = u < 1568 ? mkrs(wd + u * 1024, 1024) : mkrs(wo + (u - 1568) * 1024, u < 1960 ? 1024 : 0);
        asm volatile("buffer_load_dword %0, %1, %2, 0 offen" : "=v"(pfd) : "v"((lane & 7) * 128), "s"(rsF) : "memory");
      }
      if (n == 26) ALD16(ca, vca, WS_CS);
      if (n == 27) ALD16(stt, vst, WS_STAT);
      SB();
      if (n == 27) pa1 = MFMA(wq[n % 3][1], xf[n], acc1); else acc1 = MFMA(wq[n % 3][1], xf[n], acc1);
      SB();
      wq[(n + 2) % 3][0] = wfrag(att, n + 2, 0);
      pre(2 * n + 1);
      P0_ISSUE(1);
      if (n == 26) ALD16(cb, vcb, WS_CS);
      SB();
    });
    wq[0][0] = wq[1][0]; wq[0][1] = wq[1][1]; wq[1][0] = wq[2][0]; wq[1][1] = wq[2][1];
    STAMP(1); TK(12);
  };

  if (ffn) {
    FRESH_LANE();
    aW[0] = g16 * 8192 + (8 * hb + q4) * 128 + (((2 * role) ^ ((q4 >> 1) & 1) ^ (g16 << 1)) * 32) + p4 * 8;
    aW[1] = aW[0] ^ 32; aW[2] = aW[0] + 65536; aW[3] = aW[1] + 65536;
    OPAQUE(aW[0]); OPAQUE(aW[1]); OPAQUE(aW[2]); OPAQUE(aW[3]);
    const unsigned vcs = (1344 + g16 * 1792 + 32 * role + (lane & 15)) * 32 + hb * 16;
    pass0(std::false_type{}, vcs + fi * 2048, vcs + fi * 2048 + 512);
    const int hl2 = 20 + th * 4 + t8;
    const unsigned hb0 = WS_AFRAG + (rbg * 144 + fi * 4 + 2 * role) * 1024 + lane * 16;
    const unsigned pf = lane == 0 ? WS_AFLAG + (rbg * 2 + role) * 128 + fi * 4 : 0xffffff00u;
    const bool two = t8 == 4;
    auto passn = [&](auto first) {
      constexpr bool F = decltype(first)::value, L = !F;
      const rsrc_t rsB = two ? rsV : rsW;
      unsigned voffB = voffW, voffB2 = voffW2;
      if (F && two) { voffB = voffX; voffB2 = voffX2; OPAQUE(voffB); }
      const unsigned sinc = two ? 128 * 896 : 64 * 3584;
      const unsigned ho = F ? hb0 : hb0 + 10 * 4096, vcp = vcs + (F ? fi : 10 + fi) * 2048, vcn = vcs + (F ? 10 + fi : hl2) * 2048;
      unsigned sA = (F ? 10 : hl2 - fi) * 128 + 4 * 64 * 3584, sB = two ? 2 * 128 * 896 : (hl2 - fi) * 128 - 64 * 3584;
      const unsigned c2 = two ? 0xf0000000u : hb0 + (hl2 - fi) * 4096;
      f32x16 acc0 = zero16(), acc1 = zero16();
      f32x16 qa0, qa1;
      float gt[2], gu[2], hprev; u32x4 hq;
      auto geglu = [&](int hs) {
#pragma unroll
        for (int e = 0; e < 16; e++) {
          const int d = hs - ((F ? 12 : 4) + 12 * e / 5);
          const float G = e < 8 ? qa0[e & 7] : qa1[e & 7], U = e < 8 ? qa0[(e & 7) + 8] : qa1[(e & 7) + 8];
          if (d == 0) gt[e & 1] = vmul(G, fmaf(G * G, k1, k0));
          if (d == 1) { gt[e & 1] = __builtin_amdgcn_exp2f(gt[e & 1]); gu[e & 1] = vmul(G, U); }
          if (d == 2) gt[e & 1] = __builtin_amdgcn_rcpf(fmaf(gt[e & 1], ia2, ia2));
          if (d == 3) {
            if (e & 1) hq[(e >> 1) & 3] = pk2(hprev, gu[1] * gt[1]); else hprev = vmul(gu[0], gt[0]);
            if ((e & 7) == 7) st16(rs, ho + (e >> 3) * 1024, hq);
          }
        }
      };
      sfor<0, 28>([&](auto nn) {
        constexpr int n = decltype(nn)::value;
        if (n == 10) { if (!F) { WAITV(12); st4(rs, pf, tag); } BAR(); TK(13); }
        if (n == 18) { asm volatile("s_waitcnt vmcnt(8)" : "+v"(ca), "+v"(cb)::"memory"); BAR(); TK(14); }
        if (n == 26) { if (F) WAITV(7); BAR(); TK(15); }
        if (n == (F ? 4 : 0)) {
          if (F) asm volatile("s_waitcnt vmcnt(5)" : "+v"(ca), "+v"(cb), "+v"(stt)::"memory");
          g_check<F>(rs, ca, cb, stt, vcp, vcp + 512, vst, tag, mlo);
        }
        SB();
        if (n == 27) pa0 = MFMA(wq[n % 3][0], xf[n], acc0); else acc0 = MFMA(wq[n % 3][0], xf[n], acc0);
        SB();
        if (!L || n + 2 < 28) wq[(n + 2) % 3][1] = wfrag(std::false_type{}, n + 2, 1);
        if (F && n == 4) make_fb();
        if (n == (F ? 5 : 1)) qa0 = MFMA(fold(ca), fb, pa0);
        if (n == 0 || n == 2) DMA1A(voffW, rsW, ldsw, (5 + n / 2) * SLOT, sA, wstr);
        if (n == 1 || n == 3) DMA2(voffW2, rsW, sA);
        if (L && n >= 4 && n < 16) ITEM_V((n - 4) / 2, n & 1);
        if (L && (n == 18 || n == 19)) ITEM_V(6, n & 1);
        if (L && (n == 21 || n == 22)) ITEM_V(7, !(n & 1));
        if (L && n >= 26) ITEM_V(n - 18, 0);
        if (!L && (n == 10 || n == 12 || n == 14)) DMA1A(voffB, rsB, ldsw, ((n - 10) / 2) * SLOT, sB, sinc);
        if (!L && (n == 18 || n == 21)) DMA1A(voffB, rsB, ldsw, (3 + (n - 18) / 3) * SLOT, sB, sinc);
        if (!L && (n == 11 || n == 13 || n == 15 || n == 19 || n == 22)) DMA2(voffB2, rsB, sB);
        geglu(2 * n);
        SB();
        if (n == 27) pa1 = MFMA(wq[n % 3][1], xf[n], acc1); else acc1 = MFMA(wq[n % 3][1], xf[n], acc1);
        SB();
        if (!L || n + 2 < 28) wq[(n + 2) % 3][0] = wfrag(std::false_type{}, n + 2, 0);
        if (F && n == 4) {
          const float rstd = __uint_as_float(stt[2]);
          const float c0 = -2.f * 0.7978845608028654f * 1.4426950408889634f;
          k0 = vmul(c0, rstd); k1 = vmul(c0 * 0.044715f * rstd * rstd, rstd); ia2 = __builtin_amdgcn_rcpf(rstd * rstd);
        }
        if (n == (F ? 5 : 1)) qa1 = MFMA(fold(cb), fb, pa1);
        if (F && (n == 0 || n == 17)) st16(rs, c2 + (n & 1) * 1024, u32x4{~0u, ~0u, ~0u, ~0u});
        if (!F && n == 20) ALD4(fl, VFL, WS_AFLAG);
        if (n == 8) ALD16(ca, vcn, WS_CS);
        if (n == 9) ALD16(cb, vcn + 512, WS_CS);
        if (L && n >= 26) ITEM_V(n - 18, 1);
        geglu(2 * n + 1);
        SB();
      });
      if (!L) { wq[0][0] = wq[1][0]; wq[0][1] = wq[1][1]; wq[1][0] = wq[2][0]; wq[1][1] = wq[2][1]; }
      STAMP(F ? 2 : 3); TK(16);
    };
    passn(std::true_type{});
    if (two) { ITEM_V(0, 0); ITEM_V(0, 1); ITEM_V(1, 0); ITEM_V(1, 1); ITEM_V(2, 0); ITEM_V(2, 1); ITEM_V(8, 0); ITEM_V(8, 1); ITEM_V(9, 0); ITEM_V(9, 1); WAITV(10); st4(rs, pf, tag); fl = ~tag; }
    int t8o = t8; asm volatile("" : "+s"(t8o));
    if (t8o != 4) passn(std::false_type{});
    {
      const unsigned vcn = vcs + (two ? 10 + fi : hl2) * 2048, hs = hb0 + (two ? 10 : hl2 - fi) * 4096;
      TK2(0);
      g_check<false>(rs, ca, cb, stt, vcn, vcn + 512, vst, tag, mlo);
      pa0 = MFMA(fold(ca), fb, pa0); pa1 = MFMA(fold(cb), fb, pa1);
      asm volatile("s_waitcnt vmcnt(7)" : "+v"(fl)::"memory");
      const bool early = seen(fl, tag, 0x3ffull);
      u32x4 o[2];
#pragma unroll
      for (int t = 0; t < 2; t++) {
        const f32x16& a = t ? pa1 : pa0;
        float hv[8];
        if (t == 0) { LDA2(0); LDA2(1); } else { TK2(1); WAITV(9); TK2(2); st4(rs, two ? 0xffffff00u : pf + 40, tag); }
#pragma unroll
        for (int i = 0; i < 8; i++) {
          const float e = __builtin_amdgcn_exp2f(a[i] * fmaf(a[i] * a[i], k1, k0));
          hv[i] = (a[i] * a[i + 8]) * __builtin_amdgcn_rcpf(fmaf(e, ia2, ia2));
        }
        o[t] = u32x4{pk2(hv[0], hv[1]), pk2(hv[2], hv[3]), pk2(hv[4], hv[5]), pk2(hv[6], hv[7])};
        OPAQUE(o[t]); SB();
      }
      st16(rs, hs, o[0]); st16(rs, hs + 1024, o[1]);
      TK2(3);
      asm volatile("s_waitcnt vmcnt(3)" : PIN4(0)::"memory");
      TK2(4);
      if (!early) {
        WAITV(0); st4(rs, two ? pf + 40 : 0xffffff00u, tag);
        poll(ws, WS_AFLAG + VFL, 0x3ffull, tag); LDA2(0); LDA2(1);
        asm volatile("s_waitcnt vmcnt(0)" : PIN4(0)::"memory");
      }
      if (two) BAR();
    }
  } else {
    aW[0] = (8 * hb + q4) * 448 + 112 * role + g16 * 32 + p4 * 8; aW[1] = aW[0] + 65536;
    OPAQUE(aW[0]); OPAQUE(aW[1]);
    const int head0 = (isq ? 8 * th : 4 * g) + 2 * role;
    const unsigned vcs = ((isq ? 0 : 448) + 28 * head0 + l31) * 32 + hb * 16;
    pass0(std::true_type{}, vcs, vcs + 28 * 32);
    float rstd, os;
    const float ninf = __uint_as_float(~mlo & 0xff800000u);
    u32x4 ph[2][4];
    Sm sm;
    {
      unsigned sA = wnext + 4 * 64 * 2688;
      const unsigned vca = vcs + wnext * 16;
      const unsigned oc = isq ? WS_AFRAG + (rbg * 144 + 112 + head0 * 2) * 1024 + lane * 16 : 0xf0000000u;
      f32x16 acc0 = zero16(), acc1 = zero16();
      f32x16 qa0, qa1;
      u32x4 kf[2][2];
      f32x16 p1;
      sfor<0, 28>([&](auto nn) {
        constexpr int n = decltype(nn)::value;
        if (n == 10) { BAR(); TK(13); }
        if (n == 18) {
          asm volatile("s_waitcnt vmcnt(2)" : "+v"(ca), "+v"(cb)::"memory"); BAR(); TK(14);
          st4(rs, tid == 0 ? WS_CFLAG + ((isq ? 4 + th : g) * 16 + m) * 128 : 0xf0000000u, tag);
        }
        if (n == 26) { BAR(); TK(15); }
        if (n == 6 || n == 11) {
          FRESH_LANE();
          const unsigned co = isq ? (lane < 16 && n == 6 ? WS_KVRED + ((2 * th + (w >> 2)) * 16 + m) * 1024 + (w & 3) * 256 + lane * 16 : 0xf0000000u)
                                  : WS_KVPART + ((g * 8 + w) * 16 + m) * 2048 + (n & 1) * 1024 + lane * 16;
          st16(rs, co, u32x4{~0u, ~0u, ~0u, ~0u});
        }
        if (n == 4) {
          asm volatile("s_waitcnt vmcnt(4)" : "+v"(ca), "+v"(cb), "+v"(stt)::"memory");
          g_check<true>(rs, ca, cb, stt, vcs, vcs + 28 * 32, vst, tag, mlo);
        }
        if (n == 19) g_check<false>(rs, ca, cb, stt, vca, vca + 28 * 32, vst, tag, mlo);
        SB();
        if (n == 27) pa0 = MFMA(wq[n % 3][0], xf[n], acc0); else acc0 = MFMA(wq[n % 3][0], xf[n], acc0);
        SB();
        if (n + 2 < 28) wq[(n + 2) % 3][1] = wfrag(std::true_type{}, n + 2, 1);
        if (n == 4) { make_fb(); rstd = __uint_as_float(stt[2]); os = isq ? 1.f : rstd; }
        if (n == 5) qa0 = MFMA(fold(ca), fb, pa0);
        if (n == 7 || n == 8) kf[n - 7][0] = pack8u(n == 8 ? qa1 : qa0, 0);
        if (n == 8) sm.p = MFMA(wf[0][0], asf(kf[0][0]), zero16());
        if (n == 18) p1 = MFMA(wf[1][0], asf(kf[1][0]), zero16());
        if (n == 10) sm_minmax(sm, ninf);
        if (n == 11) sm_consts(sm, rstd);
        if (n >= 12 && n < 20) sm_exp(sm, 2 * (n - 12), ninf);
        if (n == 20) sm_sum(sm, os);
        if (n >= 21 && n < 25) ph[0][n - 21] = sm_norm(sm, n - 21);
        if (n == 25) sm_consts(sm, rstd);
        if (n >= 26) sm_exp(sm, 2 * (n - 26), ninf);
        if (n == 0 || n == 2) DMA1A(voffW, rsW, ldsw, (5 + n / 2) * SLOT, sA, wstr);
        if (n == 1 || n == 3) DMA2(voffW2, rsW, sA);
        if (n >= 4 && n < 10) ITEM_V((n - 4) / 2, n & 1);
        if (n == 10) {
          const rsrc_t rsP = mkrs(wphi + (8 * th + 4) * 1568, isq ? 4 * 1568 : 0);
          FRESH_LANE();
          DMA1I(lp + lane * 16, rsP, lp, 0, 0, st0);
        }
        if (n == 20) acc0 = MFMA(fold(ca), fb, acc0);
        SB();
        if (n == 27) pa1 = MFMA(wq[n % 3][1], xf[n], acc1); else acc1 = MFMA(wq[n % 3][1], xf[n], acc1);
        SB();
        if (n + 2 < 28) wq[(n + 2) % 3][0] = wfrag(std::true_type{}, n + 2, 0);
        if (n == 5) qa1 = MFMA(fold(cb), fb, pa1);
        if (n >= 5 && n % 3 == 2) { constexpr int i = (n - 5) / 3; __builtin_amdgcn_raw_buffer_store_b128(u32x4{~0u, ~0u, ~0u, ~0u}, rs, oc, (i & 3) * 1024 + (i >> 2) * 8192, 16); }
        if (n == 8) ALD16(ca, vca, WS_CS);
        if (n == 9) ALD16(cb, vca + 28 * 32, WS_CS);
        if (n == 7 || n == 8) { kf[n - 7][1] = pack8u(n == 8 ? qa1 : qa0, 1); kf[n - 7][1][2] &= mlo; kf[n - 7][1][3] &= mlo; }
        if (n == 8) sm.p = MFMA(wf[0][1], asf(kf[0][1]), sm.p);
        if (n == 18) p1 = MFMA(wf[1][1], asf(kf[1][1]), p1);
        if (n >= 12 && n < 20) sm_exp(sm, 2 * (n - 12) + 1, ninf);
        if (n == 20) { sm.p = p1; sm_minmax(sm, ninf); }
        if (n >= 26) sm_exp(sm, 2 * (n - 26) + 1, ninf);
        if (n == 20) acc1 = MFMA(fold(cb), fb, acc1);
        SB();
      });
      for (int i = 4; i < 16; i++) sm_exp(sm, i, ninf);
      sm_sum(sm, os);
    }
    STAMP(2); TK(16);
    FRESH_LANE();
    const unsigned xrow = 32 * rb + l31;
    if (!isq) {
      const unsigned ir = split2(__builtin_amdgcn_rcpf(rstd)) & ~mlo;
      const unsigned wa = (2 * role * 128 + xrow) * 200 + hb * 8;
#pragma unroll
      for (int u = 0; u < 2; u++) {
        if (u) for (int f = 0; f < 4; f++) ph[1][f] = sm_norm(sm, f);
#pragma unroll
        for (int f = 0; f < 6; f++) {
          u32x4 v = f < 4 ? ph[u][f] : pack8u(u ? pa1 : pa0, f - 4);
          if (f == 5) { v[2] = (v[2] & mlo) | ir; v[3] &= mlo; }
          *(LAS u32x2*)(wa + u * 25600 + f * 32) = u32x2{v[0], v[1]}; *(LAS u32x2*)(wa + u * 25600 + f * 32 + 16) = u32x2{v[2], v[3]};
        }
      }
      BAR();
      TK(17);
      unsigned ra = (w >> 1) * 25600 + (8 * hb + q4) * 200 + g16 * 32 + p4 * 8, rp = ra + (w & 1) * 64;
      OPAQUE(ra); OPAQUE(rp);
      f32x16 kv = zero16();
#pragma unroll
      for (int s = 0; s < 8; s++) kv = MFMA(trfrag(rp + s * 3200, rp + s * 3200 + 800), trfrag(ra + 128 + s * 3200, ra + 128 + s * 3200 + 800), kv);
      ALD4(fl, VFL, WS_AFLAG);
      st16(rs, WS_KVPART + ((g * 8 + w) * 16 + m) * 2048 + lane * 16, pack8u(kv, 0));
      st16(rs, WS_KVPART + ((g * 8 + w) * 16 + m) * 2048 + 1024 + lane * 16, pack8u(kv, 1));
      STAMP(3); TK(18);
      BAR();
      asm volatile("s_waitcnt vmcnt(2)" : "+v"(fl)::"memory");
      NEED(0x3ffull);
      LDA2(0); LDA2(1);
      sfor<3, 10>([&](auto cc) { ITEM_V(decltype(cc)::value, 0); ITEM_V(decltype(cc)::value, 1); });
      asm volatile("s_waitcnt vmcnt(14)" : PIN4(0)::"memory");
      TK(19);
    } else {
      u32x4 ph2[2][4];
      const unsigned vcf = (lane < 32 ? 2 * th * 16 + lane : (4 + th) * 16 + (lane & 15)) * 128;
      ALD4(fl, vcf, WS_CFLAG);
      for (int f = 0; f < 4; f++) ph[1][f] = sm_norm(sm, f);
      {
        const unsigned pa = 2 * role * 1568 + (4 * hb + q4) * 56 + g16 * 32 + p4 * 8;
#pragma unroll
        for (int u = 0; u < 2; u++) {
          u32x4 k1 = pack8u(u ? pa1 : pa0, 1); k1[2] &= mlo; k1[3] &= mlo;
          sm.p = MFMA(trfrag(pa + u * 1568, pa + u * 1568 + 448), asf(pack8u(u ? pa1 : pa0, 0)), zero16());
          sm.p = MFMA(trfrag(pa + u * 1568 + 896, pa + u * 1568 + 1344), asf(k1), sm.p);
          sm_minmax(sm, ninf); sm_consts(sm, rstd);
          for (int i = 0; i < 16; i++) sm_exp(sm, i, ninf);
          sm_sum(sm, 1.f);
          for (int f = 0; f < 4; f++) { ph2[u][f] = sm_norm(sm, f); OPAQUE(ph2[u][f]); }
        }
      }
      STAMP(3); TK(17);
      asm volatile("s_waitcnt vmcnt(0)" : "+v"(fl)::"memory");
      st4(rs, l31 == 0 ? WS_AFLAG + (rbg * 2 + hb) * 128 + (28 + th * 2 + role) * 4 : 0xf0000000u, tag);
      if (!seen(fl, tag, ~0ull)) poll(ws, WS_CFLAG + vcf, ~0ull, tag);
      {
        const int gg = 2 * th + (w >> 2);
        const unsigned pb = WS_KVPART + ((gg * 8 + (m >> 1)) * 16 + (lane >> 4)) * 2048 + (m & 1) * 1024 + ((w & 3) * 16 + (lane & 15)) * 16;
        u32x4 pv[4];
#pragma unroll 1
        for (int it = 0; it < SPIN_CAP; ++it) {
          unsigned mx = 0;
#pragma unroll
          for (int i = 0; i < 4; i++) pv[i] = ld16(rs, pb + i * 8192);
#pragma unroll
          for (int i = 0; i < 4; i++) mx = umax4(mx, pv[i]);
          if (fresh(mx)) break;
          __builtin_amdgcn_s_sleep(1);
        }
        STAMP(5); TK(18);
        float t[8];
#pragma unroll
        for (int j = 0; j < 4; j++) {
          t[2 * j] = (blo(pv[0][j]) + blo(pv[1][j])) + (blo(pv[2][j]) + blo(pv[3][j]));
          t[2 * j + 1] = (bhi(pv[0][j]) + bhi(pv[1][j])) + (bhi(pv[2][j]) + bhi(pv[3][j]));
        }
        for (int j = 0; j < 8; j++) { t[j] = add32(t[j], t[j]); t[j] = add16(t[j], t[j]); }
        st16(rs, lane < 16 ? WS_KVRED + (gg * 16 + m) * 1024 + ((w & 3) * 16 + lane) * 16 : 0xf0000000u, u32x4{pk2(t[0], t[1]), pk2(t[2], t[3]), pk2(t[4], t[5]), pk2(t[6], t[7])});
        STAMP(6); TK(19);
      }
      {
        u32x4 gv[4];
        __builtin_amdgcn_s_sleep(12);
#pragma unroll 1
        for (int it = 0; it < SPIN_CAP; ++it) {
          unsigned mx = 0;
#pragma unroll
          for (int i = 0; i < 4; i++) gv[i] = ld16(rs, WS_KVRED + ((8 * th + w) * 4 + i) * 1024 + lane * 16);
#pragma unroll
          for (int i = 0; i < 4; i++) mx = umax4(mx, gv[i]);
          if (fresh(mx)) break;
          __builtin_amdgcn_s_sleep(4);
        }
#pragma unroll
        for (int i = 0; i < 4; i++) *(LAS u32x4*)(SLOT + (w * 4 + i) * 1024 + lane * 16) = gv[i];
      }
      BAR();
      STAMP(7); TK(20);
      u32x4 kf[4][4];
#pragma unroll
      for (int u = 0; u < 4; u++)
#pragma unroll
        for (int i = 0; i < 4; i++) kf[u][i] = *(LAS u32x4*)(SLOT + ((2 * role + (u & 1) + 4 * (u >> 1)) * 4 + i) * 1024 + lane * 16);
#pragma unroll
      for (int u = 0; u < 4; u++) {
        f32x16 nm = zero16();
        for (int i = 0; i < 4; i++) nm = MFMA(asf(kf[u][i]), asf(u < 2 ? ph[u & 1][i] : ph2[u & 1][i]), nm);
        float dl, dh; bcast32(nm[12] + nm[13], dl, dh);
        const float rd = __builtin_amdgcn_rcpf(dh + 1e-6f);
        for (int i = 0; i < 16; i++) nm[i] *= rd;
        u32x4 o1 = pack8u(nm, 1); o1[2] &= mlo; o1[3] &= mlo;
        const unsigned oo = WS_AFRAG + (rbg * 144 + 112 + (head0 + (u & 1) + 4 * (u >> 1)) * 2) * 1024 + lane * 16;
        st16(rs, oo, pack8u(nm, 0));
        st16(rs, oo + 1024, o1);
      }
      STAMP(4); TK(21); DUMPTK();
      if (bx == 7 * 4 * 8 && w == 0) {
#pragma unroll 1
        for (int i = 0; i < 8; i++) poll(ws, WS_AFLAG + (2 * i + hb) * 8 * 128 + l31 * 4, 0x500003ff500003ffull, tag);
        poll(ws, WS_CFLAG + lane * 128, ~0ull, tag);
        st4(rs, WS_EPOCH, tag);
      }
      return;
    }
  }

  {
    FRESH_LANE();
    const int tid = w * 64 + lane;
    const unsigned swz = (((lane & 7) ^ rmask(16 * w + rl)) << 4);
    STAMP(5);
    LDA2(2); LDA2(3); LDA2(4);
    const unsigned r0 = 32 * kq + 4 * hb + q4;
    unsigned bV[3][4];
#pragma unroll
    for (int i = 0; i < 12; i++) { bV[i >> 2][i & 3] = ((r0 * 128 + (((2 * g16 + (p4 >> 1)) ^ rmask(r0)) << 4) + (p4 & 1) * 8) ^ ((i & 1) * 64) ^ ((i & 2) * 16)) + (i >> 2) * 65536; OPAQUE(bV[i >> 2][i & 3]); }
    const unsigned voffO = (28 * (w >> 1) + 16 * (w & 1) + rl) * 896 + swz;
    const unsigned voffO2 = voffO + (((w & 1) && rl >= 4) ? 0 : 8 * 896) - 1024;
    const rsrc_t rsO = mkrs(wo + ct * 128, 448 * 896 - ct * 128);
    const size_t go = (size_t)(rt * 64 + (tid >> 3)) * 896 + ct * 128 + (tid & 7) * 16;
    u32x4 xr;
    f32x16 acc0 = zero16(), acc1 = zero16();
    s16x8 wv[2][4];
    auto vread = [&](int c) {
      const int sl = (c + 7) % 10; const unsigned so = (sl & 3) * SLOT;
      for (int i = 0; i < 4; i++) wv[c & 1][i] = trfrag(bV[sl >> 2][i] + so + (i >> 1) * 2048, bV[sl >> 2][i] + so + (i >> 1) * 2048 + 1024);
    };
    vread(0);
    TK(20);
    sfor<0, 18>([&](auto cc) {
      constexpr int c = decltype(cc)::value;
      asm volatile("s_waitcnt vmcnt(%3)" : "+v"(af[2 * c]), "+v"(af[2 * c + 1]), "+v"(fl) : "n"(P2::wait(c)) : "memory");
      if constexpr (c >= 10) {
        constexpr int c0 = c < 14 ? 10 : 14;
#pragma unroll 1
        for (int it = 0; it < SPIN_CAP; ++it) {
          if (__builtin_expect(fresh(umax4(umax4(0u, af[2 * c]), af[2 * c + 1])), 1)) break;
          WAITV(0);
          __builtin_amdgcn_s_sleep(4);
          LDA2(c); if (c + 1 < c0 + 4) LDA2(c0 + 3); if (c + 2 < c0 + 4) LDA2(c0 + 2); if (c + 3 < c0 + 4) LDA2(c0 + 1);
          asm volatile("s_waitcnt vmcnt(0)" : PIN8(c0)::"memory");
        }
        if (c == 10) { STAMP(7); TK(21); } if (c == 14) { STAMP(12); TK(22); }
      }
      if (P2::bar(c)) BAR();
      TK2(5 + c);
      if (c + 1 < 18) vread(c + 1);
      SB();
      acc0 = MFMA(wv[c & 1][0], asf(af[2 * c]), acc0); acc1 = MFMA(wv[c & 1][1], asf(af[2 * c]), acc1);
      if (c == P2::FL1 || c == P2::FL2) ALD4(fl, VFL, WS_AFLAG);
      constexpr int j = P2::vj(c);
      if (j >= 0 && j < 4) { ITEM_V(j + 10, 0); ITEM_V(j + 10, 1); }
      if (j >= 4) { DMA1I(voffO, rsO, ldsw, ((j + 7) % 10) * SLOT, (j - 4) * (112 * 896), st0); DMA2(voffO2, rsO, st0); }
      SB();
      acc0 = MFMA(wv[c & 1][2], asf(af[2 * c + 1]), acc0); acc1 = MFMA(wv[c & 1][3], asf(af[2 * c + 1]), acc1);
      if (c == P2::N1) { NEED(0xffc00ull); STAMP(6); }
      if (c == P2::N2) NEED(0xf0000000ull);
      if constexpr (P2::a0(c) < P2::a0(c + 1)) LDA2(P2::a0(c));
      if constexpr (P2::a0(c) + 1 < P2::a0(c + 1)) LDA2(P2::a0(c) + 1);
      if constexpr (P2::a0(c) + 2 < P2::a0(c + 1)) LDA2(P2::a0(c) + 2);
      if constexpr (P2::a0(c) + 3 < P2::a0(c + 1)) LDA2(P2::a0(c) + 3);
      static_assert(P2::a0(c + 1) - P2::a0(c) <= 4, "schedule");
      if (c == 13) asm volatile("global_load_dwordx4 %0, %1, off" : "=v"(xr) : "v"(x + go) : "memory");
      SB();
    });
    STAMP(8);
    const unsigned wa = 5 * SLOT + (kq * 64 + rb2 * 32 + l31) * 272 + hb * 16;
#pragma unroll
    for (int gq = 0; gq < 4; gq++) {
      f32x4 v0 = {acc0[4 * gq], acc0[4 * gq + 1], acc0[4 * gq + 2], acc0[4 * gq + 3]};
      f32x4 v1 = {acc1[4 * gq], acc1[4 * gq + 1], acc1[4 * gq + 2], acc1[4 * gq + 3]};
      *(LAS f32x4*)(wa + gq * 32) = v0; *(LAS f32x4*)(wa + 128 + gq * 32) = v1;
    }
    BAR();
    const unsigned ra = 5 * SLOT + (tid >> 3) * 272 + (tid & 7) * 32;
    f32x4 s0 = *(LAS f32x4*)ra, s1 = *(LAS f32x4*)(ra + 16);
#pragma unroll
    for (int k = 1; k < 4; k++) { s0 += *(LAS f32x4*)(ra + k * 64 * 272); s1 += *(LAS f32x4*)(ra + k * 64 * 272 + 16); }
    asm volatile("s_waitcnt vmcnt(0)" : "+v"(xr)::"memory");
    u32x4 o;
    o[0] = pk2(s0[0] + blo(xr[0]), s0[1] + bhi(xr[0]));
    o[1] = pk2(s0[2] + blo(xr[1]), s0[3] + bhi(xr[1]));
    o[2] = pk2(s1[0] + blo(xr[2]), s1[1] + bhi(xr[2]));
    o[3] = pk2(s1[2] + blo(xr[3]), s1[3] + bhi(xr[3]));
    *(u32x4*)(out + go) = o;
    STAMP(9); STAMPC(15); TK(23); TK2(23); DUMPTK();
  }
}

extern "C" void kernel_launch(void* const* d_in, const int* in_sizes, int n_in, void* d_out, int out_size, void* d_ws, size_t ws_size, hipStream_t stream) {
  (void)hipFuncSetAttribute((const void*)fused_kernel, hipFuncAttributeMaxDynamicSharedMemorySize, LDS_BYTES);
  fused_kernel<<<256, 512, LDS_BYTES, stream>>>((const char*)d_in[0], (const char*)d_in[1], (const char*)d_in[2], (const char*)d_in[3],
                                                (const char*)d_in[5], (const char*)d_in[6], (const char*)d_in[7], (char*)d_out, (char*)d_ws);
}
```

```cpp
#include <hip/hip_runtime.h>
#include <hip/hip_bf16.h>
#include <stdint.h>
#include <type_traits>
#pragma clang diagnostic ignored "-Wint-to-pointer-cast"
#pragma clang diagnostic ignored "-Warray-bounds"

#define DI __device__ __forceinline__
#define LAS __attribute__((address_space(3)))
#define GAS __attribute__((address_space(1)))
typedef __attribute__((ext_vector_type(8))) short s16x8;
typedef __attribute__((ext_vector_type(4))) short s16x4;
typedef __attribute__((ext_vector_type(16))) float f32x16;
typedef __attribute__((ext_vector_type(4))) float f32x4;
typedef __attribute__((ext_vector_type(2))) float f32x2;
typedef __attribute__((ext_vector_type(4))) unsigned u32x4;
typedef __attribute__((ext_vector_type(2))) unsigned u32x2;
typedef __attribute__((ext_vector_type(2))) __bf16 bf16x2;
typedef GAS unsigned gu32;
typedef __amdgpu_buffer_rsrc_t rsrc_t;
#define RLX_AGENT __ATOMIC_RELAXED, __HIP_MEMORY_SCOPE_AGENT

constexpr unsigned WS_EPOCH = 0;
constexpr unsigned WS_CFLAG = 1024;
constexpr unsigned WS_AFLAG = 16384;
constexpr unsigned WS_STAT = WS_AFLAG + 128 * 128;
constexpr unsigned WS_CS = WS_STAT + 2048 * 16;
constexpr unsigned WS_KVRED = 262144;
constexpr unsigned WS_KVPART = WS_KVRED + 131072;
constexpr unsigned WS_AFRAG = WS_KVPART + (2u << 20);
constexpr unsigned WS_STAMP = WS_AFRAG + 64u * 144u * 1024u;
constexpr unsigned WS_BYTES = WS_STAMP;
static_assert(WS_CFLAG + 96 * 128 <= WS_AFLAG && WS_CS + 4928 * 32 <= WS_KVRED, "ws map");

constexpr int LDS_BYTES = 163840;
constexpr unsigned SLOT = 16384;
constexpr unsigned LDS_MISC = 0, LDS_PHI = 2048;
constexpr unsigned CODE_LINES = 296;
constexpr int SPIN_CAP = 1 << 14;

#define HOTA(c) (c)
#define HOTV(c) (c)
#define MFMA(a, b, c) __builtin_amdgcn_mfma_f32_32x32x16_bf16((a), (b), (c), 0, 0, 0)
#define WAITV(n) asm volatile("s_waitcnt vmcnt(" #n ")" ::: "memory")
#define BAR() do { asm volatile("s_waitcnt lgkmcnt(0)" ::: "memory"); __builtin_amdgcn_s_barrier(); asm volatile("" ::: "memory"); } while (0)
#define SB() __builtin_amdgcn_sched_barrier(0)
#define OPAQUE(v) asm volatile("" : "+v"(v))
template <int N> DI void waitv() { asm volatile("s_waitcnt vmcnt(%0)" ::"n"(N) : "memory"); }
template <int N> using IC = std::integral_constant<int, N>;
template <int I, int N, class F> DI void sfor(F&& f) { if constexpr (I < N) { f(IC<I>{}); sfor<I + 1, N>(f); } }

#define STAMP(i)
#define STAMPC(i)
#define TK(i)
#define TK2(i)
#define DUMPTK()

#define DMA1I(voff, rsrc, ldsb, LIMM, SOFF, tmp) asm volatile("s_add_u32 m0, %3, %4\n\ts_mov_b32 %0, %5\n\tbuffer_load_dwordx4 %1, %2, %0 offen lds" \
    : "=&s"(tmp) : "v"(voff), "s"(rsrc), "s"(ldsb), "n"(LIMM), "n"(SOFF) : "memory", "scc")
#define DMA1A(voff, rsrc, ldsb, LIMM, soff, sinc) asm volatile("s_add_u32 m0, %3, %4\n\ts_add_u32 %0, %0, %5\n\tbuffer_load_dwordx4 %1, %2, %0 offen lds" \
    : "+s"(soff) : "v"(voff), "s"(rsrc), "s"(ldsb), "n"(LIMM), "s"(sinc) : "memory", "scc")
#define DMA2(voff, rsrc, soff) asm volatile("buffer_load_dwordx4 %0, %1, %2 offen offset:1024 lds" :: "v"(voff), "s"(rsrc), "s"(soff) : "memory")
#define ALD16(dst, voff, soff) asm volatile("buffer_load_dwordx4 %0, %1, %2, %3 offen sc1" : "=v"(dst) : "v"(voff), "s"(rs), "s"(soff) : "memory")
#define ALD4(dst, voff, soff) asm volatile("buffer_load_dword %0, %1, %2, %3 offen sc1" : "=v"(dst) : "v"(voff), "s"(rs), "s"(soff) : "memory")

#define FRESH_LANE() int lane; asm volatile("v_mbcnt_lo_u32_b32 %0, -1, 0\n\tv_mbcnt_hi_u32_b32 %0, -1, %0" : "=v"(lane)); \
  const int g16 = (lane >> 4) & 1, q4 = (lane & 15) >> 2, p4 = lane & 3, l31 = lane & 31, hb = lane >> 5, rl = lane >> 3; \
  (void)g16; (void)q4; (void)p4; (void)l31; (void)hb; (void)rl
DI rsrc_t mkrs(const void* p, unsigned bytes) { return __builtin_amdgcn_make_buffer_rsrc((void*)p, 0, bytes, 0x00020000); }
DI s16x8 trfrag(unsigned a0, unsigned a1) {
  s16x4 lo = __builtin_amdgcn_ds_read_tr16_b64_v4i16((LAS s16x4*)a0);
  s16x4 hi = __builtin_amdgcn_ds_read_tr16_b64_v4i16((LAS s16x4*)a1);
  return __builtin_shufflevector(lo, hi, 0, 1, 2, 3, 4, 5, 6, 7);
}
DI unsigned pk2(float a, float b) { f32x2 p = {a, b}; return __builtin_bit_cast(unsigned, __builtin_convertvector(p, bf16x2)); }
DI float dot2(unsigned a, unsigned b, float c) { return __builtin_amdgcn_fdot2_f32_bf16(__builtin_bit_cast(bf16x2, a), __builtin_bit_cast(bf16x2, b), c, false); }
DI float blo(unsigned u) { return __uint_as_float(u << 16); }
DI float bhi(unsigned u) { return __uint_as_float(u & 0xffff0000u); }
DI float fand(float v, unsigned m) { return __uint_as_float(__float_as_uint(v) & m); }
DI float vmul(float a, float b) { float r = a * b; OPAQUE(r); return r; }
DI float vadd(float a, float b) { float r = a + b; OPAQUE(r); return r; }
DI float max3(float a, float b, float c) { return __builtin_elementwise_maximum(__builtin_elementwise_maximum(a, b), c); }
DI float min3(float a, float b, float c) { return __builtin_elementwise_minimum(__builtin_elementwise_minimum(a, b), c); }
DI float add32(float a, float b) { auto r = __builtin_amdgcn_permlane32_swap(__float_as_uint(a), __float_as_uint(b), false, false); return __uint_as_float(r[0]) + __uint_as_float(r[1]); }
DI float add16(float a, float b) { auto r = __builtin_amdgcn_permlane16_swap(__float_as_uint(a), __float_as_uint(b), false, false); return __uint_as_float(r[0]) + __uint_as_float(r[1]); }
DI void bcast32(float v, float& lo, float& hi) { auto r = __builtin_amdgcn_permlane32_swap(__float_as_uint(v), __float_as_uint(v), false, false); lo = __uint_as_float(r[0]); hi = __uint_as_float(r[1]); }
template <int CTRL> DI float dpp(float x) { return __builtin_bit_cast(float, __builtin_amdgcn_mov_dpp(__builtin_bit_cast(int, x), CTRL, 0xf, 0xf, true)); }
DI float wave_sum(float v) {
  v += dpp<0xB1>(v); v += dpp<0x4E>(v); v += dpp<0x141>(v); v += dpp<0x128>(v);
  v = add16(v, v); return add32(v, v);
}
DI unsigned split2(float v) {
  const unsigned h = pk2(v, 0.f);
  return pk2(__uint_as_float(h << 16), v - __uint_as_float(h << 16));
}
DI u32x4 pack8u(const f32x16& x, int s) {
  u32x4 u = {pk2(x[8 * s], x[8 * s + 1]), pk2(x[8 * s + 2], x[8 * s + 3]), pk2(x[8 * s + 4], x[8 * s + 5]), pk2(x[8 * s + 6], x[8 * s + 7])};
  return u;
}
DI s16x8 asf(u32x4 u) { return __builtin_bit_cast(s16x8, u); }
DI unsigned rmask(unsigned row) { return (((row >> 1) & 1) << 2) | ((row >> 2) & 1) | ((row >> 3) & 2); }
DI f32x16 zero16() { f32x16 z; for (int i = 0; i < 16; i++) z[i] = 0.f; return z; }
DI void st16(rsrc_t rs, unsigned off, u32x4 v) { __builtin_amdgcn_raw_buffer_store_b128(v, rs, off, 0, 16); }
DI void st4(rsrc_t rs, unsigned off, unsigned v) { __builtin_amdgcn_raw_buffer_store_b32(v, rs, off, 0, 16); }
DI u32x4 ld16(rsrc_t rs, unsigned off) { return __builtin_amdgcn_raw_buffer_load_b128(rs, off, 0, 16); }
DI bool seen(unsigned v, unsigned tag, unsigned long long need) { return (__builtin_amdgcn_ballot_w64(v == tag) & need) == need; }
DI void poll(char* ws, unsigned off, unsigned long long need, unsigned tag) {
#pragma unroll 1
  for (int it = 0; it < SPIN_CAP; ++it) {
    const unsigned v = __hip_atomic_load((gu32*)(ws + off), RLX_AGENT);
    if (seen(v, tag, need)) break;
    __builtin_amdgcn_s_sleep(2);
  }
  __builtin_amdgcn_fence(__ATOMIC_ACQUIRE, "wavefront");
}
template <bool ST> DI void g_check(rsrc_t rs, u32x4& a, u32x4& b, u32x4& st, unsigned va, unsigned vb, unsigned vs, unsigned tag, unsigned mlo) {
#pragma unroll 1
  for (int it = 0; it < SPIN_CAP; ++it) {
    unsigned bad = (a[1] ^ tag) | (b[1] ^ tag) | (((a[3] ^ tag) | (b[3] ^ tag)) & mlo);
    if (ST) bad |= (st[1] ^ tag) | (st[3] ^ tag);
    if (__builtin_expect(__builtin_amdgcn_ballot_w64(bad != 0) == 0ull, 1)) break;
    ALD16(a, va, WS_CS); ALD16(b, vb, WS_CS); if (ST) ALD16(st, vs, WS_STAT);
    if (ST) asm volatile("s_waitcnt vmcnt(0)" : "+v"(a), "+v"(b), "+v"(st)::"memory"); else asm volatile("s_waitcnt vmcnt(0)" : "+v"(a), "+v"(b)::"memory");
  }
}

DI unsigned umax4(unsigned m, const u32x4& v) { return max(max(max(m, v[0]), v[1]), max(v[2], v[3])); }
DI bool fresh(unsigned m) { return __builtin_amdgcn_ballot_w64(m == 0xffffffffu) == 0ull; }

struct Sm { float L, cx, cn, mx, mn, s1, s2; f32x16 p, ep, en; };
DI void sm_minmax(Sm& s, float ninf) {
  const f32x16& p = s.p;
  float a = max3(p[0], p[1], p[2]), b = min3(p[0], p[1], p[2]);
  for (int i = 3; i < 11; i += 2) { a = max3(a, p[i], p[i + 1]); b = min3(b, p[i], p[i + 1]); }
  float c = max3(p[12], p[13], p[14]), d = min3(p[12], p[13], p[14]);
  c = max3(c, p[15], p[15]) + ninf; d = min3(d, p[15], p[15]) - ninf;
  s.mx = max3(a, p[11], c); s.mn = min3(b, p[11], d);
}
DI void sm_consts(Sm& s, float sc) {
  auto r = __builtin_amdgcn_permlane32_swap(__float_as_uint(s.mx), __float_as_uint(-s.mn), false, false);
  const float m = max3(__uint_as_float(r[0]), __uint_as_float(r[1]), __uint_as_float(r[1]));
  float mx, nmn; bcast32(m, mx, nmn);
  s.L = sc * 1.4426950408889634f; s.cx = -mx * s.L; s.cn = -nmn * s.L; s.s1 = 0.f; s.s2 = 0.f;
}
DI void sm_exp(Sm& s, int i, float ninf) {
  if (i == 12) { s.cx += ninf; s.cn += ninf; }
  s.ep[i] = __builtin_amdgcn_exp2f(fmaf(s.p[i], s.L, s.cx)); s.en[i] = __builtin_amdgcn_exp2f(fmaf(s.p[i], -s.L, s.cn));
  if (i > 0) { s.s1 = vadd(s.s1, s.ep[i - 1]); s.s2 = vadd(s.s2, s.en[i - 1]); }
}
DI void sm_sum(Sm& s, float os) {
  const float t = add32(vadd(s.s1, s.ep[15]), vadd(s.s2, s.en[15]));
  bcast32(os * __builtin_amdgcn_rcpf(t), s.s1, s.s2);
}
DI u32x4 sm_norm(const Sm& s, int f) {
  const f32x16& e = f < 2 ? s.ep : s.en; const float r = f < 2 ? s.s1 : s.s2; const int o = 8 * (f & 1);
  u32x4 u;
  for (int i = 0; i < 4; i++) u[i] = pk2(vmul(e[o + 2 * i], r), vmul(e[o + 2 * i + 1], r));
  return u;
}

struct P2 {
  static constexpr int a0(int s) { return s <= 2 ? 5 : s == 3 ? 7 : s + 5 < 18 ? s + 5 : 18; }
  static constexpr int vs(int j) { return j + 1; }
  static constexpr int vj(int s) { for (int j = 0; j < 8; j++) if (vs(j) == s) return j; return -1; }
  static constexpr int FL1 = 0, N1 = 2, FL2 = 6, N2 = 9;
  static constexpr bool bar(int c) { return c == 1 || c == 3 || c == 5 || c == 7 || c == 12; }
  static constexpr int vneed(int c) { return c == 1 ? 3 : c == 3 ? 9 : c == 7 ? 12 : c == 12 ? 17 : -1; }
  static constexpr int na(int s) { return 2 * (a0(s + 1) - a0(s)); }
  static constexpr int nops(int s) { return (vj(s) >= 0 ? 2 : 0) + na(s) + (s == 13 || s == FL1 || s == FL2 ? 1 : 0); }
  static constexpr int upto(int from, int c) { int n = 0; for (int s = from; s < c; s++) n += nops(s); return n; }
  static constexpr int wait(int c) {
    int n = 63;
    if (c >= 2 && c < 5) n = 2 * (4 - c) + upto(0, c);
    for (int s = 0; s < c; s++) if (a0(s) <= c && c < a0(s + 1)) n = 2 * (a0(s + 1) - 1 - c) + upto(s + 1, c);
    if (c == N1 || c == N2) { const int f = upto(c == N1 ? FL1 : FL2, c) - 1; if (f < n) n = f; }
    if (vneed(c) >= 10) { const int s = vs(vneed(c) - 10), v = na(s) + upto(s + 1, c); if (v < n) n = v; }
    else if (vneed(c) >= 0) { const int v = 2 * (9 - vneed(c)) + 6 + upto(0, c); if (v < n) n = v; }
    return n > 63 ? 63 : n;
  }
};

__global__ __launch_bounds__(512, 2) void fused_kernel(const char* __restrict__ x, const char* __restrict__ wqkv, const char* __restrict__ wphi,
                                                       const char* __restrict__ wo, const char* __restrict__ wg, const char* __restrict__ wu,
                                                       const char* __restrict__ wd, char* __restrict__ out, char* ws) {
  const int tid = threadIdx.x, lane = tid & 63, w = __builtin_amdgcn_readfirstlane(tid >> 6);
  const int bx = blockIdx.x, xcd = bx & 7, r = bx >> 3;
  const int th = xcd & 1, m = (xcd >> 1) * 4 + (r & 3), t8 = r >> 2;
  const int g16 = (lane >> 4) & 1, q4 = (lane & 15) >> 2, p4 = lane & 3, l31 = lane & 31, hb = lane >> 5, rl = lane >> 3;
  STAMP(0); STAMPC(14); TK(0);
  const unsigned long long tstart = __builtin_amdgcn_s_memrealtime();
  unsigned cpf;
  {
    unsigned long long pc; asm volatile("s_getpc_b64 %0" : "=s"(pc));
    const unsigned line = r * 8 + w + (lane & 1) * 256;
    asm volatile("global_load_dword %0, %1, %2" : "=v"(cpf) : "v"(min(line, CODE_LINES) * 128), "s"(pc & ~127ull) : "memory");
  }
  unsigned st0, st1;
  const unsigned ldsw = w * 2048;
  const unsigned swz = (((lane & 7) ^ rmask(16 * w + rl)) << 4);
  const unsigned voffX = (16 * w + rl) * 896 + swz, voffX2 = voffX + 8 * 896 - 1024;
  const rsrc_t rsX = mkrs(x + (size_t)m * 128 * 896, 128 * 896);
#define ITEM_X(c, h) do { if (!(h)) DMA1I(voffX, rsX, ldsw, (7 + ((c) + 2) % 3) * SLOT, (c) * 128, st0); else DMA2(voffX2, rsX, st0); } while (0)
#define ITEM_V(c, h) do { if (!(h)) DMA1I(voffX, rsV, ldsw, (((c) + 7) % 10) * SLOT, HOTV(c) * (128 * 896), st0); else DMA2(voffX2, rsV, st0); } while (0)
  ITEM_X(0, 0); ITEM_X(0, 1);

  const bool ffn = t8 < 5, isq = t8 == 7;
  const int fi = th * 5 + t8, g = th * 2 + (t8 - 5);
  const int rb = w & 3, role = w >> 2;
  unsigned voffW, voffW2, wstr, wnext;
  rsrc_t rsW;
  if (ffn) {
    voffW = (16 * (w & 3) + rl) * 3584 + ((((((lane & 7) >> 1) ^ ((rl >> 1) & 1) ^ (role << 1)) << 1) | (lane & 1)) << 4);
    voffW2 = voffW + 8 * 3584 - 1024;
    rsW = mkrs((role ? wu : wg) + fi * 128, 448 * 3584 - fi * 128);
    wstr = 64 * 3584; wnext = 10 * 128;
  } else {
    const int c0 = 128 * w + lane, c1 = c0 + 64;
    const int r0 = c0 / 28, k0 = c0 - 28 * r0, r1 = c1 / 28, k1 = c1 - 28 * r1;
    voffW = r0 * 2688 + k0 * 16 + (k0 >= 14 ? 32 * 2688 - 224 : 0);
    voffW2 = r1 * 2688 + k1 * 16 + (k1 >= 14 ? 32 * 2688 - 224 : 0) - 1024;
    const int co = isq ? 448 * th : 896 + 224 * g;
    rsW = mkrs(wqkv + co, w < 7 ? 448 * 2688 - co : 0);
    wstr = 64 * 2688; wnext = isq ? 224 : 896;
  }
  DMA1I(voffW, rsW, ldsw, 0, 0, st1); DMA2(voffW2, rsW, st1);

  const rsrc_t rs = mkrs(ws, WS_BYTES);
  unsigned tag;
  const int lc = bx % 77, cpart = bx / 77;
  const int rt = xcd * 4 + (r & 3), ct = t8;
  const rsrc_t rsV = mkrs(wd + ct * 128, isq ? 0 : 1792 * 896 - ct * 128);
  const unsigned mlo = lane < 32 ? 0xffffffffu : 0u;
  const unsigned vp = w * 1024 + lane * 16, lp = w * 1024;

  const int rbg = m * 4 + rb;
  const int rb2 = w & 1, kq = w >> 1;
#define VFL (((rt * 2 + rb2) * 2 + (kq & 1)) * 128 + l31 * 4)
#define AB (((rt * 2 + rb2) * 144 + 2 * kq) * 1024 + lane * 16)
  u32x4 af[36];
#define LDA2(c) asm volatile("s_mov_b32 %2, %5\n\tbuffer_load_dwordx4 %0, %3, %4, %2 offen sc1\n\tbuffer_load_dwordx4 %1, %3, %4, %2 offen offset:1024 sc1" \
    : "=&v"(af[2 * (c)]), "=&v"(af[2 * (c) + 1]), "=&s"(st1) : "v"(AB), "s"(rs), "n"(WS_AFRAG + HOTA(c) * 8192) : "memory")
#define PIN4(c) "+v"(af[2 * (c)]), "+v"(af[2 * (c) + 1]), "+v"(af[2 * (c) + 2]), "+v"(af[2 * (c) + 3])
#define PIN8(c) "+v"(af[2 * (c)]), "+v"(af[2 * (c) + 1]), "+v"(af[2 * (c) + 2]), "+v"(af[2 * (c) + 3]), "+v"(af[2 * (c) + 4]), "+v"(af[2 * (c) + 5]), "+v"(af[2 * (c) + 6]), "+v"(af[2 * (c) + 7])
#define NEED(mask) do { if (!seen(fl, tag, mask)) poll(ws, WS_AFLAG + VFL, mask, tag); } while (0)
  f32x16 pa0, pa1;
  float k0, k1, ia2;
  unsigned fl = 0;

  const unsigned xrow = 32 * rb + l31;
  const unsigned vst = (m * 128 + xrow) * 16;
  s16x8 xf[28], wq[3][2], fb, wf[2][2];
  u32x4 stt, ca, cb;
  unsigned aW[4];
  auto fold = [&](u32x4 c) { const unsigned c2 = c[2] & mlo; u32x4 u = {c[0], c2, c[0], c2}; return asf(u); };
  auto make_fb = [&]() { const unsigned a = stt[0] & 0xffffu, b = stt[0] >> 16; u32x4 u = {a | (a << 16), a | (a << 16), b | (b << 16), b | (b << 16)}; fb = asf(u); };

  auto wfrag = [&](auto att, int n, int t) {
    const int sl = (n >> 2) % 7;
    if constexpr (decltype(att)::value) {
      const unsigned o = (sl & 3) * SLOT + (n & 1) * 7168 + ((n >> 1) & 1) * 224 + t * 56, b = aW[sl >> 2];
      return trfrag(b + o, b + o + 1792);
    } else {
      const unsigned o = (sl & 3) * SLOT + (n & 3) * 2048, b = aW[(sl >> 2) * 2 + t];
      return trfrag(b + o, b + o + 512);
    }
  };
  auto pass0 = [&](auto att, unsigned vca, unsigned vcb) {
    constexpr bool A = decltype(att)::value;
    constexpr unsigned WS_ = A ? 64 * 2688 : 64 * 3584;
#define ITEM_W(c, h) do { if (!(h)) DMA1I(voffW, rsW, ldsw, (c) * SLOT, (c) * WS_, st1); else DMA2(voffW2, rsW, st1); } while (0)
#define ITEM_N(c, h) do { if (!(h)) { st1 = (c) * WS_; DMA1A(voffW, rsW, ldsw, (c) * SLOT, st1, wnext); } else DMA2(voffW2, rsW, st1); } while (0)
    u32x4 px, pw[3];
    {
      asm volatile("buffer_load_dword %0, off, %1, 0 sc1" : "=v"(tag) : "s"(rs) : "memory");
      asm volatile("global_load_dwordx4 %0, %1, %2" : "=v"(px) : "v"(min(lane, 55) * 16), "s"(x + (size_t)(bx * 8 + w) * 896) : "memory");
      const char* cb = lc < 21 ? wqkv + lc * 128 : (lc < 49 ? wg + (lc - 21) * 128 : wu + (lc - 49) * 128);
      const unsigned pitch = lc < 21 ? 2688 : 3584;
      const unsigned vo = rl * pitch + (lane & 7) * 16;
#pragma unroll
      for (int i = 0; i < 3; i++) {
        const int row = min(152 * cpart + 8 * (w + 8 * i), 440);
        asm volatile("global_load_dwordx4 %0, %1, %2" : "=v"(pw[i]) : "v"(vo), "s"(cb + (size_t)row * pitch) : "memory");
      }
    }
    ITEM_X(1, 0); ITEM_X(1, 1);
    ITEM_W(1, 0); ITEM_W(1, 1);
    TK(1);
    unsigned aX[4];
#pragma unroll
    for (int ks = 0; ks < 4; ks++) { aX[ks] = 7 * SLOT + xrow * 128 + (((2 * ks + hb) ^ rmask(xrow)) << 4); OPAQUE(aX[ks]); }
    auto xread = [&](int n) { xf[n] = *(LAS s16x8*)(aX[n & 3] + (((n >> 2) + 2) % 3) * SLOT); };
    f32x16 acc0 = zero16(), acc1 = zero16();
    const unsigned m56 = lane < 56 ? 0xffffffffu : 0u;
    float cs[8], s = 0.f, q = 0.f; unsigned pfd;
    asm volatile("s_waitcnt vmcnt(9)" :: "v"(cpf) : "memory");
    BAR();
    TK(4);
    xread(0); wq[0][1] = wfrag(att, 0, 1); wq[0][0] = wfrag(att, 0, 0);
    xread(1); wq[1][1] = wfrag(att, 1, 1); wq[1][0] = wfrag(att, 1, 0);
    ITEM_X(2, 0); ITEM_X(2, 1);
    auto pre = [&](int p) {
      if (p == 4) { tag += 1; for (int i = 0; i < 4; i++) px[i] &= m56; }
      if (p == 5) { for (int i = 0; i < 4; i++) { s = dot2(px[i], 0x3f803f80u, s); q = dot2(px[i], px[i], q); } OPAQUE(s); OPAQUE(q); }
      if (p == 6) { s = add32(s, q); s += dpp<0xB1>(s); OPAQUE(s); }
      if (p == 7) { s += dpp<0x4E>(s); s += dpp<0x141>(s); OPAQUE(s); }
      if (p == 8) { s += dpp<0x128>(s); s = add16(s, s); OPAQUE(s); }
      if (p == 9) { bcast32(s * (1.f / 448.f), s, q); q = fmaxf(fmaf(-s, s, q), 0.f) + 1e-5f; OPAQUE(s); OPAQUE(q); }
      if (p == 10) {
        u32x4 o = {split2(-s), tag, __float_as_uint(__builtin_amdgcn_rsqf(q)), tag};
        st16(rs, lane == 0 ? WS_STAT + (bx * 8 + w) * 16 : 0xfffffff0u, o);
      }
      if (p >= 12 && p < 20) {
        const int i = (p - 12) >> 1;
        if (!(p & 1)) { pw[2][i] &= (w + 16 < (cpart == 2 ? 18 : 19) ? 0xffffffffu : 0u); cs[2 * i] = vadd(vadd(blo(pw[0][i]), blo(pw[1][i])), blo(pw[2][i])); }
        else cs[2 * i + 1] = vadd(vadd(bhi(pw[0][i]), bhi(pw[1][i])), bhi(pw[2][i]));
      }
      if (p == 20 || p == 21) for (int i = 2 * (p - 20); i < 2 * (p - 19); i++) { cs[i] = add32(cs[i], cs[i + 4]); OPAQUE(cs[i]); }
      if (p == 22) for (int i = 0; i < 2; i++) { cs[i] = add16(cs[i], cs[i + 2]); OPAQUE(cs[i]); }
      if (p == 23) for (int i = 0; i < 2; i++) { cs[i] += dpp<0x128>(cs[i]); OPAQUE(cs[i]); }
      if (p == 24) {
        *(LAS f32x2*)(LDS_MISC + w * 256 + ((lane & 7) * 8 + hb * 4 + g16 * 2) * 4) = f32x2{cs[0], cs[1]};
        if (A) *(LAS u32x2*)(LDS_PHI + 4 * 1568 + lane * 8) = u32x2{0u, 0u};
      }
    };
    sfor<0, 28>([&](auto nn) {
      constexpr int n = decltype(nn)::value;
      constexpr int c = (n + 2) >> 2, j = (n + 2) & 3;
      if (j == 0) {
        if (c == 1) asm volatile("s_waitcnt vmcnt(6)" : "+v"(tag), "+v"(px), "+v"(pw[0]), "+v"(pw[1]), "+v"(pw[2])::"memory");
        if (c == 2 || c == 3) WAITV(7); if (c == 4) WAITV(6); if (c == 5 || c == 6) WAITV(9); if (c == 7) asm volatile("s_waitcnt vmcnt(4)" :: "v"(pfd) : "memory");
        BAR();
        TK(4 + c);
      }
      if (A && n == 15) {
        const unsigned pa = LDS_PHI + 2 * role * 1568 + (4 * hb + q4) * 56 + g16 * 32 + p4 * 8;
        for (int u = 0; u < 2; u++) { wf[u][0] = trfrag(pa + u * 1568, pa + u * 1568 + 448); wf[u][1] = trfrag(pa + u * 1568 + 896, pa + u * 1568 + 1344); }
      }
      if (n == 14 && w == 7 && cpart < 3) {
        float t = 0.f;
        for (int i = 0; i < 8; i++) t += *(LAS float*)(LDS_MISC + i * 256 + lane * 4);
        u32x2 o = {split2(t), tag};
        __builtin_amdgcn_raw_buffer_store_b64(o, rs, WS_CS + (lc * 64 + lane) * 32 + cpart * 8, 0, 16);
      }
#define P0_ISSUE(h) do { \
      if (n < 2) ITEM_W(2 + n, h); \
      if (c >= 1 && c <= 4 && j == 0) ITEM_X(c + 2, h); \
      if (c >= 1 && c <= 3 && j == 2) ITEM_W(c + 3, h); \
      if (c == 4 && j >= 2) ITEM_N(j - 1, h); \
      if (c == 5 && j == 0) ITEM_N(0, h); \
      if (c == 5 && j == 2) ITEM_N(3, h); \
      if (c == 6 && j == 0) ITEM_N(4, h); } while (0)
      SB();
      if (n == 27) pa0 = MFMA(wq[n % 3][0], xf[n], acc0); else acc0 = MFMA(wq[n % 3][0], xf[n], acc0);
      SB();
      if (n + 2 < 28) xread(n + 2);
      wq[(n + 2) % 3][1] = wfrag(att, n + 2, 1);
      pre(2 * n);
      P0_ISSUE(0);
      if (n == 5) {
        const rsrc_t rsP = mkrs(wphi + (isq ? 8 * th : 4 * g) * 1568, A ? 4 * 1568 : 0);
        DMA1I(vp, rsP, lp, A ? LDS_PHI : LDS_BYTES, 0, st0);
      }
      if (n == 15) {
        const int u = bx * 8 + w;
        const rsrc_t rsF = u < 1568 ? mkrs(wd + u * 1024, 1024) : mkrs(wo + (u - 1568) * 1024, u < 1960 ? 1024 : 0);
        asm volatile("buffer_load_dword %0, %1, %2, 0 offen" : "=v"(pfd) : "v"((lane & 7) * 128), "s"(rsF) : "memory");
      }
      if (n == 26) ALD16(ca, vca, WS_CS);
      if (n == 27) ALD16(stt, vst, WS_STAT);
      SB();
      if (n == 27) pa1 = MFMA(wq[n % 3][1], xf[n], acc1); else acc1 = MFMA(wq[n % 3][1], xf[n], acc1);
      SB();
      wq[(n + 2) % 3][0] = wfrag(att, n + 2, 0);
      pre(2 * n + 1);
      P0_ISSUE(1);
      if (n == 26) ALD16(cb, vcb, WS_CS);
      SB();
    });
    wq[0][0] = wq[1][0]; wq[0][1] = wq[1][1]; wq[1][0] = wq[2][0]; wq[1][1] = wq[2][1];
    STAMP(1); TK(12);
  };

  if (ffn) {
    FRESH_LANE();
    aW[0] = g16 * 8192 + (8 * hb + q4) * 128 + (((2 * role) ^ ((q4 >> 1) & 1) ^ (g16 << 1)) * 32) + p4 * 8;
    aW[1] = aW[0] ^ 32; aW[2] = aW[0] + 65536; aW[3] = aW[1] + 65536;
    OPAQUE(aW[0]); OPAQUE(aW[1]); OPAQUE(aW[2]); OPAQUE(aW[3]);
    const unsigned vcs = (1344 + g16 * 1792 + 32 * role + (lane & 15)) * 32 + hb * 16;
    pass0(std::false_type{}, vcs + fi * 2048, vcs + fi * 2048 + 512);
    const int hl2 = 20 + th * 4 + t8;
    const unsigned hb0 = WS_AFRAG + (rbg * 144 + fi * 4 + 2 * role) * 1024 + lane * 16;
    const unsigned pf = lane == 0 ? WS_AFLAG + (rbg * 2 + role) * 128 + fi * 4 : 0xffffff00u;
    const bool two = t8 == 4;
    auto passn = [&](auto first) {
      constexpr bool F = decltype(first)::value, L = !F;
      const rsrc_t rsB = two ? rsV : rsW;
      unsigned voffB = voffW, voffB2 = voffW2;
      if (F && two) { voffB = voffX; voffB2 = voffX2; OPAQUE(voffB); }
      const unsigned sinc = two ? 128 * 896 : 64 * 3584;
      const unsigned ho = F ? hb0 : hb0 + 10 * 4096, vcp = vcs + (F ? fi : 10 + fi) * 2048, vcn = vcs + (F ? 10 + fi : hl2) * 2048;
      unsigned sA = (F ? 10 : hl2 - fi) * 128 + 4 * 64 * 3584, sB = two ? 2 * 128 * 896 : (hl2 - fi) * 128 - 64 * 3584;
      const unsigned c2 = two ? 0xf0000000u : hb0 + (hl2 - fi) * 4096;
      f32x16 acc0 = zero16(), acc1 = zero16();
      f32x16 qa0, qa1;
      float gt[2], gu[2], hprev; u32x4 hq;
      auto geglu = [&](int hs) {
#pragma unroll
        for (int e = 0; e < 16; e++) {
          const int d = hs - ((F ? 12 : 4) + 12 * e / 5);
          const float G = e < 8 ? qa0[e & 7] : qa1[e & 7], U = e < 8 ? qa0[(e & 7) + 8] : qa1[(e & 7) + 8];
          if (d == 0) gt[e & 1] = vmul(G, fmaf(G * G, k1, k0));
          if (d == 1) { gt[e & 1] = __builtin_amdgcn_exp2f(gt[e & 1]); gu[e & 1] = vmul(G, U); }
          if (d == 2) gt[e & 1] = __builtin_amdgcn_rcpf(fmaf(gt[e & 1], ia2, ia2));
          if (d == 3) {
            if (e & 1) hq[(e >> 1) & 3] = pk2(hprev, gu[1] * gt[1]); else hprev = vmul(gu[0], gt[0]);
            if ((e & 7) == 7) st16(rs, ho + (e >> 3) * 1024, hq);
          }
        }
      };
      sfor<0, 28>([&](auto nn) {
        constexpr int n = decltype(nn)::value;
        if (n == 10) { if (!F) { WAITV(12); st4(rs, pf, tag); } BAR(); TK(13); }
        if (n == 18) { asm volatile("s_waitcnt vmcnt(8)" : "+v"(ca), "+v"(cb)::"memory"); BAR(); TK(14); }
        if (n == 26) { if (F) WAITV(7); BAR(); TK(15); }
        if (n == (F ? 4 : 0)) {
          if (F) asm volatile("s_waitcnt vmcnt(5)" : "+v"(ca), "+v"(cb), "+v"(stt)::"memory");
          g_check<F>(rs, ca, cb, stt, vcp, vcp + 512, vst, tag, mlo);
        }
        SB();
        if (n == 27) pa0 = MFMA(wq[n % 3][0], xf[n], acc0); else acc0 = MFMA(wq[n % 3][0], xf[n], acc0);
        SB();
        if (!L || n + 2 < 28) wq[(n + 2) % 3][1] = wfrag(std::false_type{}, n + 2, 1);
        if (F && n == 4) make_fb();
        if (n == (F ? 5 : 1)) qa0 = MFMA(fold(ca), fb, pa0);
        if (n == 0 || n == 2) DMA1A(voffW, rsW, ldsw, (5 + n / 2) * SLOT, sA, wstr);
        if (n == 1 || n == 3) DMA2(voffW2, rsW, sA);
        if (L && n >= 4 && n < 16) ITEM_V((n - 4) / 2, n & 1);
        if (L && (n == 18 || n == 19)) ITEM_V(6, n & 1);
        if (L && (n == 21 || n == 22)) ITEM_V(7, !(n & 1));
        if (L && n >= 26) ITEM_V(n - 18, 0);
        if (!L && (n == 10 || n == 12 || n == 14)) DMA1A(voffB, rsB, ldsw, ((n - 10) / 2) * SLOT, sB, sinc);
        if (!L && (n == 18 || n == 21)) DMA1A(voffB, rsB, ldsw, (3 + (n - 18) / 3) * SLOT, sB, sinc);
        if (!L && (n == 11 || n == 13 || n == 15 || n == 19 || n == 22)) DMA2(voffB2, rsB, sB);
        geglu(2 * n);
        SB();
        if (n == 27) pa1 = MFMA(wq[n % 3][1], xf[n], acc1); else acc1 = MFMA(wq[n % 3][1], xf[n], acc1);
        SB();
        if (!L || n + 2 < 28) wq[(n + 2) % 3][0] = wfrag(std::false_type{}, n + 2, 0);
        if (F && n == 4) {
          const float rstd = __uint_as_float(stt[2]);
          const float c0 = -2.f * 0.7978845608028654f * 1.4426950408889634f;
          k0 = vmul(c0, rstd); k1 = vmul(c0 * 0.044715f * rstd * rstd, rstd); ia2 = __builtin_amdgcn_rcpf(rstd * rstd);
        }
        if (n == (F ? 5 : 1)) qa1 = MFMA(fold(cb), fb, pa1);
        if (F && (n == 0 || n == 17)) st16(rs, c2 + (n & 1) * 1024, u32x4{~0u, ~0u, ~0u, ~0u});
        if (!F && n == 20) ALD4(fl, VFL, WS_AFLAG);
        if (n == 8) ALD16(ca, vcn, WS_CS);
        if (n == 9) ALD16(cb, vcn + 512, WS_CS);
        if (L && n >= 26) ITEM_V(n - 18, 1);
        geglu(2 * n + 1);
        SB();
      });
      if (!L) { wq[0][0] = wq[1][0]; wq[0][1] = wq[1][1]; wq[1][0] = wq[2][0]; wq[1][1] = wq[2][1]; }
      STAMP(F ? 2 : 3); TK(16);
    };
    passn(std::true_type{});
    if (two) { ITEM_V(0, 0); ITEM_V(0, 1); ITEM_V(1, 0); ITEM_V(1, 1); ITEM_V(2, 0); ITEM_V(2, 1); ITEM_V(8, 0); ITEM_V(8, 1); ITEM_V(9, 0); ITEM_V(9, 1); WAITV(10); st4(rs, pf, tag); fl = ~tag; }
    int t8o = t8; asm volatile("" : "+s"(t8o));
    if (t8o != 4) passn(std::false_type{});
    {
      const unsigned vcn = vcs + (two ? 10 + fi : hl2) * 2048, hs = hb0 + (two ? 10 : hl2 - fi) * 4096;
      TK2(0);
      g_check<false>(rs, ca, cb, stt, vcn, vcn + 512, vst, tag, mlo);
      pa0 = MFMA(fold(ca), fb, pa0); pa1 = MFMA(fold(cb), fb, pa1);
      asm volatile("s_waitcnt vmcnt(7)" : "+v"(fl)::"memory");
      const bool early = seen(fl, tag, 0x3ffull);
      u32x4 o[2];
#pragma unroll
      for (int t = 0; t < 2; t++) {
        const f32x16& a = t ? pa1 : pa0;
        float hv[8];
        if (t == 0) { LDA2(0); LDA2(1); } else { TK2(1); WAITV(9); TK2(2); st4(rs, two ? 0xffffff00u : pf + 40, tag); }
#pragma unroll
        for (int i = 0; i < 8; i++) {
          const float e = __builtin_amdgcn_exp2f(a[i] * fmaf(a[i] * a[i], k1, k0));
          hv[i] = (a[i] * a[i + 8]) * __builtin_amdgcn_rcpf(fmaf(e, ia2, ia2));
        }
        o[t] = u32x4{pk2(hv[0], hv[1]), pk2(hv[2], hv[3]), pk2(hv[4], hv[5]), pk2(hv[6], hv[7])};
        OPAQUE(o[t]); SB();
      }
      st16(rs, hs, o[0]); st16(rs, hs + 1024, o[1]);
      TK2(3);
      asm volatile("s_waitcnt vmcnt(3)" : PIN4(0)::"memory");
      TK2(4);
      if (!early) {
        WAITV(0); st4(rs, two ? pf + 40 : 0xffffff00u, tag);
        poll(ws, WS_AFLAG + VFL, 0x3ffull, tag); LDA2(0); LDA2(1);
        asm volatile("s_waitcnt vmcnt(0)" : PIN4(0)::"memory");
      }
      if (two) BAR();
    }
  } else {
    aW[0] = (8 * hb + q4) * 448 + 112 * role + g16 * 32 + p4 * 8; aW[1] = aW[0] + 65536;
    OPAQUE(aW[0]); OPAQUE(aW[1]);
    const int head0 = (isq ? 8 * th : 4 * g) + 2 * role;
    const unsigned vcs = ((isq ? 0 : 448) + 28 * head0 + l31) * 32 + hb * 16;
    pass0(std::true_type{}, vcs, vcs + 28 * 32);
    float rstd, os;
    const float ninf = __uint_as_float(~mlo & 0xff800000u);
    u32x4 ph[2][4];
    Sm sm;
    {
      unsigned sA = wnext + 4 * 64 * 2688;
      const unsigned vca = vcs + wnext * 16;
      const unsigned oc = isq ? WS_AFRAG + (rbg * 144 + 112 + head0 * 2) * 1024 + lane * 16 : 0xf0000000u;
      f32x16 acc0 = zero16(), acc1 = zero16();
      f32x16 qa0, qa1;
      u32x4 kf[2][2];
      f32x16 p1;
      sfor<0, 28>([&](auto nn) {
        constexpr int n = decltype(nn)::value;
        if (n == 10) { BAR(); TK(13); }
        if (n == 18) {
          asm volatile("s_waitcnt vmcnt(2)" : "+v"(ca), "+v"(cb)::"memory"); BAR(); TK(14);
          st4(rs, tid == 0 ? WS_CFLAG + ((isq ? 4 + th : g) * 16 + m) * 128 : 0xf0000000u, tag);
        }
        if (n == 26) { BAR(); TK(15); }
        if (n == 6 || n == 11) {
          FRESH_LANE();
          const unsigned co = isq ? (lane < 16 && n == 6 ? WS_KVRED + ((2 * th + (w >> 2)) * 16 + m) * 1024 + (w & 3) * 256 + lane * 16 : 0xf0000000u)
                                  : WS_KVPART + ((g * 8 + w) * 16 + m) * 2048 + (n & 1) * 1024 + lane * 16;
          st16(rs, co, u32x4{~0u, ~0u, ~0u, ~0u});
        }
        if (n == 4) {
          asm volatile("s_waitcnt vmcnt(4)" : "+v"(ca), "+v"(cb), "+v"(stt)::"memory");
          g_check<true>(rs, ca, cb, stt, vcs, vcs + 28 * 32, vst, tag, mlo);
        }
        if (n == 19) g_check<false>(rs, ca, cb, stt, vca, vca + 28 * 32, vst, tag, mlo);
        SB();
        if (n == 27) pa0 = MFMA(wq[n % 3][0], xf[n], acc0); else acc0 = MFMA(wq[n % 3][0], xf[n], acc0);
        SB();
        if (n + 2 < 28) wq[(n + 2) % 3][1] = wfrag(std::true_type{}, n + 2, 1);
        if (n == 4) { make_fb(); rstd = __uint_as_float(stt[2]); os = isq ? 1.f : rstd; }
        if (n == 5) qa0 = MFMA(fold(ca), fb, pa0);
        if (n == 7 || n == 8) kf[n - 7][0] = pack8u(n == 8 ? qa1 : qa0, 0);
        if (n == 8) sm.p = MFMA(wf[0][0], asf(kf[0][0]), zero16());
        if (n == 18) p1 = MFMA(wf[1][0], asf(kf[1][0]), zero16());
        if (n == 10) sm_minmax(sm, ninf);
        if (n == 11) sm_consts(sm, rstd);
        if (n >= 12 && n < 20) sm_exp(sm, 2 * (n - 12), ninf);
        if (n == 20) sm_sum(sm, os);
        if (n >= 21 && n < 25) ph[0][n - 21] = sm_norm(sm, n - 21);
        if (n == 25) sm_consts(sm, rstd);
        if (n >= 26) sm_exp(sm, 2 * (n - 26), ninf);
        if (n == 0 || n == 2) DMA1A(voffW, rsW, ldsw, (5 + n / 2) * SLOT, sA, wstr);
        if (n == 1 || n == 3) DMA2(voffW2, rsW, sA);
        if (n >= 4 && n < 10) ITEM_V((n - 4) / 2, n & 1);
        if (n == 10) {
          const rsrc_t rsP = mkrs(wphi + (8 * th + 4) * 1568, isq ? 4 * 1568 : 0);
          FRESH_LANE();
          DMA1I(lp + lane * 16, rsP, lp, 0, 0, st0);
        }
        if (n == 20) acc0 = MFMA(fold(ca), fb, acc0);
        SB();
        if (n == 27) pa1 = MFMA(wq[n % 3][1], xf[n], acc1); else acc1 = MFMA(wq[n % 3][1], xf[n], acc1);
        SB();
        if (n + 2 < 28) wq[(n + 2) % 3][0] = wfrag(std::true_type{}, n + 2, 0);
        if (n == 5) qa1 = MFMA(fold(cb), fb, pa1);
        if (n >= 5 && n % 3 == 2) { constexpr int i = (n - 5) / 3; __builtin_amdgcn_raw_buffer_store_b128(u32x4{~0u, ~0u, ~0u, ~0u}, rs, oc, (i & 3) * 1024 + (i >> 2) * 8192, 16); }
        if (n == 8) ALD16(ca, vca, WS_CS);
        if (n == 9) ALD16(cb, vca + 28 * 32, WS_CS);
        if (n == 7 || n == 8) { kf[n - 7][1] = pack8u(n == 8 ? qa1 : qa0, 1); kf[n - 7][1][2] &= mlo; kf[n - 7][1][3] &= mlo; }
        if (n == 8) sm.p = MFMA(wf[0][1], asf(kf[0][1]), sm.p);
        if (n == 18) p1 = MFMA(wf[1][1], asf(kf[1][1]), p1);
        if (n >= 12 && n < 20) sm_exp(sm, 2 * (n - 12) + 1, ninf);
        if (n == 20) { sm.p = p1; sm_minmax(sm, ninf); }
        if (n >= 26) sm_exp(sm, 2 * (n - 26) + 1, ninf);
        if (n == 20) acc1 = MFMA(fold(cb), fb, acc1);
        SB();
      });
      for (int i = 4; i < 16; i++) sm_exp(sm, i, ninf);
      sm_sum(sm, os);
    }
    STAMP(2); TK(16);
    FRESH_LANE();
    const unsigned xrow = 32 * rb + l31;
    if (!isq) {
      const unsigned ir = split2(__builtin_amdgcn_rcpf(rstd)) & ~mlo;
      const unsigned wa = (2 * role * 128 + xrow) * 200 + hb * 8;
#pragma unroll
      for (int u = 0; u < 2; u++) {
        if (u) for (int f = 0; f < 4; f++) ph[1][f] = sm_norm(sm, f);
#pragma unroll
        for (int f = 0; f < 6; f++) {
          u32x4 v = f < 4 ? ph[u][f] : pack8u(u ? pa1 : pa0, f - 4);
          if (f == 5) { v[2] = (v[2] & mlo) | ir; v[3] &= mlo; }
          *(LAS u32x2*)(wa + u * 25600 + f * 32) = u32x2{v[0], v[1]}; *(LAS u32x2*)(wa + u * 25600 + f * 32 + 16) = u32x2{v[2], v[3]};
        }
      }
      BAR();
      TK(17);
      unsigned ra = (w >> 1) * 25600 + (8 * hb + q4) * 200 + g16 * 32 + p4 * 8, rp = ra + (w & 1) * 64;
      OPAQUE(ra); OPAQUE(rp);
      f32x16 kv = zero16();
#pragma unroll
      for (int s = 0; s < 8; s++) kv = MFMA(trfrag(rp + s * 3200, rp + s * 3200 + 800), trfrag(ra + 128 + s * 3200, ra + 128 + s * 3200 + 800), kv);
      ALD4(fl, VFL, WS_AFLAG);
      st16(rs, WS_KVPART + ((g * 8 + w) * 16 + m) * 2048 + lane * 16, pack8u(kv, 0));
      st16(rs, WS_KVPART + ((g * 8 + w) * 16 + m) * 2048 + 1024 + lane * 16, pack8u(kv, 1));
      STAMP(3); TK(18);
      BAR();
      asm volatile("s_waitcnt vmcnt(2)" : "+v"(fl)::"memory");
      NEED(0x3ffull);
      LDA2(0); LDA2(1);
      sfor<3, 10>([&](auto cc) { ITEM_V(decltype(cc)::value, 0); ITEM_V(decltype(cc)::value, 1); });
      asm volatile("s_waitcnt vmcnt(14)" : PIN4(0)::"memory");
      TK(19);
    } else {
      u32x4 ph2[2][4];
      const unsigned vcf = (lane < 32 ? 2 * th * 16 + lane : (4 + th) * 16 + (lane & 15)) * 128;
      ALD4(fl, vcf, WS_CFLAG);
      for (int f = 0; f < 4; f++) ph[1][f] = sm_norm(sm, f);
      {
        const unsigned pa = 2 * role * 1568 + (4 * hb + q4) * 56 + g16 * 32 + p4 * 8;
#pragma unroll
        for (int u = 0; u < 2; u++) {
          u32x4 k1 = pack8u(u ? pa1 : pa0, 1); k1[2] &= mlo; k1[3] &= mlo;
          sm.p = MFMA(trfrag(pa + u * 1568, pa + u * 1568 + 448), asf(pack8u(u ? pa1 : pa0, 0)), zero16());
          sm.p = MFMA(trfrag(pa + u * 1568 + 896, pa + u * 1568 + 1344), asf(k1), sm.p);
          sm_minmax(sm, ninf); sm_consts(sm, rstd);
          for (int i = 0; i < 16; i++) sm_exp(sm, i, ninf);
          sm_sum(sm, 1.f);
          for (int f = 0; f < 4; f++) { ph2[u][f] = sm_norm(sm, f); OPAQUE(ph2[u][f]); }
        }
      }
      STAMP(3); TK(17);
      asm volatile("s_waitcnt vmcnt(0)" : "+v"(fl)::"memory");
      st4(rs, l31 == 0 ? WS_AFLAG + (rbg * 2 + hb) * 128 + (28 + th * 2 + role) * 4 : 0xf0000000u, tag);
      if (!seen(fl, tag, ~0ull)) poll(ws, WS_CFLAG + vcf, ~0ull, tag);
      {
        const int gg = 2 * th + (w >> 2);
        const unsigned pb = WS_KVPART + ((gg * 8 + (m >> 1)) * 16 + (lane >> 4)) * 2048 + (m & 1) * 1024 + ((w & 3) * 16 + (lane & 15)) * 16;
        u32x4 pv[4];
#pragma unroll 1
        for (int it = 0; it < SPIN_CAP; ++it) {
          unsigned mx = 0;
#pragma unroll
          for (int i = 0; i < 4; i++) pv[i] = ld16(rs, pb + i * 8192);
#pragma unroll
          for (int i = 0; i < 4; i++) mx = umax4(mx, pv[i]);
          if (fresh(mx)) break;
          __builtin_amdgcn_s_sleep(1);
        }
        STAMP(5); TK(18);
        float t[8];
#pragma unroll
        for (int j = 0; j < 4; j++) {
          t[2 * j] = (blo(pv[0][j]) + blo(pv[1][j])) + (blo(pv[2][j]) + blo(pv[3][j]));
          t[2 * j + 1] = (bhi(pv[0][j]) + bhi(pv[1][j])) + (bhi(pv[2][j]) + bhi(pv[3][j]));
        }
        for (int j = 0; j < 8; j++) { t[j] = add32(t[j], t[j]); t[j] = add16(t[j], t[j]); }
        st16(rs, lane < 16 ? WS_KVRED + (gg * 16 + m) * 1024 + ((w & 3) * 16 + lane) * 16 : 0xf0000000u, u32x4{pk2(t[0], t[1]), pk2(t[2], t[3]), pk2(t[4], t[5]), pk2(t[6], t[7])});
        STAMP(6); TK(19);
      }
      {
        u32x4 gv[4];
        __builtin_amdgcn_s_sleep(12);
#pragma unroll 1
        for (int it = 0; it < SPIN_CAP; ++it) {
          unsigned mx = 0;
#pragma unroll
          for (int i = 0; i < 4; i++) gv[i] = ld16(rs, WS_KVRED + ((8 * th + w) * 4 + i) * 1024 + lane * 16);
#pragma unroll
          for (int i = 0; i < 4; i++) mx = umax4(mx, gv[i]);
          if (fresh(mx)) break;
          __builtin_amdgcn_s_sleep(4);
        }
#pragma unroll
        for (int i = 0; i < 4; i++) *(LAS u32x4*)(SLOT + (w * 4 + i) * 1024 + lane * 16) = gv[i];
      }
      BAR();
      STAMP(7); TK(20);
      u32x4 kf[4][4];
#pragma unroll
      for (int u = 0; u < 4; u++)
#pragma unroll
        for (int i = 0; i < 4; i++) kf[u][i] = *(LAS u32x4*)(SLOT + ((2 * role + (u & 1) + 4 * (u >> 1)) * 4 + i) * 1024 + lane * 16);
#pragma unroll
      for (int u = 0; u < 4; u++) {
        f32x16 nm = zero16();
        for (int i = 0; i < 4; i++) nm = MFMA(asf(kf[u][i]), asf(u < 2 ? ph[u & 1][i] : ph2[u & 1][i]), nm);
        float dl, dh; bcast32(nm[12] + nm[13], dl, dh);
        const float rd = __builtin_amdgcn_rcpf(dh + 1e-6f);
        for (int i = 0; i < 16; i++) nm[i] *= rd;
        u32x4 o1 = pack8u(nm, 1); o1[2] &= mlo; o1[3] &= mlo;
        const unsigned oo = WS_AFRAG + (rbg * 144 + 112 + (head0 + (u & 1) + 4 * (u >> 1)) * 2) * 1024 + lane * 16;
        st16(rs, oo, pack8u(nm, 0));
        st16(rs, oo + 1024, o1);
      }
      STAMP(4); TK(21); DUMPTK();
      if (bx == 7 * 4 * 8 && w == 0) {
#pragma unroll 1
        for (int i = 0; i < 8; i++) poll(ws, WS_AFLAG + (2 * i + hb) * 8 * 128 + l31 * 4, 0x500003ff500003ffull, tag);
        poll(ws, WS_CFLAG + lane * 128, ~0ull, tag);
        st4(rs, WS_EPOCH, tag);
      }
      return;
    }
  }

  {
    FRESH_LANE();
    const int tid = w * 64 + lane;
    const unsigned swz = (((lane & 7) ^ rmask(16 * w + rl)) << 4);
    STAMP(5);
    LDA2(2); LDA2(3); LDA2(4);
    const unsigned r0 = 32 * kq + 4 * hb + q4;
    unsigned bV[3][4];
#pragma unroll
    for (int i = 0; i < 12; i++) { bV[i >> 2][i & 3] = ((r0 * 128 + (((2 * g16 + (p4 >> 1)) ^ rmask(r0)) << 4) + (p4 & 1) * 8) ^ ((i & 1) * 64) ^ ((i & 2) * 16)) + (i >> 2) * 65536; OPAQUE(bV[i >> 2][i & 3]); }
    const unsigned voffO = (28 * (w >> 1) + 16 * (w & 1) + rl) * 896 + swz;
    const unsigned voffO2 = voffO + (((w & 1) && rl >= 4) ? 0 : 8 * 896) - 1024;
    const rsrc_t rsO = mkrs(wo + ct * 128, 448 * 896 - ct * 128);
    const size_t go = (size_t)(rt * 64 + (tid >> 3)) * 896 + ct * 128 + (tid & 7) * 16;
    u32x4 xr;
    f32x16 acc0 = zero16(), acc1 = zero16();
    s16x8 wv[2][4];
    auto vread = [&](int c) {
      const int sl = (c + 7) % 10; const unsigned so = (sl & 3) * SLOT;
      for (int i = 0; i < 4; i++) wv[c & 1][i] = trfrag(bV[sl >> 2][i] + so + (i >> 1) * 2048, bV[sl >> 2][i] + so + (i >> 1) * 2048 + 1024);
    };
    vread(0);
    TK(20);
    sfor<0, 18>([&](auto cc) {
      constexpr int c = decltype(cc)::value;
      asm volatile("s_waitcnt vmcnt(%3)" : "+v"(af[2 * c]), "+v"(af[2 * c + 1]), "+v"(fl) : "n"(P2::wait(c)) : "memory");
      if constexpr (c >= 10) {
        constexpr int c0 = c < 14 ? 10 : 14;
#pragma unroll 1
        for (int it = 0; it < SPIN_CAP; ++it) {
          if (__builtin_expect(fresh(umax4(umax4(0u, af[2 * c]), af[2 * c + 1])), 1)) break;
          WAITV(0);
          __builtin_amdgcn_s_sleep(4);
          LDA2(c); if (c + 1 < c0 + 4) LDA2(c0 + 3); if (c + 2 < c0 + 4) LDA2(c0 + 2); if (c + 3 < c0 + 4) LDA2(c0 + 1);
          asm volatile("s_waitcnt vmcnt(0)" : PIN8(c0)::"memory");
        }
        if (c == 10) { STAMP(7); TK(21); } if (c == 14) { STAMP(12); TK(22); }
      }
      if (P2::bar(c)) BAR();
      TK2(5 + c);
      if (c + 1 < 18) vread(c + 1);
      SB();
      acc0 = MFMA(wv[c & 1][0], asf(af[2 * c]), acc0); acc1 = MFMA(wv[c & 1][1], asf(af[2 * c]), acc1);
      if (c == P2::FL1 || c == P2::FL2) ALD4(fl, VFL, WS_AFLAG);
      constexpr int j = P2::vj(c);
      if (j >= 0 && j < 4) { ITEM_V(j + 10, 0); ITEM_V(j + 10, 1); }
      if (j >= 4) { DMA1I(voffO, rsO, ldsw, ((j + 7) % 10) * SLOT, (j - 4) * (112 * 896), st0); DMA2(voffO2, rsO, st0); }
      SB();
      acc0 = MFMA(wv[c & 1][2], asf(af[2 * c + 1]), acc0); acc1 = MFMA(wv[c & 1][3], asf(af[2 * c + 1]), acc1);
      if (c == P2::N1) { NEED(0xffc00ull); STAMP(6); }
      if (c == P2::N2) NEED(0xf0000000ull);
      if constexpr (P2::a0(c) < P2::a0(c + 1)) LDA2(P2::a0(c));
      if constexpr (P2::a0(c) + 1 < P2::a0(c + 1)) LDA2(P2::a0(c) + 1);
      if constexpr (P2::a0(c) + 2 < P2::a0(c + 1)) LDA2(P2::a0(c) + 2);
      if constexpr (P2::a0(c) + 3 < P2::a0(c + 1)) LDA2(P2::a0(c) + 3);
      static_assert(P2::a0(c + 1) - P2::a0(c) <= 4, "schedule");
      if (c == 13) asm volatile("global_load_dwordx4 %0, %1, off" : "=v"(xr) : "v"(x + go) : "memory");
      SB();
    });
    STAMP(8);
    const unsigned wa = 5 * SLOT + (kq * 64 + rb2 * 32 + l31) * 272 + hb * 16;
#pragma unroll
    for (int gq = 0; gq < 4; gq++) {
      f32x4 v0 = {acc0[4 * gq], acc0[4 * gq + 1], acc0[4 * gq + 2], acc0[4 * gq + 3]};
      f32x4 v1 = {acc1[4 * gq], acc1[4 * gq + 1], acc1[4 * gq + 2], acc1[4 * gq + 3]};
      *(LAS f32x4*)(wa + gq * 32) = v0; *(LAS f32x4*)(wa + 128 + gq * 32) = v1;
    }
    BAR();
    const unsigned ra = 5 * SLOT + (tid >> 3) * 272 + (tid & 7) * 32;
    f32x4 s0 = *(LAS f32x4*)ra, s1 = *(LAS f32x4*)(ra + 16);
#pragma unroll
    for (int k = 1; k < 4; k++) { s0 += *(LAS f32x4*)(ra + k * 64 * 272); s1 += *(LAS f32x4*)(ra + k * 64 * 272 + 16); }
    asm volatile("s_waitcnt vmcnt(0)" : "+v"(xr)::"memory");
    u32x4 o;
    o[0] = pk2(s0[0] + blo(xr[0]), s0[1] + bhi(xr[0]));
    o[1] = pk2(s0[2] + blo(xr[1]), s0[3] + bhi(xr[1]));
    o[2] = pk2(s1[0] + blo(xr[2]), s1[1] + bhi(xr[2]));
    o[3] = pk2(s1[2] + blo(xr[3]), s1[3] + bhi(xr[3]));
    __builtin_amdgcn_raw_buffer_store_b128(o, mkrs(out, 2048 * 896), (unsigned)go, 0, 16);
    while (__builtin_amdgcn_s_memrealtime() - tstart < 5000) __builtin_amdgcn_s_sleep(1);
    STAMP(9); STAMPC(15); TK(23); TK2(23); DUMPTK();
  }
}

extern "C" void kernel_launch(void* const* d_in, const int* in_sizes, int n_in, void* d_out, int out_size, void* d_ws, size_t ws_size, hipStream_t stream) {
  (void)hipFuncSetAttribute((const void*)fused_kernel, hipFuncAttributeMaxDynamicSharedMemorySize, LDS_BYTES);
  fused_kernel<<<256, 512, LDS_BYTES, stream>>>((const char*)d_in[0], (const char*)d_in[1], (const char*)d_in[2], (const char*)d_in[3],
                                                (const char*)d_in[5], (const char*)d_in[6], (const char*)d_in[7], (char*)d_out, (char*)d_ws);
}
```

```cpp
#include <hip/hip_runtime.h>
#include <hip/hip_bf16.h>
#include <stdint.h>
#include <type_traits>
#pragma clang diagnostic ignored "-Wint-to-pointer-cast"
#pragma clang diagnostic ignored "-Warray-bounds"

#define DI __device__ __forceinline__
#define LAS __attribute__((address_space(3)))
#define GAS __attribute__((address_space(1)))
typedef __attribute__((ext_vector_type(8))) short s16x8;
typedef __attribute__((ext_vector_type(4))) short s16x4;
typedef __attribute__((ext_vector_type(16))) float f32x16;
typedef __attribute__((ext_vector_type(4))) float f32x4;
typedef __attribute__((ext_vector_type(2))) float f32x2;
typedef __attribute__((ext_vector_type(4))) unsigned u32x4;
typedef __attribute__((ext_vector_type(2))) unsigned u32x2;
typedef __attribute__((ext_vector_type(2))) __bf16 bf16x2;
typedef GAS unsigned gu32;
typedef __amdgpu_buffer_rsrc_t rsrc_t;
#define RLX_AGENT __ATOMIC_RELAXED, __HIP_MEMORY_SCOPE_AGENT

constexpr unsigned WS_EPOCH = 0;
constexpr unsigned WS_CFLAG = 1024;
constexpr unsigned WS_AFLAG = 16384;
constexpr unsigned WS_STAT = WS_AFLAG + 128 * 128;
constexpr unsigned WS_CS = WS_STAT + 2048 * 16;
constexpr unsigned WS_KVRED = 262144;
constexpr unsigned WS_KVPART = WS_KVRED + 131072;
constexpr unsigned WS_AFRAG = WS_KVPART + (2u << 20);
constexpr unsigned WS_STAMP = WS_AFRAG + 64u * 144u * 1024u;
constexpr unsigned WS_BYTES = WS_STAMP;
static_assert(WS_CFLAG + 96 * 128 <= WS_AFLAG && WS_CS + 4928 * 32 <= WS_KVRED, "ws map");

constexpr int LDS_BYTES = 163840;
constexpr unsigned SLOT = 16384;
constexpr unsigned LDS_MISC = 0, LDS_PHI = 2048;
constexpr int SPIN_CAP = 1 << 14;

#define HOTA(c) (c)
#define HOTV(c) (c)
#define MFMA(a, b, c) __builtin_amdgcn_mfma_f32_32x32x16_bf16((a), (b), (c), 0, 0, 0)
#define WAITV(n) asm volatile("s_waitcnt vmcnt(" #n ")" ::: "memory")
#define BAR() do { asm volatile("s_waitcnt lgkmcnt(0)" ::: "memory"); __builtin_amdgcn_s_barrier(); asm volatile("" ::: "memory"); } while (0)
#define SB() __builtin_amdgcn_sched_barrier(0)
#define OPAQUE(v) asm volatile("" : "+v"(v))
template <int N> DI void waitv() { asm volatile("s_waitcnt vmcnt(%0)" ::"n"(N) : "memory"); }
template <int N> using IC = std::integral_constant<int, N>;
template <int I, int N, class F> DI void sfor(F&& f) { if constexpr (I < N) { f(IC<I>{}); sfor<I + 1, N>(f); } }

#define STAMP(i)
#define STAMPC(i)
#define TK(i)
#define TK2(i)
#define DUMPTK()

#define DMA1I(voff, rsrc, ldsb, LIMM, SOFF, tmp) asm volatile("s_add_u32 m0, %3, %4\n\ts_mov_b32 %0, %5\n\tbuffer_load_dwordx4 %1, %2, %0 offen lds" \
    : "=&s"(tmp) : "v"(voff), "s"(rsrc), "s"(ldsb), "n"(LIMM), "n"(SOFF) : "memory", "scc")
#define DMA1P(voff, rsrc, ldsb, LIMM, sa, sb, tmp) asm volatile("s_add_u32 m0, %3, %4\n\ts_add_u32 %0, %5, %6\n\tbuffer_load_dwordx4 %1, %2, %0 offen lds" \
    : "=&s"(tmp) : "v"(voff), "s"(rsrc), "s"(ldsb), "n"(LIMM), "s"(sa), "s"(sb) : "memory", "scc")
#define DMA2(voff, rsrc, soff) asm volatile("buffer_load_dwordx4 %0, %1, %2 offen offset:1024 lds" :: "v"(voff), "s"(rsrc), "s"(soff) : "memory")
#define ALD16(dst, voff, soff) asm volatile("buffer_load_dwordx4 %0, %1, %2, %3 offen sc1" : "=v"(dst) : "v"(voff), "s"(rs), "s"(soff) : "memory")
#define ALD4(dst, voff, soff) asm volatile("buffer_load_dword %0, %1, %2, %3 offen sc1" : "=v"(dst) : "v"(voff), "s"(rs), "s"(soff) : "memory")

#define FRESH_LANE() int lane; asm volatile("v_mbcnt_lo_u32_b32 %0, -1, 0\n\tv_mbcnt_hi_u32_b32 %0, -1, %0" : "=v"(lane)); \
  const int g16 = (lane >> 4) & 1, q4 = (lane & 15) >> 2, p4 = lane & 3, l31 = lane & 31, hb = lane >> 5, rl = lane >> 3; \
  (void)g16; (void)q4; (void)p4; (void)l31; (void)hb; (void)rl
DI rsrc_t mkrs(const void* p, unsigned bytes) { return __builtin_amdgcn_make_buffer_rsrc((void*)p, 0, bytes, 0x00020000); }
DI s16x8 trfrag(unsigned a0, unsigned a1) {
  s16x4 lo = __builtin_amdgcn_ds_read_tr16_b64_v4i16((LAS s16x4*)a0);
  s16x4 hi = __builtin_amdgcn_ds_read_tr16_b64_v4i16((LAS s16x4*)a1);
  return __builtin_shufflevector(lo, hi, 0, 1, 2, 3, 4, 5, 6, 7);
}
DI unsigned pk2(float a, float b) { f32x2 p = {a, b}; return __builtin_bit_cast(unsigned, __builtin_convertvector(p, bf16x2)); }
DI float dot2(unsigned a, unsigned b, float c) { return __builtin_amdgcn_fdot2_f32_bf16(__builtin_bit_cast(bf16x2, a), __builtin_bit_cast(bf16x2, b), c, false); }
DI float blo(unsigned u) { return __uint_as_float(u << 16); }
DI float bhi(unsigned u) { return __uint_as_float(u & 0xffff0000u); }
DI float fand(float v, unsigned m) { return __uint_as_float(__float_as_uint(v) & m); }
DI float vmul(float a, float b) { float r = a * b; OPAQUE(r); return r; }
DI float vadd(float a, float b) { float r = a + b; OPAQUE(r); return r; }
DI float max3(float a, float b, float c) { return __builtin_elementwise_maximum(__builtin_elementwise_maximum(a, b), c); }
DI float min3(float a, float b, float c) { return __builtin_elementwise_minimum(__builtin_elementwise_minimum(a, b), c); }
DI float add32(float a, float b) { auto r = __builtin_amdgcn_permlane32_swap(__float_as_uint(a), __float_as_uint(b), false, false); return __uint_as_float(r[0]) + __uint_as_float(r[1]); }
DI float add16(float a, float b) { auto r = __builtin_amdgcn_permlane16_swap(__float_as_uint(a), __float_as_uint(b), false, false); return __uint_as_float(r[0]) + __uint_as_float(r[1]); }
DI void bcast32(float v, float& lo, float& hi) { auto r = __builtin_amdgcn_permlane32_swap(__float_as_uint(v), __float_as_uint(v), false, false); lo = __uint_as_float(r[0]); hi = __uint_as_float(r[1]); }
template <int CTRL> DI float dpp(float x) { return __builtin_bit_cast(float, __builtin_amdgcn_mov_dpp(__builtin_bit_cast(int, x), CTRL, 0xf, 0xf, true)); }
DI float wave_sum(float v) {
  v += dpp<0xB1>(v); v += dpp<0x4E>(v); v += dpp<0x141>(v); v += dpp<0x128>(v);
  v = add16(v, v); return add32(v, v);
}
DI unsigned split2(float v) {
  const unsigned h = pk2(v, 0.f);
  return pk2(__uint_as_float(h << 16), v - __uint_as_float(h << 16));
}
DI u32x4 pack8u(const f32x16& x, int s) {
  u32x4 u = {pk2(x[8 * s], x[8 * s + 1]), pk2(x[8 * s + 2], x[8 * s + 3]), pk2(x[8 * s + 4], x[8 * s + 5]), pk2(x[8 * s + 6], x[8 * s + 7])};
  return u;
}
DI s16x8 asf(u32x4 u) { return __builtin_bit_cast(s16x8, u); }
DI unsigned rmask(unsigned row) { return (((row >> 1) & 1) << 2) | ((row >> 2) & 1) | ((row >> 3) & 2); }
DI f32x16 zero16() { f32x16 z; for (int i = 0; i < 16; i++) z[i] = 0.f; return z; }
DI void st16(rsrc_t rs, unsigned off, u32x4 v) { __builtin_amdgcn_raw_buffer_store_b128(v, rs, off, 0, 16); }
DI void st4(rsrc_t rs, unsigned off, unsigned v) { __builtin_amdgcn_raw_buffer_store_b32(v, rs, off, 0, 16); }
DI u32x4 ld16(rsrc_t rs, unsigned off) { return __builtin_amdgcn_raw_buffer_load_b128(rs, off, 0, 16); }
DI bool seen(unsigned v, unsigned tag, unsigned long long need) { return (__builtin_amdgcn_ballot_w64(v == tag) & need) == need; }
DI void poll(char* ws, unsigned off, unsigned long long need, unsigned tag) {
#pragma unroll 1
  for (int it = 0; it < SPIN_CAP; ++it) {
    const unsigned v = __hip_atomic_load((gu32*)(ws + off), RLX_AGENT);
    if (seen(v, tag, need)) break;
    __builtin_amdgcn_s_sleep(2);
  }
  __builtin_amdgcn_fence(__ATOMIC_ACQUIRE, "wavefront");
}
template <bool ST> DI void g_check(rsrc_t rs, u32x4& a, u32x4& b, u32x4& st, unsigned va, unsigned vb, unsigned vs, unsigned tag, unsigned mlo) {
#pragma unroll 1
  for (int it = 0; it < SPIN_CAP; ++it) {
    unsigned bad = (a[1] ^ tag) | (b[1] ^ tag) | (((a[3] ^ tag) | (b[3] ^ tag)) & mlo);
    if (ST) bad |= (st[1] ^ tag) | (st[3] ^ tag);
    if (__builtin_expect(__builtin_amdgcn_ballot_w64(bad != 0) == 0ull, 1)) break;
    ALD16(a, va, WS_CS); ALD16(b, vb, WS_CS); if (ST) ALD16(st, vs, WS_STAT);
    if (ST) asm volatile("s_waitcnt vmcnt(0)" : "+v"(a), "+v"(b), "+v"(st)::"memory"); else asm volatile("s_waitcnt vmcnt(0)" : "+v"(a), "+v"(b)::"memory");
  }
}

DI unsigned umax4(unsigned m, const u32x4& v) { return max(max(max(m, v[0]), v[1]), max(v[2], v[3])); }
DI bool fresh(unsigned m) { return __builtin_amdgcn_ballot_w64(m == 0xffffffffu) == 0ull; }

struct Sm { float L, cx, cn, mx, mn, s1, s2; f32x16 p, ep, en; };
DI void sm_max(Sm& s, float ninf) {
  const f32x16& p = s.p;
  float a = max3(p[0], p[1], p[2]);
  for (int i = 3; i < 11; i += 2) a = max3(a, p[i], p[i + 1]);
  s.mx = max3(a, p[11], max3(max3(p[12], p[13], p[14]), p[15], p[15]) + ninf);
}
DI void sm_min(Sm& s, float ninf) {
  const f32x16& p = s.p;
  float b = min3(p[0], p[1], p[2]);
  for (int i = 3; i < 11; i += 2) b = min3(b, p[i], p[i + 1]);
  s.mn = min3(b, p[11], min3(min3(p[12], p[13], p[14]), p[15], p[15]) - ninf);
}
DI void sm_consts(Sm& s, float sc) {
  auto r = __builtin_amdgcn_permlane32_swap(__float_as_uint(s.mx), __float_as_uint(-s.mn), false, false);
  const float m = max3(__uint_as_float(r[0]), __uint_as_float(r[1]), __uint_as_float(r[1]));
  float mx, nmn; bcast32(m, mx, nmn);
  s.L = sc * 1.4426950408889634f; s.cx = -mx * s.L; s.cn = -nmn * s.L; s.s1 = 0.f; s.s2 = 0.f;
}
DI void sm_exp(Sm& s, int i, float ninf) {
  if (i == 12) { s.cx += ninf; s.cn += ninf; }
  s.ep[i] = __builtin_amdgcn_exp2f(fmaf(s.p[i], s.L, s.cx)); s.en[i] = __builtin_amdgcn_exp2f(fmaf(s.p[i], -s.L, s.cn));
  if (i > 0) { s.s1 = vadd(s.s1, s.ep[i - 1]); s.s2 = vadd(s.s2, s.en[i - 1]); }
}
DI void sm_sum(Sm& s, float os) {
  const float t = add32(vadd(s.s1, s.ep[15]), vadd(s.s2, s.en[15]));
  bcast32(os * __builtin_amdgcn_rcpf(t), s.s1, s.s2);
}
DI void sm_norm2(const Sm& s, int f, int h, u32x4& u) {
  const f32x16& e = f < 2 ? s.ep : s.en; const float r = f < 2 ? s.s1 : s.s2; const int o = 8 * (f & 1);
  for (int i = 2 * h; i < 2 * h + 2; i++) u[i] = pk2(vmul(e[o + 2 * i], r), vmul(e[o + 2 * i + 1], r));
}
DI u32x4 sm_norm(const Sm& s, int f) {
  const f32x16& e = f < 2 ? s.ep : s.en; const float r = f < 2 ? s.s1 : s.s2; const int o = 8 * (f & 1);
  u32x4 u;
  for (int i = 0; i < 4; i++) u[i] = pk2(vmul(e[o + 2 * i], r), vmul(e[o + 2 * i + 1], r));
  return u;
}

struct P2 {
  static constexpr int a0(int s) { return s <= 3 ? 7 + s : s <= 7 ? 6 + s : s + 5 < 18 ? (s == 8 ? 14 : s + 5) : 18; }
  static constexpr int vs(int j) { return j + 1; }
  static constexpr int vj(int s) { for (int j = 0; j < 8; j++) if (vs(j) == s) return j; return -1; }
  static constexpr int FL2 = 6, N2 = 9;
  static constexpr bool bar(int c) { return c == 1 || c == 3 || c == 5 || c == 7 || c == 12; }
  static constexpr int vneed(int c) { return c == 1 ? 3 : c == 3 ? 5 : c == 5 ? 9 : c == 7 ? 12 : c == 12 ? 17 : -1; }
  static constexpr int na(int s) { return 2 * (a0(s + 1) - a0(s)); }
  static constexpr int nops(int s) { return (vj(s) >= 0 ? 2 : 0) + na(s) + (s == 13 || s == FL2 ? 1 : 0); }
  static constexpr int upto(int from, int c) { int n = 0; for (int s = from; s < c; s++) n += nops(s); return n; }
  static constexpr int wait(int c) {
    int n = 63;
    if (c >= 4 && c < 7) n = 2 * (6 - c) + upto(0, c);
    for (int s = 0; s < c; s++) if (a0(s) <= c && c < a0(s + 1)) n = 2 * (a0(s + 1) - 1 - c) + upto(s + 1, c);
    if (c == N2) { const int f = upto(FL2, c) - 1; if (f < n) n = f; }
    if (vneed(c) >= 10) { const int s = vs(vneed(c) - 10), v = na(s) + upto(s + 1, c); if (v < n) n = v; }
    else if (vneed(c) >= 0) { const int v = 2 * (9 - vneed(c)) + 6 + upto(0, c); if (v < n) n = v; }
    return n > 63 ? 63 : n;
  }
};

__global__ __launch_bounds__(512, 2) void fused_kernel(const char* __restrict__ x, const char* __restrict__ wqkv, const char* __restrict__ wphi,
                                                       const char* __restrict__ wo, const char* __restrict__ wg, const char* __restrict__ wu,
                                                       const char* __restrict__ wd, char* __restrict__ out, char* ws) {
  const int tid = threadIdx.x, lane = tid & 63, w = __builtin_amdgcn_readfirstlane(tid >> 6);
  const int bx = blockIdx.x, xcd = bx & 7, r = bx >> 3;
  const int tg = xcd & 3, th = tg >> 1, m = (xcd >> 2) * 8 + (r & 7), ts = r >> 3;
  const int g16 = (lane >> 4) & 1, q4 = (lane & 15) >> 2, p4 = lane & 3, l31 = lane & 31, hb = lane >> 5, rl = lane >> 3;
  STAMP(0); STAMPC(14); TK(0);
  unsigned st0, st1;
  const unsigned ldsw = w * 2048;
  const unsigned swz = (((lane & 7) ^ rmask(16 * w + rl)) << 4);
  const unsigned voffX = (16 * w + rl) * 896 + swz, voffX2 = voffX + 8 * 896 - 1024;
  const rsrc_t rsX = mkrs(x + (size_t)m * 128 * 896, 128 * 896);
  const int rot = ((m + ts) & 1) * 4;
  unsigned rx[7], rw[7];
#pragma unroll
  for (int c = 0; c < 7; c++) rx[c] = (c + rot >= 7 ? c + rot - 7 : c + rot) * 128;
#define ITEM_X(c, h) do { if (!(h)) DMA1P(voffX, rsX, ldsw, (7 + ((c) + 2) % 3) * SLOT, rx[c], 0, st0); else DMA2(voffX2, rsX, st0); } while (0)
#define ITEM_V(c, h) do { if (!(h)) DMA1I(voffX, rsV, ldsw, (((c) + 7) % 10) * SLOT, HOTV(c) * (128 * 896), st0); else DMA2(voffX2, rsV, st0); } while (0)
  ITEM_X(0, 0); ITEM_X(0, 1);

  const bool isq = ts == 3 && (tg & 1), ffn = ts != 2 && !isq;
  const int fi = ts == 3 ? 8 + th : 2 * tg + ts, g = tg;
  const int rb = w & 3, role = w >> 2;
  unsigned voffW, voffW2, wstr, wnext;
  rsrc_t rsW;
  if (ffn) {
    voffW = (16 * (w & 3) + rl) * 3584 + ((((((lane & 7) >> 1) ^ ((rl >> 1) & 1) ^ (role << 1)) << 1) | (lane & 1)) << 4);
    voffW2 = voffW + 8 * 3584 - 1024;
    rsW = mkrs((role ? wu : wg) + fi * 128, 448 * 3584 - fi * 128);
    wstr = 64 * 3584; wnext = 10 * 128;
  } else {
    const int c0 = 128 * w + lane, c1 = c0 + 64;
    const int r0 = c0 / 28, k0 = c0 - 28 * r0, r1 = c1 / 28, k1 = c1 - 28 * r1;
    voffW = r0 * 2688 + k0 * 16 + (k0 >= 14 ? 32 * 2688 - 224 : 0);
    voffW2 = r1 * 2688 + k1 * 16 + (k1 >= 14 ? 32 * 2688 - 224 : 0) - 1024;
    const int co = isq ? 448 * th : 896 + 224 * g;
    rsW = mkrs(wqkv + co, w < 7 ? 448 * 2688 - co : 0);
    wstr = 64 * 2688; wnext = isq ? 224 : 896;
  }
#pragma unroll
  for (int c = 0; c < 7; c++) rw[c] = rx[c] * (wstr / 128);
#define ITEM_WD(c, h, dcol) do { if (!(h)) DMA1P(voffW, rsW, ldsw, (c) * SLOT, rw[c], dcol, st1); else DMA2(voffW2, rsW, st1); } while (0)
#define ITEM_W(c, h) ITEM_WD(c, h, 0)
#define ITEM_N(c, h) ITEM_WD(c, h, wnext)
  ITEM_W(0, 0); ITEM_W(0, 1); ITEM_X(1, 0); ITEM_X(1, 1); ITEM_W(1, 0); ITEM_W(1, 1);

  const rsrc_t rs = mkrs(ws, WS_BYTES);
  unsigned tag;
  const int pu = (xcd >> 2) * 32 + r, li = pu / 3;
  int cpart = pu - 3 * li, lc;
  if (li < 12) { const int a = li >= 6, j = li - 6 * a; lc = 21 + 28 * (j & 1) + 10 * (j >> 1) + 2 * tg + a; }
  else if (li < 16) { const int l = 2 * tg + (li & 1); lc = 7 + 7 * ((li >> 1) & 1) + l; if (l > 6) cpart = 3; }
  else if (tg & 1) { lc = 4 * th + li - 16; if (lc > 6 || li > 19) { cpart = 3; lc = 0; } }
  else { lc = 21 + 28 * (li & 1) + 10 * ((li >> 1) & 1) + 8 + th; if (li > 19) cpart = 3; }
  const int srow = (xcd * 32 + r) * 8 + w;
  const int tidx = (xcd >> 1) * 56 + (xcd & 1) * 32 + r, rt = tidx / 7, ct = tidx - 7 * rt;
  const rsrc_t rsV = mkrs(wd + ct * 128, isq ? 0 : 1792 * 896 - ct * 128);
  const unsigned mlo = lane < 32 ? 0xffffffffu : 0u;
  const unsigned vp = w * 1024 + lane * 16, lp = w * 1024;

  const int rbg = m * 4 + rb;
  const int rb2 = w & 1, kq = w >> 1;
#define VFL (((rt * 2 + rb2) * 2 + (kq & 1)) * 128 + l31 * 4)
#define AB (((rt * 2 + rb2) * 144 + 2 * kq) * 1024 + lane * 16)
  u32x4 af[36];
#define LDA2(c) asm volatile("s_mov_b32 %2, %5\n\tbuffer_load_dwordx4 %0, %3, %4, %2 offen sc1\n\tbuffer_load_dwordx4 %1, %3, %4, %2 offen offset:1024 sc1" \
    : "=&v"(af[2 * (c)]), "=&v"(af[2 * (c) + 1]), "=&s"(st1) : "v"(AB), "s"(rs), "n"(WS_AFRAG + HOTA(c) * 8192) : "memory")
#define PIN4(c) "+v"(af[2 * (c)]), "+v"(af[2 * (c) + 1]), "+v"(af[2 * (c) + 2]), "+v"(af[2 * (c) + 3])
#define PIN8(c) "+v"(af[2 * (c)]), "+v"(af[2 * (c) + 1]), "+v"(af[2 * (c) + 2]), "+v"(af[2 * (c) + 3]), "+v"(af[2 * (c) + 4]), "+v"(af[2 * (c) + 5]), "+v"(af[2 * (c) + 6]), "+v"(af[2 * (c) + 7])
#define NEED(mask) do { if (!seen(fl, tag, mask)) poll(ws, WS_AFLAG + VFL, mask, tag); } while (0)
  f32x16 pa0, pa1;
  float k0, k1, ia2;
  unsigned fl = 0;

  const unsigned xrow = 32 * rb + l31;
  const unsigned vst = (m * 128 + xrow) * 16;
  s16x8 xf[28], wq[3][2], fb, wf[2][2];
  u32x4 stt, ca, cb;
  unsigned aW[4];
  auto fold = [&](u32x4 c) { const unsigned c2 = c[2] & mlo; u32x4 u = {c[0], c2, c[0], c2}; return asf(u); };
  auto make_fb = [&]() { const unsigned a = stt[0] & 0xffffu, b = stt[0] >> 16; u32x4 u = {a | (a << 16), a | (a << 16), b | (b << 16), b | (b << 16)}; fb = asf(u); };

  auto wfrag = [&](auto att, int n, int t) {
    const int sl = (n >> 2) % 7;
    if constexpr (decltype(att)::value) {
      const unsigned o = (sl & 3) * SLOT + (n & 1) * 7168 + ((n >> 1) & 1) * 224 + t * 56, b = aW[sl >> 2];
      return trfrag(b + o, b + o + 1792);
    } else {
      const unsigned o = (sl & 3) * SLOT + (n & 3) * 2048, b = aW[(sl >> 2) * 2 + t];
      return trfrag(b + o, b + o + 512);
    }
  };
  auto pass0 = [&](auto att, unsigned vca, unsigned vcb) {
    constexpr bool A = decltype(att)::value;
    u32x4 px, pw[3];
    TK(1);
    unsigned aX[4];
#pragma unroll
    for (int ks = 0; ks < 4; ks++) { aX[ks] = 7 * SLOT + xrow * 128 + (((2 * ks + hb) ^ rmask(xrow)) << 4); OPAQUE(aX[ks]); }
    auto xread = [&](int n) { xf[n] = *(LAS s16x8*)(aX[n & 3] + (((n >> 2) + 2) % 3) * SLOT); };
    f32x16 acc0 = zero16(), acc1 = zero16();
    const unsigned m56 = lane < 56 ? 0xffffffffu : 0u;
    float cs[8], s = 0.f, q = 0.f; unsigned pfd;
    WAITV(4);
    BAR();
    TK(4);
    xread(0); wq[0][1] = wfrag(att, 0, 1); wq[0][0] = wfrag(att, 0, 0);
    xread(1); wq[1][1] = wfrag(att, 1, 1); wq[1][0] = wfrag(att, 1, 0);
    ITEM_X(2, 0); ITEM_X(2, 1);
    auto pre = [&](int p) {
      if (p == 2) {
        asm volatile("buffer_load_dword %0, off, %1, 0 sc1" : "=v"(tag) : "s"(rs) : "memory");
        asm volatile("global_load_dwordx4 %0, %1, %2" : "=v"(px) : "v"(min(lane, 55) * 16), "s"(x + (size_t)srow * 896) : "memory");
      }
      if (p == 3) {
        const char* cb = lc < 21 ? wqkv + lc * 128 : (lc < 49 ? wg + (lc - 21) * 128 : wu + (lc - 49) * 128);
        const unsigned pitch = lc < 21 ? 2688 : 3584;
        const unsigned vo = rl * pitch + (lane & 7) * 16;
#pragma unroll
        for (int i = 0; i < 3; i++) {
          const int row = min(152 * cpart + 8 * (w + 8 * i), 440);
          asm volatile("global_load_dwordx4 %0, %1, %2" : "=v"(pw[i]) : "v"(vo), "s"(cb + (size_t)row * pitch) : "memory");
        }
      }
      if (p == 12) { tag += 1; for (int i = 0; i < 4; i++) px[i] &= m56; }
      if (p == 13) { for (int i = 0; i < 4; i++) { s = dot2(px[i], 0x3f803f80u, s); q = dot2(px[i], px[i], q); } OPAQUE(s); OPAQUE(q); }
      if (p == 14) { s = add32(s, q); s += dpp<0xB1>(s); OPAQUE(s); }
      if (p == 15) { s += dpp<0x4E>(s); s += dpp<0x141>(s); OPAQUE(s); }
      if (p == 16) { s += dpp<0x128>(s); s = add16(s, s); OPAQUE(s); }
      if (p == 17) { bcast32(s * (1.f / 448.f), s, q); q = fmaxf(fmaf(-s, s, q), 0.f) + 1e-5f; OPAQUE(s); OPAQUE(q); }
      if (p == 18) {
        u32x4 o = {split2(-s), tag, __float_as_uint(__builtin_amdgcn_rsqf(q)), tag};
        st16(rs, lane == 0 ? WS_STAT + srow * 16 : 0xfffffff0u, o);
      }
      if (p >= 15 && p < 23) {
        const int i = (p - 15) >> 1;
        if (p & 1) { pw[2][i] &= (w + 16 < (cpart == 2 ? 18 : 19) ? 0xffffffffu : 0u); cs[2 * i] = vadd(vadd(blo(pw[0][i]), blo(pw[1][i])), blo(pw[2][i])); }
        else cs[2 * i + 1] = vadd(vadd(bhi(pw[0][i]), bhi(pw[1][i])), bhi(pw[2][i]));
      }
      if (p == 23 || p == 24) for (int i = 2 * (p - 23); i < 2 * (p - 22); i++) { cs[i] = add32(cs[i], cs[i + 4]); OPAQUE(cs[i]); }
      if (p == 25) for (int i = 0; i < 2; i++) { cs[i] = add16(cs[i], cs[i + 2]); OPAQUE(cs[i]); }
      if (p == 26) for (int i = 0; i < 2; i++) { cs[i] += dpp<0x128>(cs[i]); OPAQUE(cs[i]); }
      if (p == 27) {
        *(LAS f32x2*)(LDS_MISC + w * 256 + ((lane & 7) * 8 + hb * 4 + g16 * 2) * 4) = f32x2{cs[0], cs[1]};
        if (A) *(LAS u32x2*)(LDS_PHI + 4 * 1568 + lane * 8) = u32x2{0u, 0u};
      }
    };
    sfor<0, 28>([&](auto nn) {
      constexpr int n = decltype(nn)::value;
      constexpr int c = (n + 2) >> 2, j = (n + 2) & 3;
      if (j == 0) {
        if (c == 2) asm volatile("s_waitcnt vmcnt(7)" : "+v"(tag), "+v"(px), "+v"(pw[0]), "+v"(pw[1]), "+v"(pw[2])::"memory");
        if (c == 3) WAITV(7); if (c == 4) WAITV(6); if (c == 1 || c == 5 || c == 6) WAITV(9);
        if (c == 7) asm volatile("s_waitcnt vmcnt(%1)" :: "v"(pfd), "n"(A ? 7 : 4) : "memory");
        BAR();
        TK(4 + c);
      }
      if (A && n == 15) {
        const unsigned pa = LDS_PHI + 2 * role * 1568 + (4 * hb + q4) * 56 + g16 * 32 + p4 * 8;
        for (int u = 0; u < 2; u++) { wf[u][0] = trfrag(pa + u * 1568, pa + u * 1568 + 448); wf[u][1] = trfrag(pa + u * 1568 + 896, pa + u * 1568 + 1344); }
      }
      if (n == 14 && w == 7 && cpart < 3) {
        float t = 0.f;
        for (int i = 0; i < 8; i++) t += *(LAS float*)(LDS_MISC + i * 256 + lane * 4);
        u32x2 o = {split2(t), tag};
        __builtin_amdgcn_raw_buffer_store_b64(o, rs, WS_CS + (lc * 64 + lane) * 32 + cpart * 8, 0, 16);
      }
#define P0_ISSUE(h) do { \
      if (n == 0 || n == 3) ITEM_W(n == 0 ? 2 : 3, h); \
      if (c >= 1 && c <= 4 && j == 0) ITEM_X(c + 2, h); \
      if (c >= 1 && c <= 3 && j == 2) ITEM_W(c + 3, h); \
      if (c == 4 && j >= 2) ITEM_N(j - 1, h); \
      if (c == 5 && j == 0) ITEM_N(0, h); \
      if (c == 5 && j == 2) ITEM_N(3, h); \
      if (c == 6 && j == 0) ITEM_N(4, h); } while (0)
      SB();
      if (n == 27) pa0 = MFMA(wq[n % 3][0], xf[n], acc0); else acc0 = MFMA(wq[n % 3][0], xf[n], acc0);
      SB();
      if (n + 2 < 28) xread(n + 2);
      wq[(n + 2) % 3][1] = wfrag(att, n + 2, 1);
      pre(2 * n);
      P0_ISSUE(0);
      if (n == 5) {
        const rsrc_t rsP = mkrs(wphi + (isq ? 8 * th : 4 * g) * 1568, A ? 4 * 1568 : 0);
        DMA1I(vp, rsP, lp, A ? LDS_PHI : LDS_BYTES, 0, st0);
      }
      if (n == 15) {
        const int u = bx * 8 + w;
        const rsrc_t rsF = u < 1568 ? mkrs(wd + u * 1024, 1024) : mkrs(wo + (u - 1568) * 1024, u < 1960 ? 1024 : 0);
        asm volatile("buffer_load_dword %0, %1, %2, 0 offen" : "=v"(pfd) : "v"((lane & 7) * 128), "s"(rsF) : "memory");
      }
      if (n == (A ? 24 : 26)) ALD16(ca, vca, WS_CS);
      if (n == (A ? 25 : 27)) ALD16(stt, vst, WS_STAT);
      SB();
      if (n == 27) pa1 = MFMA(wq[n % 3][1], xf[n], acc1); else acc1 = MFMA(wq[n % 3][1], xf[n], acc1);
      SB();
      wq[(n + 2) % 3][0] = wfrag(att, n + 2, 0);
      pre(2 * n + 1);
      P0_ISSUE(1);
      if (n == (A ? 24 : 26)) ALD16(cb, vcb, WS_CS);
      SB();
    });
    wq[0][0] = wq[1][0]; wq[0][1] = wq[1][1]; wq[1][0] = wq[2][0]; wq[1][1] = wq[2][1];
    STAMP(1); TK(12);
  };

  if (ffn) {
    FRESH_LANE();
    aW[0] = g16 * 8192 + (8 * hb + q4) * 128 + (((2 * role) ^ ((q4 >> 1) & 1) ^ (g16 << 1)) * 32) + p4 * 8;
    aW[1] = aW[0] ^ 32; aW[2] = aW[0] + 65536; aW[3] = aW[1] + 65536;
    OPAQUE(aW[0]); OPAQUE(aW[1]); OPAQUE(aW[2]); OPAQUE(aW[3]);
    const unsigned vcs = (1344 + g16 * 1792 + 32 * role + (lane & 15)) * 32 + hb * 16;
    pass0(std::false_type{}, vcs + fi * 2048, vcs + fi * 2048 + 512);
    const int hl2 = 20 + fi;
    const unsigned hb0 = WS_AFRAG + (rbg * 144 + fi * 4 + 2 * role) * 1024 + lane * 16;
    const unsigned pf = lane == 0 ? WS_AFLAG + (rbg * 2 + role) * 128 + fi * 4 : 0xffffff00u;
    const bool two = ts == 3;
    auto passn = [&](auto first) {
      constexpr bool F = decltype(first)::value, L = !F;
      const rsrc_t rsB = two ? rsV : rsW;
      unsigned voffB = voffW, voffB2 = voffW2;
      if (F && two) { voffB = voffX; voffB2 = voffX2; OPAQUE(voffB); }
      const unsigned ho = F ? hb0 : hb0 + 10 * 4096, vcp = vcs + (F ? fi : 10 + fi) * 2048, vcn = vcs + (F ? 10 + fi : hl2) * 2048;
      const unsigned dA = (F ? 10 : hl2 - fi) * 128, dB = two ? 0 : (hl2 - fi) * 128;
      const unsigned c2 = two ? 0xf0000000u : hb0 + (hl2 - fi) * 4096;
      f32x16 acc0 = zero16(), acc1 = zero16();
      f32x16 qa0, qa1;
      float gt[2], gu[2], hprev; u32x4 hq;
      auto geglu = [&](int hs) {
#pragma unroll
        for (int e = 0; e < 16; e++) {
          const int d = hs - ((F ? 12 : 4) + 12 * e / 5);
          const float G = e < 8 ? qa0[e & 7] : qa1[e & 7], U = e < 8 ? qa0[(e & 7) + 8] : qa1[(e & 7) + 8];
          if (d == 0) gt[e & 1] = vmul(G, fmaf(G * G, k1, k0));
          if (d == 1) { gt[e & 1] = __builtin_amdgcn_exp2f(gt[e & 1]); gu[e & 1] = vmul(G, U); }
          if (d == 2) gt[e & 1] = __builtin_amdgcn_rcpf(fmaf(gt[e & 1], ia2, ia2));
          if (d == 3) {
            if (e & 1) hq[(e >> 1) & 3] = pk2(hprev, gu[1] * gt[1]); else hprev = vmul(gu[0], gt[0]);
            if ((e & 7) == 7) st16(rs, ho + (e >> 3) * 1024, hq);
          }
        }
      };
      sfor<0, 28>([&](auto nn) {
        constexpr int n = decltype(nn)::value;
        if (!F && n == 7) { WAITV(4); st4(rs, pf, tag); }
        if (n == 10) { BAR(); TK(13); }
        if (n == 18) { asm volatile("s_waitcnt vmcnt(8)" : "+v"(ca), "+v"(cb)::"memory"); BAR(); TK(14); }
        if (L && n == 22) asm volatile("s_waitcnt vmcnt(4)" : "+v"(fl)::"memory");
        if (n == 26) { if (F) WAITV(10); BAR(); TK(15); }
        if (n == (F ? 4 : 0)) {
          if (F) asm volatile("s_waitcnt vmcnt(5)" : "+v"(ca), "+v"(cb), "+v"(stt)::"memory");
          g_check<F>(rs, ca, cb, stt, vcp, vcp + 512, vst, tag, mlo);
        }
        SB();
        if (n == 27) pa0 = MFMA(wq[n % 3][0], xf[n], acc0); else acc0 = MFMA(wq[n % 3][0], xf[n], acc0);
        SB();
        if (!L || n + 2 < 28) wq[(n + 2) % 3][1] = wfrag(std::false_type{}, n + 2, 1);
        if (F && n == 4) make_fb();
        if (n == (F ? 5 : 1)) qa0 = MFMA(fold(ca), fb, pa0);
        if (n < 4) ITEM_WD(5 + n / 2, n & 1, dA);
        if (F && n >= 4 && n < 8) ITEM_V((n - 4) / 2, n & 1);
        if (F && (n == 23 || n == 24)) ITEM_V(2, !(n & 1));
        if (L && n >= 10 && n < 16) ITEM_V((n - 4) / 2, n & 1);
        if (L && (n == 18 || n == 19)) ITEM_V(6, n & 1);
        if (L && (n == 21 || n == 22)) ITEM_V(7, !(n & 1));
        if (L && n >= 26) ITEM_V(n - 18, 0);
        if (!L && (n == 10 || n == 12 || n == 14 || n == 18 || n == 21)) {
          constexpr int bi = n < 18 ? (n - 10) / 2 : 3 + (n - 18) / 3;
          DMA1P(voffB, rsB, ldsw, bi * SLOT, two ? (3 + bi) * 128 * 896 : rw[bi], dB, st1);
        }
        if (!L && (n == 11 || n == 13 || n == 15 || n == 19 || n == 22)) DMA2(voffB2, rsB, st1);
        if constexpr (L && n >= 22 && n < 26) LDA2(n - 22);
        geglu(2 * n);
        SB();
        if (n == 27) pa1 = MFMA(wq[n % 3][1], xf[n], acc1); else acc1 = MFMA(wq[n % 3][1], xf[n], acc1);
        SB();
        if (!L || n + 2 < 28) wq[(n + 2) % 3][0] = wfrag(std::false_type{}, n + 2, 0);
        if (F && n == 4) {
          const float rstd = __uint_as_float(stt[2]);
          const float c0 = -2.f * 0.7978845608028654f * 1.4426950408889634f;
          k0 = vmul(c0, rstd); k1 = vmul(c0 * 0.044715f * rstd * rstd, rstd); ia2 = __builtin_amdgcn_rcpf(rstd * rstd);
        }
        if (n == (F ? 5 : 1)) qa1 = MFMA(fold(cb), fb, pa1);
        if (F && (n == 0 || n == 7 || n == 17 || n == 20)) st16(rs, (n < 8 ? hb0 + 10 * 4096 : c2) + (n & 1) * 1024, u32x4{~0u, ~0u, ~0u, ~0u});
        if (!F && n == 17) ALD4(fl, VFL, WS_AFLAG);
        if (n == 8) ALD16(ca, vcn, WS_CS);
        if (n == 9) ALD16(cb, vcn + 512, WS_CS);
        if (L && n >= 26) ITEM_V(n - 18, 1);
        geglu(2 * n + 1);
        SB();
      });
      if (!L) { wq[0][0] = wq[1][0]; wq[0][1] = wq[1][1]; wq[1][0] = wq[2][0]; wq[1][1] = wq[2][1]; }
      STAMP(F ? 2 : 3); TK(16);
    };
    passn(std::true_type{});
    if (two) { ITEM_V(8, 0); ITEM_V(8, 1); ITEM_V(9, 0); ITEM_V(9, 1); WAITV(4); st4(rs, pf, tag); fl = ~tag; }
    int tso = ts; asm volatile("" : "+s"(tso));
    if (tso != 3) passn(std::false_type{});
    {
      const unsigned vcn = vcs + (two ? 10 + fi : hl2) * 2048, hs = hb0 + (two ? 10 : hl2 - fi) * 4096;
      TK2(0);
      g_check<false>(rs, ca, cb, stt, vcn, vcn + 512, vst, tag, mlo);
      pa0 = MFMA(fold(ca), fb, pa0); pa1 = MFMA(fold(cb), fb, pa1);
      const bool early = seen(fl, tag, 0x3ffull);
      u32x4 o[2];
#pragma unroll
      for (int t = 0; t < 2; t++) {
        const f32x16& a = t ? pa1 : pa0;
        float hv[8];
        if (t) { TK2(1); }
#pragma unroll
        for (int i = 0; i < 8; i++) {
          const float e = __builtin_amdgcn_exp2f(a[i] * fmaf(a[i] * a[i], k1, k0));
          hv[i] = (a[i] * a[i + 8]) * __builtin_amdgcn_rcpf(fmaf(e, ia2, ia2));
        }
        o[t] = u32x4{pk2(hv[0], hv[1]), pk2(hv[2], hv[3]), pk2(hv[4], hv[5]), pk2(hv[6], hv[7])};
        OPAQUE(o[t]); SB();
      }
      st16(rs, hs, o[0]); st16(rs, hs + 1024, o[1]);
      TK2(3);
      asm volatile("s_waitcnt vmcnt(6)" : PIN8(0)::"memory");
      TK2(4);
      if (!early) {
        poll(ws, WS_AFLAG + VFL, 0x3ffull, tag); LDA2(0); LDA2(1); LDA2(2); LDA2(3);
        asm volatile("s_waitcnt vmcnt(0)" : PIN8(0)::"memory");
      }
    }
  } else {
    aW[0] = (8 * hb + q4) * 448 + 112 * role + g16 * 32 + p4 * 8; aW[1] = aW[0] + 65536;
    OPAQUE(aW[0]); OPAQUE(aW[1]);
    const int head0 = (isq ? 8 * th : 4 * g) + 2 * role;
    const unsigned vcs = ((isq ? 0 : 448) + 28 * head0 + l31) * 32 + hb * 16;
    pass0(std::true_type{}, vcs, vcs + 28 * 32);
    float rstd, os;
    const float ninf = __uint_as_float(~mlo & 0xff800000u);
    u32x4 ph[2][4];
    Sm sm;
    {
      const unsigned vca = vcs + wnext * 16;
      const unsigned oc = isq ? WS_AFRAG + (rbg * 144 + 112 + head0 * 2) * 1024 + lane * 16 : 0xf0000000u;
      f32x16 acc0 = zero16(), acc1 = zero16();
      f32x16 qa0, qa1;
      u32x4 kf[2][2];
      f32x16 p1;
      unsigned wa = 0;
      auto piece = [&](int h) {
        if (h == 2) { make_fb(); rstd = __uint_as_float(stt[2]); os = isq ? 1.f : rstd; }
        if (h == 3) qa0 = MFMA(fold(ca), fb, pa0);
        if (h == 4) qa1 = MFMA(fold(cb), fb, pa1);
        if (h == 5 || h == 7) kf[h == 7][0] = pack8u(h == 7 ? qa1 : qa0, 0);
        if (h == 6 || h == 8) { kf[h == 8][1] = pack8u(h == 8 ? qa1 : qa0, 1); kf[h == 8][1][2] &= mlo; kf[h == 8][1][3] &= mlo; }
        if (h == 6) sm.p = MFMA(wf[0][0], asf(kf[0][0]), zero16());
        if (h == 7) sm.p = MFMA(wf[0][1], asf(kf[0][1]), sm.p);
        if (h == 24) p1 = MFMA(wf[1][0], asf(kf[1][0]), zero16());
        if (h == 25) p1 = MFMA(wf[1][1], asf(kf[1][1]), p1);
        if (h == 28) { sm_sum(sm, os); sm.p = p1; }
        if (h == 9 || h == 29) sm_max(sm, ninf);
        if (h == 10 || h == 30) sm_min(sm, ninf);
        if (h == 11 || h == 39) sm_consts(sm, rstd);
        if (h >= 12 && h < 28) sm_exp(sm, h - 12, ninf);
        if (h >= 31 && h < 39) sm_norm2(sm, (h - 31) >> 1, (h - 31) & 1, ph[0][(h - 31) >> 1]);
        if (h >= 40) sm_exp(sm, h - 40, ninf);
        if (h == 39) { FRESH_LANE(); wa = (2 * role * 128 + 32 * rb + l31) * 200 + hb * 8; }
        if (h >= 40 && h < 48) { const u32x4& v = ph[0][(h - 40) >> 1]; *(LAS u32x2*)(wa + (h - 40) * 16) = u32x2{v[2 * (h & 1)], v[2 * (h & 1) + 1]}; }
      };
      sfor<0, 28>([&](auto nn) {
        constexpr int n = decltype(nn)::value;
        if (n == 10) { BAR(); TK(13); }
        if (n == 18) {
          asm volatile("s_waitcnt vmcnt(2)" : "+v"(ca), "+v"(cb)::"memory"); BAR(); TK(14);
          st4(rs, tid == 0 ? WS_CFLAG + ((isq ? 4 + th : g) * 16 + m) * 128 : 0xf0000000u, tag);
        }
        if (n == 26) { BAR(); TK(15); }
        if (n == 6 || n == 11) {
          FRESH_LANE();
          const unsigned co = isq ? (lane < 16 && n == 6 ? WS_KVRED + ((2 * th + (w >> 2)) * 16 + m) * 1024 + (w & 3) * 256 + lane * 16 : 0xf0000000u)
                                  : WS_KVPART + ((g * 8 + w) * 16 + m) * 2048 + (n & 1) * 1024 + lane * 16;
          st16(rs, co, u32x4{~0u, ~0u, ~0u, ~0u});
        }
        if (n == 1) {
          asm volatile("s_waitcnt vmcnt(1)" : "+v"(ca), "+v"(cb), "+v"(stt)::"memory");
          g_check<true>(rs, ca, cb, stt, vcs, vcs + 28 * 32, vst, tag, mlo);
        }
        if (n == 19) g_check<false>(rs, ca, cb, stt, vca, vca + 28 * 32, vst, tag, mlo);
        SB();
        if (n == 27) pa0 = MFMA(wq[n % 3][0], xf[n], acc0); else acc0 = MFMA(wq[n % 3][0], xf[n], acc0);
        SB();
        if (n + 2 < 28) wq[(n + 2) % 3][1] = wfrag(std::true_type{}, n + 2, 1);
        piece(2 * n);
        if (n < 4) ITEM_N(5 + n / 2, n & 1);
        if (n >= 4 && n < 10) ITEM_V((n - 4) / 2, n & 1);
        if (n == 10) {
          const rsrc_t rsP = mkrs(wphi + (8 * th + 4) * 1568, isq ? 4 * 1568 : 0);
          FRESH_LANE();
          DMA1I(lp + lane * 16, rsP, lp, 25600, 0, st0);
        }
        if (n == 20) acc0 = MFMA(fold(ca), fb, acc0);
        SB();
        if (n == 27) pa1 = MFMA(wq[n % 3][1], xf[n], acc1); else acc1 = MFMA(wq[n % 3][1], xf[n], acc1);
        SB();
        if (n + 2 < 28) wq[(n + 2) % 3][0] = wfrag(std::true_type{}, n + 2, 0);
        piece(2 * n + 1);
        if (n >= 5 && n % 3 == 2) { constexpr int i = (n - 5) / 3; __builtin_amdgcn_raw_buffer_store_b128(u32x4{~0u, ~0u, ~0u, ~0u}, rs, oc, (i & 3) * 1024 + (i >> 2) * 8192, 16); }
        if (n == 8) ALD16(ca, vca, WS_CS);
        if (n == 9) ALD16(cb, vca + 28 * 32, WS_CS);
        if (n == 20) acc1 = MFMA(fold(cb), fb, acc1);
        SB();
      });
      sm_sum(sm, os);
    }
    STAMP(2); TK(16);
    FRESH_LANE();
    const unsigned xrow = 32 * rb + l31;
    if (!isq) {
      const unsigned ir = split2(__builtin_amdgcn_rcpf(rstd)) & ~mlo;
      const unsigned wa = (2 * role * 128 + xrow) * 200 + hb * 8;
#pragma unroll
      for (int u = 0; u < 2; u++) {
        if (u) for (int f = 0; f < 4; f++) ph[1][f] = sm_norm(sm, f);
#pragma unroll
        for (int f = u ? 0 : 4; f < 6; f++) {
          u32x4 v = f < 4 ? ph[u][f] : pack8u(u ? pa1 : pa0, f - 4);
          if (f == 5) { v[2] = (v[2] & mlo) | ir; v[3] &= mlo; }
          *(LAS u32x2*)(wa + u * 25600 + f * 32) = u32x2{v[0], v[1]}; *(LAS u32x2*)(wa + u * 25600 + f * 32 + 16) = u32x2{v[2], v[3]};
        }
      }
      BAR();
      TK(17);
      unsigned ra = (w >> 1) * 25600 + (8 * hb + q4) * 200 + g16 * 32 + p4 * 8, rp = ra + (w & 1) * 64;
      OPAQUE(ra); OPAQUE(rp);
      f32x16 kv = zero16();
#pragma unroll
      for (int s = 0; s < 8; s++) kv = MFMA(trfrag(rp + s * 3200, rp + s * 3200 + 800), trfrag(ra + 128 + s * 3200, ra + 128 + s * 3200 + 800), kv);
      ALD4(fl, VFL, WS_AFLAG);
      st16(rs, WS_KVPART + ((g * 8 + w) * 16 + m) * 2048 + lane * 16, pack8u(kv, 0));
      st16(rs, WS_KVPART + ((g * 8 + w) * 16 + m) * 2048 + 1024 + lane * 16, pack8u(kv, 1));
      STAMP(3); TK(18);
      BAR();
      asm volatile("s_waitcnt vmcnt(2)" : "+v"(fl)::"memory");
      NEED(0x3ffull);
      LDA2(0); LDA2(1); LDA2(2); LDA2(3);
      sfor<3, 10>([&](auto cc) { ITEM_V(decltype(cc)::value, 0); ITEM_V(decltype(cc)::value, 1); });
      asm volatile("s_waitcnt vmcnt(14)" : PIN8(0)::"memory");
      TK(19);
    } else {
      u32x4 ph2[2][4];
      const unsigned vcf = (lane < 32 ? 2 * th * 16 + lane : (4 + th) * 16 + (lane & 15)) * 128;
      ALD4(fl, vcf, WS_CFLAG);
      for (int f = 0; f < 4; f++) ph[1][f] = sm_norm(sm, f);
      {
        const unsigned pa = 25600 + 2 * role * 1568 + (4 * hb + q4) * 56 + g16 * 32 + p4 * 8;
#pragma unroll
        for (int u = 0; u < 2; u++) {
          u32x4 k1 = pack8u(u ? pa1 : pa0, 1); k1[2] &= mlo; k1[3] &= mlo;
          sm.p = MFMA(trfrag(pa + u * 1568, pa + u * 1568 + 448), asf(pack8u(u ? pa1 : pa0, 0)), zero16());
          sm.p = MFMA(trfrag(pa + u * 1568 + 896, pa + u * 1568 + 1344), asf(k1), sm.p);
          sm_max(sm, ninf); sm_min(sm, ninf); sm_consts(sm, rstd);
          for (int i = 0; i < 16; i++) sm_exp(sm, i, ninf);
          sm_sum(sm, 1.f);
          for (int f = 0; f < 4; f++) { ph2[u][f] = sm_norm(sm, f); OPAQUE(ph2[u][f]); }
        }
      }
      STAMP(3); TK(17);
      asm volatile("s_waitcnt vmcnt(0)" : "+v"(fl)::"memory");
      st4(rs, l31 == 0 ? WS_AFLAG + (rbg * 2 + hb) * 128 + (28 + th * 2 + role) * 4 : 0xf0000000u, tag);
      if (!seen(fl, tag, ~0ull)) poll(ws, WS_CFLAG + vcf, ~0ull, tag);
      {
        const int gg = 2 * th + (w >> 2);
        const unsigned pb = WS_KVPART + ((gg * 8 + (m >> 1)) * 16 + (lane >> 4)) * 2048 + (m & 1) * 1024 + ((w & 3) * 16 + (lane & 15)) * 16;
        u32x4 pv[4];
#pragma unroll 1
        for (int it = 0; it < SPIN_CAP; ++it) {
          unsigned mx = 0;
#pragma unroll
          for (int i = 0; i < 4; i++) pv[i] = ld16(rs, pb + i * 8192);
#pragma unroll
          for (int i = 0; i < 4; i++) mx = umax4(mx, pv[i]);
          if (fresh(mx)) break;
          __builtin_amdgcn_s_sleep(1);
        }
        STAMP(5); TK(18);
        float t[8];
#pragma unroll
        for (int j = 0; j < 4; j++) {
          t[2 * j] = (blo(pv[0][j]) + blo(pv[1][j])) + (blo(pv[2][j]) + blo(pv[3][j]));
          t[2 * j + 1] = (bhi(pv[0][j]) + bhi(pv[1][j])) + (bhi(pv[2][j]) + bhi(pv[3][j]));
        }
        for (int j = 0; j < 8; j++) { t[j] = add32(t[j], t[j]); t[j] = add16(t[j], t[j]); }
        st16(rs, lane < 16 ? WS_KVRED + (gg * 16 + m) * 1024 + ((w & 3) * 16 + lane) * 16 : 0xf0000000u, u32x4{pk2(t[0], t[1]), pk2(t[2], t[3]), pk2(t[4], t[5]), pk2(t[6], t[7])});
        STAMP(6); TK(19);
      }
      {
        u32x4 gv[4];
        __builtin_amdgcn_s_sleep(12);
#pragma unroll 1
        for (int it = 0; it < SPIN_CAP; ++it) {
          unsigned mx = 0;
#pragma unroll
          for (int i = 0; i < 4; i++) gv[i] = ld16(rs, WS_KVRED + ((8 * th + w) * 4 + i) * 1024 + lane * 16);
#pragma unroll
          for (int i = 0; i < 4; i++) mx = umax4(mx, gv[i]);
          if (fresh(mx)) break;
          __builtin_amdgcn_s_sleep(4);
        }
#pragma unroll
        for (int i = 0; i < 4; i++) *(LAS u32x4*)(4 * SLOT + (w * 4 + i) * 1024 + lane * 16) = gv[i];
      }
      BAR();
      STAMP(7); TK(20);
      u32x4 kf[4][4];
#pragma unroll
      for (int u = 0; u < 4; u++)
#pragma unroll
        for (int i = 0; i < 4; i++) kf[u][i] = *(LAS u32x4*)(4 * SLOT + ((2 * role + (u & 1) + 4 * (u >> 1)) * 4 + i) * 1024 + lane * 16);
#pragma unroll
      for (int u = 0; u < 4; u++) {
        f32x16 nm = zero16();
        for (int i = 0; i < 4; i++) nm = MFMA(asf(kf[u][i]), asf(u < 2 ? ph[u & 1][i] : ph2[u & 1][i]), nm);
        float dl, dh; bcast32(nm[12] + nm[13], dl, dh);
        const float rd = __builtin_amdgcn_rcpf(dh + 1e-6f);
        for (int i = 0; i < 16; i++) nm[i] *= rd;
        u32x4 o1 = pack8u(nm, 1); o1[2] &= mlo; o1[3] &= mlo;
        const unsigned oo = WS_AFRAG + (rbg * 144 + 112 + (head0 + (u & 1) + 4 * (u >> 1)) * 2) * 1024 + lane * 16;
        st16(rs, oo, pack8u(nm, 0));
        st16(rs, oo + 1024, o1);
      }
      STAMP(4); TK(21); DUMPTK();
      if (bx == 24 * 8 + 1 && w == 0) {
#pragma unroll 1
        for (int i = 0; i < 8; i++) poll(ws, WS_AFLAG + (2 * i + hb) * 8 * 128 + l31 * 4, 0x500003ff500003ffull, tag);
        poll(ws, WS_CFLAG + lane * 128, ~0ull, tag);
        st4(rs, WS_EPOCH, tag);
      }
      return;
    }
  }

  {
    FRESH_LANE();
    const int tid = w * 64 + lane;
    const unsigned swz = (((lane & 7) ^ rmask(16 * w + rl)) << 4);
    STAMP(5);
    LDA2(4); LDA2(5); LDA2(6);
    const unsigned r0 = 32 * kq + 4 * hb + q4;
    unsigned bV[3][4];
#pragma unroll
    for (int i = 0; i < 12; i++) { bV[i >> 2][i & 3] = ((r0 * 128 + (((2 * g16 + (p4 >> 1)) ^ rmask(r0)) << 4) + (p4 & 1) * 8) ^ ((i & 1) * 64) ^ ((i & 2) * 16)) + (i >> 2) * 65536; OPAQUE(bV[i >> 2][i & 3]); }
    const unsigned voffO = (28 * (w >> 1) + 16 * (w & 1) + rl) * 896 + swz;
    const unsigned voffO2 = voffO + (((w & 1) && rl >= 4) ? 0 : 8 * 896) - 1024;
    const rsrc_t rsO = mkrs(wo + ct * 128, 448 * 896 - ct * 128);
    const size_t go = (size_t)(rt * 64 + (tid >> 3)) * 896 + ct * 128 + (tid & 7) * 16;
    u32x4 xr;
    f32x16 acc0 = zero16(), acc1 = zero16();
    s16x8 wv[2][4];
    auto vread = [&](int c) {
      const int sl = (c + 7) % 10; const unsigned so = (sl & 3) * SLOT;
      for (int i = 0; i < 4; i++) wv[c & 1][i] = trfrag(bV[sl >> 2][i] + so + (i >> 1) * 2048, bV[sl >> 2][i] + so + (i >> 1) * 2048 + 1024);
    };
    vread(0);
    TK(20);
    sfor<0, 18>([&](auto cc) {
      constexpr int c = decltype(cc)::value;
      asm volatile("s_waitcnt vmcnt(%3)" : "+v"(af[2 * c]), "+v"(af[2 * c + 1]), "+v"(fl) : "n"(P2::wait(c)) : "memory");
      if constexpr (c >= 5) {
        constexpr int ce = c < 10 ? 10 : c < 14 ? 14 : 18, c0 = c + 4 <= ce ? c : ce - 4;
#pragma unroll 1
        for (int it = 0; it < SPIN_CAP; ++it) {
          if (__builtin_expect(fresh(umax4(umax4(0u, af[2 * c]), af[2 * c + 1])), 1)) break;
          WAITV(0);
          __builtin_amdgcn_s_sleep(4);
          LDA2(c); if (c < c0 + 3) LDA2(c0 + 3); if (c < c0 + 2) LDA2(c0 + 2); if (c < c0 + 1) LDA2(c0 + 1);
          asm volatile("s_waitcnt vmcnt(0)" : PIN8(c0)::"memory");
        }
        if (c == 5) { STAMP(6); } if (c == 10) { STAMP(7); TK(21); } if (c == 14) { STAMP(12); TK(22); }
      }
      if (P2::bar(c)) BAR();
      TK2(5 + c);
      if (c + 1 < 18) vread(c + 1);
      SB();
      acc0 = MFMA(wv[c & 1][0], asf(af[2 * c]), acc0); acc1 = MFMA(wv[c & 1][1], asf(af[2 * c]), acc1);
      if (c == P2::FL2) ALD4(fl, VFL, WS_AFLAG);
      constexpr int j = P2::vj(c);
      if (j >= 0 && j < 4) { ITEM_V(j + 10, 0); ITEM_V(j + 10, 1); }
      if (j >= 4) { DMA1I(voffO, rsO, ldsw, ((j + 7) % 10) * SLOT, (j - 4) * (112 * 896), st0); DMA2(voffO2, rsO, st0); }
      SB();
      acc0 = MFMA(wv[c & 1][2], asf(af[2 * c + 1]), acc0); acc1 = MFMA(wv[c & 1][3], asf(af[2 * c + 1]), acc1);
      if (c == P2::N2) NEED(0xf0000000ull);
      if constexpr (P2::a0(c) < P2::a0(c + 1)) LDA2(P2::a0(c));
      if constexpr (P2::a0(c) + 1 < P2::a0(c + 1)) LDA2(P2::a0(c) + 1);
      if constexpr (P2::a0(c) + 2 < P2::a0(c + 1)) LDA2(P2::a0(c) + 2);
      if constexpr (P2::a0(c) + 3 < P2::a0(c + 1)) LDA2(P2::a0(c) + 3);
      static_assert(P2::a0(c + 1) - P2::a0(c) <= 4, "schedule");
      if (c == 13) asm volatile("global_load_dwordx4 %0, %1, off" : "=v"(xr) : "v"(x + go) : "memory");
      SB();
    });
    STAMP(8);
    const unsigned wa = 5 * SLOT + (kq * 64 + rb2 * 32 + l31) * 272 + hb * 16;
#pragma unroll
    for (int gq = 0; gq < 4; gq++) {
      f32x4 v0 = {acc0[4 * gq], acc0[4 * gq + 1], acc0[4 * gq + 2], acc0[4 * gq + 3]};
      f32x4 v1 = {acc1[4 * gq], acc1[4 * gq + 1], acc1[4 * gq + 2], acc1[4 * gq + 3]};
      *(LAS f32x4*)(wa + gq * 32) = v0; *(LAS f32x4*)(wa + 128 + gq * 32) = v1;
    }
    BAR();
    const unsigned ra = 5 * SLOT + (tid >> 3) * 272 + (tid & 7) * 32;
    f32x4 s0 = *(LAS f32x4*)ra, s1 = *(LAS f32x4*)(ra + 16);
#pragma unroll
    for (int k = 1; k < 4; k++) { s0 += *(LAS f32x4*)(ra + k * 64 * 272); s1 += *(LAS f32x4*)(ra + k * 64 * 272 + 16); }
    asm volatile("s_waitcnt vmcnt(0)" : "+v"(xr)::"memory");
    u32x4 o;
    o[0] = pk2(s0[0] + blo(xr[0]), s0[1] + bhi(xr[0]));
    o[1] = pk2(s0[2] + blo(xr[1]), s0[3] + bhi(xr[1]));
    o[2] = pk2(s1[0] + blo(xr[2]), s1[1] + bhi(xr[2]));
    o[3] = pk2(s1[2] + blo(xr[3]), s1[3] + bhi(xr[3]));
    __builtin_amdgcn_raw_buffer_store_b128(o, mkrs(out, 2048 * 896), (unsigned)go, 0, 16);
    STAMP(9); STAMPC(15); TK(23); TK2(23); DUMPTK();
  }
}

extern "C" void kernel_launch(void* const* d_in, const int* in_sizes, int n_in, void* d_out, int out_size, void* d_ws, size_t ws_size, hipStream_t stream) {
  (void)hipFuncSetAttribute((const void*)fused_kernel, hipFuncAttributeMaxDynamicSharedMemorySize, LDS_BYTES);
  fused_kernel<<<256, 512, LDS_BYTES, stream>>>((const char*)d_in[0], (const char*)d_in[1], (const char*)d_in[2], (const char*)d_in[3],
                                                (const char*)d_in[5], (const char*)d_in[6], (const char*)d_in[7], (char*)d_out, (char*)d_ws);
}
```

```cpp
#define MK 1
#include <hip/hip_runtime.h>
#include <hip/hip_bf16.h>
#include <stdint.h>
#include <type_traits>
#pragma clang diagnostic ignored "-Wint-to-pointer-cast"
#pragma clang diagnostic ignored "-Warray-bounds"

#define DI __device__ __forceinline__
#define LAS __attribute__((address_space(3)))
#define GAS __attribute__((address_space(1)))
typedef __attribute__((ext_vector_type(8))) short s16x8;
typedef __attribute__((ext_vector_type(4))) short s16x4;
typedef __attribute__((ext_vector_type(16))) float f32x16;
typedef __attribute__((ext_vector_type(4))) float f32x4;
typedef __attribute__((ext_vector_type(2))) float f32x2;
typedef __attribute__((ext_vector_type(4))) unsigned u32x4;
typedef __attribute__((ext_vector_type(2))) unsigned u32x2;
typedef __attribute__((ext_vector_type(2))) __bf16 bf16x2;
typedef GAS unsigned gu32;
typedef __amdgpu_buffer_rsrc_t rsrc_t;
#define RLX_AGENT __ATOMIC_RELAXED, __HIP_MEMORY_SCOPE_AGENT

constexpr unsigned WS_EPOCH = 0;
constexpr unsigned WS_CFLAG = 1024;
constexpr unsigned WS_AFLAG = 16384;
constexpr unsigned WS_STAT = WS_AFLAG + 128 * 128;
constexpr unsigned WS_CS = WS_STAT + 2048 * 16;
constexpr unsigned WS_KVRED = 262144;
constexpr unsigned WS_KVPART = WS_KVRED + 131072;
constexpr unsigned WS_AFRAG = WS_KVPART + (2u << 20);
constexpr unsigned WS_STAMP = WS_AFRAG + 64u * 144u * 1024u;
constexpr unsigned WS_BYTES = WS_STAMP;
static_assert(WS_CFLAG + 128 * 64 <= WS_AFLAG && WS_CS + 4928 * 32 <= WS_KVRED, "ws map");

constexpr int LDS_BYTES = 163840;
constexpr unsigned SLOT = 16384;
constexpr unsigned LDS_MISC = 0, LDS_PHI = 2048;
constexpr int SPIN_CAP = 1 << 14;

#define HOTA(c) (c)
#define HOTV(c) (c)
#define MFMA(a, b, c) __builtin_amdgcn_mfma_f32_32x32x16_bf16((a), (b), (c), 0, 0, 0)
#define WAITV(n) asm volatile("s_waitcnt vmcnt(" #n ")" ::: "memory")
#define BAR() do { asm volatile("s_waitcnt lgkmcnt(0)" ::: "memory"); __builtin_amdgcn_s_barrier(); asm volatile("" ::: "memory"); } while (0)
#define SB() __builtin_amdgcn_sched_barrier(0)
#define OPAQUE(v) asm volatile("" : "+v"(v))
template <int N> DI void waitv() { asm volatile("s_waitcnt vmcnt(%0)" ::"n"(N) : "memory"); }
template <int N> using IC = std::integral_constant<int, N>;
template <int I, int N, class F> DI void sfor(F&& f) { if constexpr (I < N) { f(IC<I>{}); sfor<I + 1, N>(f); } }

#define STAMP(i)
#define STAMPC(i)
#define TK(i)
#define TK2(i)
#define DUMPTK()

#define DMA1I(voff, rsrc, ldsb, LIMM, SOFF, tmp) asm volatile("s_add_u32 m0, %3, %4\n\ts_mov_b32 %0, %5\n\tbuffer_load_dwordx4 %1, %2, %0 offen lds" \
    : "=&s"(tmp) : "v"(voff), "s"(rsrc), "s"(ldsb), "n"(LIMM), "n"(SOFF) : "memory", "scc")
#define DMA1P(voff, rsrc, ldsb, LIMM, sa, sb, tmp) asm volatile("s_add_u32 m0, %3, %4\n\ts_add_u32 %0, %5, %6\n\tbuffer_load_dwordx4 %1, %2, %0 offen lds" \
    : "=&s"(tmp) : "v"(voff), "s"(rsrc), "s"(ldsb), "n"(LIMM), "s"(sa), "s"(sb) : "memory", "scc")
#define DMA1M(voff, rsrc, ldsb, LIMM, sa, sb, tmp) asm volatile("s_add_u32 m0, %3, %4\n\ts_mul_i32 %0, %5, %6\n\tbuffer_load_dwordx4 %1, %2, %0 offen lds" \
    : "=&s"(tmp) : "v"(voff), "s"(rsrc), "s"(ldsb), "n"(LIMM), "s"(sa), "s"(sb) : "memory", "scc")
#define DMA2(voff, rsrc, soff) asm volatile("buffer_load_dwordx4 %0, %1, %2 offen offset:1024 lds" :: "v"(voff), "s"(rsrc), "s"(soff) : "memory")
#define ALD16(dst, voff, soff) asm volatile("buffer_load_dwordx4 %0, %1, %2, %3 offen sc1" : "=v"(dst) : "v"(voff), "s"(rs), "s"(soff) : "memory")
#define ALD4(dst, voff, soff) asm volatile("buffer_load_dword %0, %1, %2, %3 offen sc1" : "=v"(dst) : "v"(voff), "s"(rs), "s"(soff) : "memory")

#define FRESH_LANE() int lane; asm volatile("v_mbcnt_lo_u32_b32 %0, -1, 0\n\tv_mbcnt_hi_u32_b32 %0, -1, %0" : "=v"(lane)); \
  const int g16 = (lane >> 4) & 1, q4 = (lane & 15) >> 2, p4 = lane & 3, l31 = lane & 31, hb = lane >> 5, rl = lane >> 3; \
  (void)g16; (void)q4; (void)p4; (void)l31; (void)hb; (void)rl
DI unsigned usel(bool c, unsigned a, unsigned b) { const unsigned k = c ? ~0u : 0u; return (a & k) | (b & ~k); }
DI const char* psel(bool c, const char* a, const char* b) { return c ? a : b; }
DI rsrc_t mkoff(const char* p, unsigned off, unsigned bytes) { return __builtin_amdgcn_make_buffer_rsrc((void*)(p + off), 0, bytes - off, 0x00020000); }
DI rsrc_t mkrs(const void* p, unsigned bytes) { return __builtin_amdgcn_make_buffer_rsrc((void*)p, 0, bytes, 0x00020000); }
DI s16x8 trfrag(unsigned a0, unsigned a1) {
  s16x4 lo = __builtin_amdgcn_ds_read_tr16_b64_v4i16((LAS s16x4*)a0);
  s16x4 hi = __builtin_amdgcn_ds_read_tr16_b64_v4i16((LAS s16x4*)a1);
  return __builtin_shufflevector(lo, hi, 0, 1, 2, 3, 4, 5, 6, 7);
}
DI unsigned pk2(float a, float b) { f32x2 p = {a, b}; return __builtin_bit_cast(unsigned, __builtin_convertvector(p, bf16x2)); }
DI float dot2(unsigned a, unsigned b, float c) { return __builtin_amdgcn_fdot2_f32_bf16(__builtin_bit_cast(bf16x2, a), __builtin_bit_cast(bf16x2, b), c, false); }
DI float blo(unsigned u) { return __uint_as_float(u << 16); }
DI float bhi(unsigned u) { return __uint_as_float(u & 0xffff0000u); }
DI float fand(float v, unsigned m) { return __uint_as_float(__float_as_uint(v) & m); }
DI float vmul(float a, float b) { float r = a * b; OPAQUE(r); return r; }
DI float vadd(float a, float b) { float r = a + b; OPAQUE(r); return r; }
DI float max3(float a, float b, float c) { return __builtin_elementwise_maximum(__builtin_elementwise_maximum(a, b), c); }
DI float min3(float a, float b, float c) { return __builtin_elementwise_minimum(__builtin_elementwise_minimum(a, b), c); }
DI float add32(float a, float b) { auto r = __builtin_amdgcn_permlane32_swap(__float_as_uint(a), __float_as_uint(b), false, false); return __uint_as_float(r[0]) + __uint_as_float(r[1]); }
DI float add16(float a, float b) { auto r = __builtin_amdgcn_permlane16_swap(__float_as_uint(a), __float_as_uint(b), false, false); return __uint_as_float(r[0]) + __uint_as_float(r[1]); }
DI void bcast32(float v, float& lo, float& hi) { auto r = __builtin_amdgcn_permlane32_swap(__float_as_uint(v), __float_as_uint(v), false, false); lo = __uint_as_float(r[0]); hi = __uint_as_float(r[1]); }
template <int CTRL> DI float dpp(float x) { return __builtin_bit_cast(float, __builtin_amdgcn_mov_dpp(__builtin_bit_cast(int, x), CTRL, 0xf, 0xf, true)); }
DI float wave_sum(float v) {
  v += dpp<0xB1>(v); v += dpp<0x4E>(v); v += dpp<0x141>(v); v += dpp<0x128>(v);
  v = add16(v, v); return add32(v, v);
}
DI unsigned split2(float v) {
  const unsigned h = pk2(v, 0.f);
  return pk2(__uint_as_float(h << 16), v - __uint_as_float(h << 16));
}
DI u32x4 pack8u(const f32x16& x, int s) {
  u32x4 u = {pk2(x[8 * s], x[8 * s + 1]), pk2(x[8 * s + 2], x[8 * s + 3]), pk2(x[8 * s + 4], x[8 * s + 5]), pk2(x[8 * s + 6], x[8 * s + 7])};
  return u;
}
DI s16x8 asf(u32x4 u) { return __builtin_bit_cast(s16x8, u); }
DI unsigned rmask(unsigned row) { return (((row >> 1) & 1) << 2) | ((row >> 2) & 1) | ((row >> 3) & 2); }
DI f32x16 zero16() { f32x16 z; for (int i = 0; i < 16; i++) z[i] = 0.f; return z; }
DI void st16(rsrc_t rs, unsigned off, u32x4 v) { __builtin_amdgcn_raw_buffer_store_b128(v, rs, off, 0, 16); }
DI void st4(rsrc_t rs, unsigned off, unsigned v) { __builtin_amdgcn_raw_buffer_store_b32(v, rs, off, 0, 16); }
DI u32x4 ld16(rsrc_t rs, unsigned off) { return __builtin_amdgcn_raw_buffer_load_b128(rs, off, 0, 16); }
DI bool seen(unsigned v, unsigned tag, unsigned long long need) { return (__builtin_amdgcn_ballot_w64(v == tag) & need) == need; }
DI void poll(char* ws, unsigned off, unsigned long long need, unsigned tag) {
#pragma unroll 1
  for (int it = 0; it < SPIN_CAP; ++it) {
    const unsigned v = __hip_atomic_load((gu32*)(ws + off), RLX_AGENT);
    if (seen(v, tag, need)) break;
    __builtin_amdgcn_s_sleep(2);
  }
  __builtin_amdgcn_fence(__ATOMIC_ACQUIRE, "wavefront");
}
template <bool ST> DI void g_check(rsrc_t rs, u32x4& a, u32x4& b, u32x4& st, unsigned va, unsigned vb, unsigned vs, unsigned tag, unsigned mlo) {
#pragma unroll 1
  for (int it = 0; it < SPIN_CAP; ++it) {
    unsigned bad = (a[1] ^ tag) | (b[1] ^ tag) | (((a[3] ^ tag) | (b[3] ^ tag)) & mlo);
    if (ST) bad |= (st[1] ^ tag) | (st[3] ^ tag);
    if (__builtin_expect(__builtin_amdgcn_ballot_w64(bad != 0) == 0ull, 1)) break;
    ALD16(a, va, WS_CS); ALD16(b, vb, WS_CS); if (ST) ALD16(st, vs, WS_STAT);
    if (ST) asm volatile("s_waitcnt vmcnt(0)" : "+v"(a), "+v"(b), "+v"(st)::"memory"); else asm volatile("s_waitcnt vmcnt(0)" : "+v"(a), "+v"(b)::"memory");
  }
}

DI unsigned umax4(unsigned m, const u32x4& v) { return max(max(max(m, v[0]), v[1]), max(v[2], v[3])); }
DI bool fresh(unsigned m) { return __builtin_amdgcn_ballot_w64(m == 0xffffffffu) == 0ull; }

struct Sm { float L, cx, cn, mx, mn, s1, s2; f32x16 p, ep, en; };
DI void sm_max(Sm& s, float ninf) {
  const f32x16& p = s.p;
  float a = max3(p[0], p[1], p[2]);
  for (int i = 3; i < 11; i += 2) a = max3(a, p[i], p[i + 1]);
  s.mx = max3(a, p[11], max3(max3(p[12], p[13], p[14]), p[15], p[15]) + ninf);
}
DI void sm_min(Sm& s, float ninf) {
  const f32x16& p = s.p;
  float b = min3(p[0], p[1], p[2]);
  for (int i = 3; i < 11; i += 2) b = min3(b, p[i], p[i + 1]);
  s.mn = min3(b, p[11], min3(min3(p[12], p[13], p[14]), p[15], p[15]) - ninf);
}
DI void sm_consts(Sm& s, float sc) {
  auto r = __builtin_amdgcn_permlane32_swap(__float_as_uint(s.mx), __float_as_uint(-s.mn), false, false);
  const float m = max3(__uint_as_float(r[0]), __uint_as_float(r[1]), __uint_as_float(r[1]));
  float mx, nmn; bcast32(m, mx, nmn);
  s.L = sc * 1.4426950408889634f; s.cx = -mx * s.L; s.cn = -nmn * s.L; s.s1 = 0.f; s.s2 = 0.f;
}
DI void sm_exp(Sm& s, int i, float ninf) {
  if (i == 12) { s.cx += ninf; s.cn += ninf; }
  s.ep[i] = __builtin_amdgcn_exp2f(fmaf(s.p[i], s.L, s.cx)); s.en[i] = __builtin_amdgcn_exp2f(fmaf(s.p[i], -s.L, s.cn));
  if (i > 0) { s.s1 = vadd(s.s1, s.ep[i - 1]); s.s2 = vadd(s.s2, s.en[i - 1]); }
}
DI void sm_sum(Sm& s, float os) {
  const float t = add32(vadd(s.s1, s.ep[15]), vadd(s.s2, s.en[15]));
  bcast32(os * __builtin_amdgcn_rcpf(t), s.s1, s.s2);
}
DI void sm_norm2(const Sm& s, int f, int h, u32x4& u) {
  const f32x16& e = f < 2 ? s.ep : s.en; const float r = f < 2 ? s.s1 : s.s2; const int o = 8 * (f & 1);
  for (int i = 2 * h; i < 2 * h + 2; i++) u[i] = pk2(vmul(e[o + 2 * i], r), vmul(e[o + 2 * i + 1], r));
}
DI u32x4 sm_norm(const Sm& s, int f) {
  const f32x16& e = f < 2 ? s.ep : s.en; const float r = f < 2 ? s.s1 : s.s2; const int o = 8 * (f & 1);
  u32x4 u;
  for (int i = 0; i < 4; i++) u[i] = pk2(vmul(e[o + 2 * i], r), vmul(e[o + 2 * i + 1], r));
  return u;
}

struct P2 {
  static constexpr int a0(int s) { return s <= 1 ? 7 + s : s <= 3 ? 8 : s + 5 < 18 ? s + 5 : 18; }
  static constexpr int vs(int j) { return j + 1; }
  static constexpr int vj(int s) { for (int j = 0; j < 8; j++) if (vs(j) == s) return j; return -1; }
  static constexpr int FL2 = 2, N2 = 7;
  static constexpr bool bar(int c) { return c == 1 || c == 3 || c == 5 || c == 7 || c == 12; }
  static constexpr int vneed(int c) { return c == 5 ? 9 : c == 7 ? 12 : c == 12 ? 17 : -1; }
  static constexpr int na(int s) { return 2 * (a0(s + 1) - a0(s)); }
  static constexpr int nops(int s) { return (vj(s) >= 0 ? 2 : 0) + na(s) + (s == 13 || s == FL2 ? 1 : 0); }
  static constexpr int upto(int from, int c) { int n = 0; for (int s = from; s < c; s++) n += nops(s); return n; }
  static constexpr int wait(int c) {
    int n = 63;
    if (c >= 4 && c < 7) n = 2 * (6 - c) + upto(0, c);
    for (int s = 0; s < c; s++) if (a0(s) <= c && c < a0(s + 1)) n = 2 * (a0(s + 1) - 1 - c) + upto(s + 1, c);
    if (c == N2) { const int f = upto(FL2, c) - 1; if (f < n) n = f; }
    if (vneed(c) >= 10) { const int s = vs(vneed(c) - 10), v = na(s) + upto(s + 1, c); if (v < n) n = v; }
    return n > 63 ? 63 : n;
  }
};

__global__ __launch_bounds__(512, 2) void fused_kernel(const char* __restrict__ x, const char* __restrict__ wqkv, const char* __restrict__ wphi,
                                                       const char* __restrict__ wo, const char* __restrict__ wg, const char* __restrict__ wu,
                                                       const char* __restrict__ wd, char* __restrict__ out, char* ws) {
  const int lane = threadIdx.x & 63, w = __builtin_amdgcn_readfirstlane(threadIdx.x >> 6);
  const int bx = blockIdx.x, xcd = bx & 7, r = bx >> 3;
  const int tg = xcd & 3, m = (xcd >> 2) * 8 + (r & 7), ts = r >> 3;
  const int g16 = (lane >> 4) & 1, q4 = (lane & 15) >> 2, p4 = lane & 3, l31 = lane & 31, hb = lane >> 5, rl = lane >> 3;
  STAMP(0); STAMPC(14); TK(0);
  unsigned long long tmark = 0;
#define MARK(i) do { if (MK == (i)) { SB(); asm volatile("s_memrealtime %0\n\ts_waitcnt lgkmcnt(0)" : "=s"(tmark) :: "memory"); SB(); } } while (0)
  unsigned st0, st1;
  const unsigned ldsw = w * 2048;
  const unsigned voffX = (16 * w + rl) * 896 + (((lane & 7) ^ rmask(16 * w + rl)) << 4), voffX2 = voffX + 8 * 896 - 1024;
  const rsrc_t rsX = mkrs(x + (size_t)m * 128 * 896, 128 * 896);
  const int rot = ((m + ts) & 1) * 4;
  unsigned rx[7];
#pragma unroll
  for (int c = 0; c < 7; c++) rx[c] = (c + rot >= 7 ? c + rot - 7 : c + rot) * 128;
#define ITEM_X(c, h) do { if (!(h)) DMA1P(voffX, rsX, ldsw, (7 + ((c) + 2) % 3) * SLOT, rx[c], 0, st0); else DMA2(voffX2, rsX, st0); } while (0)
#define ITEM_V(c, h) do { if (!(h)) DMA1I(voffX, rsV, ldsw, (((c) + 7) % 10) * SLOT, (c) * (128 * 896), st0); else DMA2(voffX2, rsV, st0); } while (0)
  ITEM_X(0, 0); ITEM_X(0, 1);

  const bool isf = ts < 2, isq = ts == 3;
  const int fi = 2 * tg + (ts & 1);
  const int rb = w & 3, role = w >> 2;
#define VOFF(f) ((16 * rb + rl) * ((f) ? 3584 : 2688) + (usel(f, (((((lane & 7) >> 1) ^ ((rl >> 1) & 1) ^ (role << 1)) << 1) | (lane & 1)), min((lane & 7) ^ (rl << 1 & 4), 6)) << 4))
  const char* wgu = psel(role, wu, wg);
  unsigned voffW = VOFF(isf), wmul = isf ? 1792 : 1344;
  unsigned voffW2 = voffW + wmul * 16 - 1024;
  rsrc_t rsW = mkoff(psel(isf, wgu, wqkv), fi * (isf ? 128 : 112) + (isf ? 0 : 896 + 896 * role), wmul * 896);
#define ITEM_W(c, h) do { if (!(h)) DMA1M(voffW, rsW, ldsw, (c) * SLOT, rx[c], wmul, st1); else DMA2(voffW2, rsW, st1); } while (0)
  ITEM_W(0, 0); ITEM_W(0, 1); ITEM_X(1, 0); ITEM_X(1, 1); ITEM_W(1, 0); ITEM_W(1, 1);

  const rsrc_t rs = mkrs(ws, WS_BYTES);
  unsigned tag;
  const int pu = (xcd >> 2) * 32 + r, li = pu / 3;
  const int la = li >= 6, lj = li - 6 * la, ll = 2 * tg + (li & 1);
  const int lc = li < 12 ? 21 + 28 * (lj & 1) + 8 * (lj >> 1) + 2 * tg + la : li < 14 ? 45 + 28 * (li & 1) + tg : 7 * ((li - 14) >> 1) + ll;
  const int cpart = li >= 14 && (ll > 6 || li > 19) ? 3 : pu - 3 * li;
  const int srow = (xcd * 32 + r) * 8 + w;
  const int tidx = (xcd >> 1) * 56 + (xcd & 1) * 32 + r;
  const bool notile = isq && (xcd & 1);
  const int rt = notile ? 0 : tidx / 7, ct = notile ? 0 : tidx - 7 * rt;
  const rsrc_t rsV = mkrs(wd + ct * 128, notile ? 0 : 1792 * 896 - ct * 128);
  const rsrc_t rsP = mkrs(wphi + 4 * tg * 1568, isf ? 0 : 4 * 1568);
  const unsigned mlo = lane < 32 ? 0xffffffffu : 0u;
  const unsigned lp = w * 1024;

  const int rbg = m * 4 + rb;
  const int rb2 = w & 1, kq = w >> 1;
#define VFL (((rt * 2 + rb2) * 2 + (kq & 1)) * 128 + l31 * 4)
#define AB (((rt * 2 + rb2) * 144 + 2 * kq) * 1024 + lane * 16)
  u32x4 af[36];
#define LDA2(c) asm volatile("s_mov_b32 %2, %5\n\tbuffer_load_dwordx4 %0, %3, %4, %2 offen sc1\n\tbuffer_load_dwordx4 %1, %3, %4, %2 offen offset:1024 sc1" \
    : "=&v"(af[2 * (c)]), "=&v"(af[2 * (c) + 1]), "=&s"(st1) : "v"(AB), "s"(rs), "n"(WS_AFRAG + (c) * 8192) : "memory")
#define PIN8(c) "+v"(af[2 * (c)]), "+v"(af[2 * (c) + 1]), "+v"(af[2 * (c) + 2]), "+v"(af[2 * (c) + 3]), "+v"(af[2 * (c) + 4]), "+v"(af[2 * (c) + 5]), "+v"(af[2 * (c) + 6]), "+v"(af[2 * (c) + 7])
#define NEED(mask) do { if (!seen(fl, tag, mask)) poll(ws, WS_AFLAG + VFL, mask, tag); } while (0)
#define ONES (u32x4{~0u, ~0u, ~0u, ~0u})
  f32x16 pa0, pa1;
  float rstd;
  unsigned fl = 0;

  const unsigned xrow = 32 * rb + l31;
  const unsigned vst = (m * 128 + xrow) * 16;
  s16x8 xf[28], wq[3][2], fb, wf[2], wfq[2][2];
  u32x4 stt, ca, cb;
#define AW_F() (g16 * 8192 + (8 * hb + q4) * 128 + (((2 * role) ^ ((q4 >> 1) & 1) ^ (g16 << 1)) * 32) + p4 * 8)
#define AW_A(half, sel) ((half) * 8192 + (8 * hb + q4) * 128 + ((56 * (sel) + 32 * g16 + 8 * p4) ^ (q4 << 5 & 64)))
#define VCS(f, col) ((g16 * ((f) ? 1792 : 16) + (lane & 15)) * 32 + hb * 16 + (col) * 32)
  unsigned aW[4], vca, vcb;
  aW[0] = usel(isf, AW_F(), AW_A(0, role)); aW[1] = aW[0] ^ (isf ? 32 : 8192); aW[2] = aW[0] + 65536; aW[3] = aW[1] + 65536;
  OPAQUE(aW[0]); OPAQUE(aW[1]); OPAQUE(aW[2]); OPAQUE(aW[3]);
  auto fold = [&](u32x4 c) { const unsigned c2 = c[2] & mlo; u32x4 u = {c[0], c2, c[0], c2}; return asf(u); };
  auto make_fb = [&]() { const unsigned a = stt[0] & 0xffffu, b = stt[0] >> 16; u32x4 u = {a | (a << 16), a | (a << 16), b | (b << 16), b | (b << 16)}; fb = asf(u); rstd = __uint_as_float(stt[2]); };
  auto wfrag = [&](int n, int t) {
    const int sl = (n >> 2) % 7;
    const unsigned o = (sl & 3) * SLOT + (n & 3) * 2048, b = aW[(sl >> 2) * 2 + t];
    return trfrag(b + o, b + o + 512);
  };

  {
    u32x4 px, pw[3];
    TK(1);
    unsigned aX[4];
#pragma unroll
    for (int ks = 0; ks < 4; ks++) { aX[ks] = 7 * SLOT + xrow * 128 + (((2 * ks + hb) ^ rmask(xrow)) << 4); OPAQUE(aX[ks]); }
    auto xread = [&](int n) { xf[n] = *(LAS s16x8*)(aX[n & 3] + (((n >> 2) + 2) % 3) * SLOT); };
    f32x16 acc0 = zero16(), acc1 = zero16();
    const unsigned m56 = lane < 56 ? 0xffffffffu : 0u;
    float cs[8], s = 0.f, q = 0.f; unsigned pfd;
    unsigned voffN = VOFF(!isq); OPAQUE(voffN);
    WAITV(4);
    BAR();
    TK(4); MARK(1);
    xread(0); wq[0][1] = wfrag(0, 1); wq[0][0] = wfrag(0, 0);
    xread(1); wq[1][1] = wfrag(1, 1); wq[1][0] = wfrag(1, 0);
    ITEM_X(2, 0); ITEM_X(2, 1);
    auto pre = [&](int p) {
      if (p == 2) {
        asm volatile("buffer_load_dword %0, off, %1, 0 sc1" : "=v"(tag) : "s"(rs) : "memory");
        asm volatile("global_load_dwordx4 %0, %1, %2" : "=v"(px) : "v"(min(lane, 55) * 16), "s"(x + (size_t)srow * 896) : "memory");
      }
      if (p == 3) {
        const char* cb = psel(lc < 21, wqkv, psel(lc < 49, wg, wu)) + (lc - (lc < 21 ? 0 : lc < 49 ? 21 : 49)) * 128;
        const unsigned pitch = lc < 21 ? 2688 : 3584;
        const unsigned vo = rl * pitch + (lane & 7) * 16;
#pragma unroll
        for (int i = 0; i < 3; i++) {
          const int row = min(152 * cpart + 8 * (w + 8 * i), 440);
          asm volatile("global_load_dwordx4 %0, %1, %2" : "=v"(pw[i]) : "v"(vo), "s"(cb + (size_t)row * pitch) : "memory");
        }
      }
      if (p == 12) { tag += 1; for (int i = 0; i < 4; i++) px[i] &= m56; }
      if (p == 13) { for (int i = 0; i < 4; i++) { s = dot2(px[i], 0x3f803f80u, s); q = dot2(px[i], px[i], q); } OPAQUE(s); OPAQUE(q); }
      if (p == 14) { s = add32(s, q); s += dpp<0xB1>(s); OPAQUE(s); }
      if (p == 15) { s += dpp<0x4E>(s); s += dpp<0x141>(s); OPAQUE(s); }
      if (p == 16) { s += dpp<0x128>(s); s = add16(s, s); OPAQUE(s); }
      if (p == 17) { bcast32(s * (1.f / 448.f), s, q); q = fmaxf(fmaf(-s, s, q), 0.f) + 1e-5f; OPAQUE(s); OPAQUE(q); }
      if (p == 18) {
        u32x4 o = {split2(-s), tag, __float_as_uint(__builtin_amdgcn_rsqf(q)), tag};
        st16(rs, lane == 0 ? WS_STAT + srow * 16 : 0xfffffff0u, o);
      }
      if (p >= 15 && p < 23) {
        const int i = (p - 15) >> 1;
        if (p & 1) { pw[2][i] &= (w + 16 < (cpart == 2 ? 18 : 19) ? 0xffffffffu : 0u); cs[2 * i] = vadd(vadd(blo(pw[0][i]), blo(pw[1][i])), blo(pw[2][i])); }
        else cs[2 * i + 1] = vadd(vadd(bhi(pw[0][i]), bhi(pw[1][i])), bhi(pw[2][i]));
      }
      if (p == 23 || p == 24) for (int i = 2 * (p - 23); i < 2 * (p - 22); i++) { cs[i] = add32(cs[i], cs[i + 4]); OPAQUE(cs[i]); }
      if (p == 25) for (int i = 0; i < 2; i++) { cs[i] = add16(cs[i], cs[i + 2]); OPAQUE(cs[i]); }
      if (p == 26) for (int i = 0; i < 2; i++) { cs[i] += dpp<0x128>(cs[i]); OPAQUE(cs[i]); }
      if (p == 27) {
        *(LAS f32x2*)(LDS_MISC + w * 256 + ((lane & 7) * 8 + hb * 4 + g16 * 2) * 4) = f32x2{cs[0], cs[1]};
        *(LAS u32x2*)(LDS_PHI + 4 * 1568 + lane * 8) = u32x2{0u, 0u};
      }
    };
    sfor<0, 28>([&](auto nn) {
      constexpr int n = decltype(nn)::value;
      constexpr int c = (n + 2) >> 2, j = (n + 2) & 3;
      if (j == 0) {
        if (c == 2) asm volatile("s_waitcnt vmcnt(7)" : "+v"(tag), "+v"(px), "+v"(pw[0]), "+v"(pw[1]), "+v"(pw[2])::"memory");
        if (c == 3) WAITV(7); if (c == 4) WAITV(6); if (c == 1 || c == 5 || c == 6) WAITV(9);
        if (c == 7) asm volatile("s_waitcnt vmcnt(4)" :: "v"(pfd) : "memory");
        BAR();
        TK(4 + c);
      }
      if (n == 14 && w == 7 && cpart < 3) {
        float t = 0.f;
        for (int i = 0; i < 8; i++) t += *(LAS float*)(LDS_MISC + i * 256 + lane * 4);
        u32x2 o = {split2(t), tag};
        __builtin_amdgcn_raw_buffer_store_b64(o, rs, WS_CS + (lc * 64 + lane) * 32 + cpart * 8, 0, 16);
      }
#define P0_ISSUE(h) do { \
      if (n == 0 || n == 3) ITEM_W(n == 0 ? 2 : 3, h); \
      if (c >= 1 && c <= 4 && j == 0) ITEM_X(c + 2, h); \
      if (c >= 1 && c <= 3 && j == 2) ITEM_W(c + 3, h); \
      if (c == 4 && j >= 2) ITEM_W(j - 1, h); \
      if (c == 5 && j == 0) ITEM_W(0, h); \
      if (c == 5 && j == 2) ITEM_W(3, h); \
      if (c == 6 && j == 0) ITEM_W(4, h); } while (0)
      SB();
      if (n == 27) pa0 = MFMA(wq[n % 3][0], xf[n], acc0); else acc0 = MFMA(wq[n % 3][0], xf[n], acc0);
      SB();
      if (n + 2 < 28) xread(n + 2);
      wq[(n + 2) % 3][1] = wfrag(n + 2, 1);
      pre(2 * n);
      P0_ISSUE(0);
      if (n == 5) DMA1I(lp + lane * 16, rsP, lp, LDS_PHI, 0, st0);
      if (n == 15) {
        const int u = bx * 8 + w;
        const rsrc_t rsF = mkrs(psel(u < 1568, wd, wo) + (u < 1568 ? u : u - 1568) * 1024, u < 1960 ? 1024 : 0);
        asm volatile("buffer_load_dword %0, %1, %2, 0 offen" : "=v"(pfd) : "v"((lane & 7) * 128), "s"(rsF) : "memory");
      }
      if (n == 23) { vca = VCS(isf, fi * (isf ? 64 : 56) + role * (isf ? 32 : 28) + (isf ? 1344 : 448)); vcb = vca + (isf ? 512 : 448 * 32); }
      if (n == 26) ALD16(ca, vca, WS_CS);
      if (n == 27) ALD16(stt, vst, WS_STAT);
      SB();
      if (n == 27) pa1 = MFMA(wq[n % 3][1], xf[n], acc1); else acc1 = MFMA(wq[n % 3][1], xf[n], acc1);
      SB();
      wq[(n + 2) % 3][0] = wfrag(n + 2, 0);
      pre(2 * n + 1);
      P0_ISSUE(1);
      if (n == 14) {
        wmul = isq ? 1344 : 1792;
        rsW = mkoff(psel(isq, wqkv, wgu), usel(isq, 224 * tg + 112 * role, usel(isf, 8 + fi, 24 + tg) * 128), wmul * 896);
        voffW = voffN; voffW2 = voffW + wmul * 16 - 1024;
      }
      if (n == 15) {
        const unsigned pa = LDS_PHI + (2 * (ts & 1) + role) * 1568 + (4 * hb + q4) * 56 + g16 * 32 + p4 * 8;
        wf[0] = trfrag(pa, pa + 448); wf[1] = trfrag(pa + 896, pa + 1344);
      }
      if (n == 16 || n == 17) {
        const unsigned pa = LDS_PHI + (2 * role + n - 16) * 1568 + (4 * hb + q4) * 56 + g16 * 32 + p4 * 8;
        wfq[n - 16][0] = trfrag(pa, pa + 448); wfq[n - 16][1] = trfrag(pa + 896, pa + 1344);
      }
      if (n == 26) ALD16(cb, vcb, WS_CS);
      SB();
    });
    wq[0][0] = wq[1][0]; wq[0][1] = wq[1][1]; wq[1][0] = wq[2][0]; wq[1][1] = wq[2][1];
    STAMP(1); TK(12); MARK(2);
  }

  float k0, k1, ia2;
  auto gconst = [&]() {
    const float c0 = -2.f * 0.7978845608028654f * 1.4426950408889634f;
    k0 = vmul(c0, rstd); k1 = vmul(c0 * 0.044715f * rstd * rstd, rstd); ia2 = __builtin_amdgcn_rcpf(rstd * rstd);
  };
  auto passn = [&](auto kind) {
    constexpr int K = decltype(kind)::value;
    constexpr bool F = K == 1, L = K != 1, G = K != 3;
    FRESH_LANE();
    const unsigned hb0 = WS_AFRAG + (rbg * 144 + 2 * role) * 1024 + lane * 16;
    const unsigned ho = hb0 + (F ? fi : 8 + fi) * 4096;
    unsigned fo = 0;
    if (K == 3) {
      const unsigned f0 = w == 0 ? WS_CFLAG + (fi * 16 + m) * 64 : 0xffffff00u, f1 = WS_AFLAG + rbg * 256 + (isq ? 16 + 2 * tg + role : 8 + tg) * 4;
      const unsigned fa = isq || role == 0 ? f1 : 0xffffff00u, fb2 = isq || role == 1 ? f1 + 128 : 0xffffff00u;
      fo = lane == 0 ? f0 : 0xffffff00u; fo = lane == 1 ? fa : fo; fo = lane == 2 ? fb2 : fo;
      OPAQUE(fo);
    }
    const rsrc_t rsB = mkoff(wgu, (16 + fi) * 128, 448 * 3584);
    f32x16 acc0 = zero16(), acc1 = zero16();
    f32x16 qa0, qa1;
    float gt[2], gu[2], hprev; u32x4 hq;
    auto geglu = [&](int hs) {
#pragma unroll
      for (int e = 0; e < 16; e++) {
        const int d = hs - ((F ? 12 : 4) + 12 * e / 5);
        const float Gv = e < 8 ? qa0[e & 7] : qa1[e & 7], U = e < 8 ? qa0[(e & 7) + 8] : qa1[(e & 7) + 8];
        if (d == 0) gt[e & 1] = vmul(Gv, fmaf(Gv * Gv, k1, k0));
        if (d == 1) { gt[e & 1] = __builtin_amdgcn_exp2f(gt[e & 1]); gu[e & 1] = vmul(Gv, U); }
        if (d == 2) gt[e & 1] = __builtin_amdgcn_rcpf(fmaf(gt[e & 1], ia2, ia2));
        if (d == 3) {
          if (e & 1) hq[(e >> 1) & 3] = pk2(hprev, gu[1] * gt[1]); else hprev = vmul(gu[0], gt[0]);
          if ((e & 7) == 7) st16(rs, ho + (e >> 3) * 1024, hq);
        }
      }
    };
    sfor<0, 28>([&](auto nn) {
      constexpr int n = decltype(nn)::value;
      if (K == 2 && n == 7) { WAITV(4); st4(rs, lane == 0 ? WS_AFLAG + (rbg * 2 + role) * 128 + fi * 4 : 0xffffff00u, tag); }
      if (n == 10) { if (K == 3) WAITV(8); BAR(); TK(K == 1 ? 13 : 17); if (K == 3) st4(rs, fo, tag); }
      if (n == 18) {
        asm volatile("s_waitcnt vmcnt(%2)" : "+v"(ca), "+v"(cb) : "n"(8) : "memory"); BAR(); TK(K == 1 ? 14 : 18);
        if (K == 3 && notile) {
          const int gg = tg - 1 + role;
          poll(ws, WS_CFLAG + ((2 * gg + (m >> 3)) * 16 + (lane & 15)) * 64, ~0ull, tag);
          const unsigned pb = WS_KVPART + ((gg * 8 + (m >> 1)) * 16 + (lane >> 4)) * 2048 + (m & 1) * 1024 + (rb * 16 + (lane & 15)) * 16;
          u32x4 pv[4];
#pragma unroll
          for (int i = 0; i < 4; i++) pv[i] = ld16(rs, pb + i * 8192);
          float t[8];
#pragma unroll
          for (int j = 0; j < 4; j++) {
            t[2 * j] = (blo(pv[0][j]) + blo(pv[1][j])) + (blo(pv[2][j]) + blo(pv[3][j]));
            t[2 * j + 1] = (bhi(pv[0][j]) + bhi(pv[1][j])) + (bhi(pv[2][j]) + bhi(pv[3][j]));
          }
#pragma unroll
          for (int j = 0; j < 8; j++) { t[j] = add32(t[j], t[j]); t[j] = add16(t[j], t[j]); }
          st16(rs, lane < 16 ? WS_KVRED + (gg * 16 + m) * 1024 + (rb * 16 + lane) * 16 : 0xf0000000u, u32x4{pk2(t[0], t[1]), pk2(t[2], t[3]), pk2(t[4], t[5]), pk2(t[6], t[7])});
          STAMP(6);
        }
      }
      if (L && n == 22) asm volatile("s_waitcnt vmcnt(%1)" : "+v"(fl) : "n"(K == 3 ? 3 : 4) : "memory");
      if (n == 26) { if (F) WAITV(10); BAR(); TK(K == 1 ? 15 : 19); }
      if (G && n == (F ? 4 : 0)) {
        if (F) asm volatile("s_waitcnt vmcnt(5)" : "+v"(ca), "+v"(cb), "+v"(stt)::"memory");
        g_check<F>(rs, ca, cb, stt, vca, vcb, vst, tag, mlo);
      }
      SB();
      if (n == 27) pa0 = MFMA(wq[n % 3][0], xf[n], acc0); else acc0 = MFMA(wq[n % 3][0], xf[n], acc0);
      SB();
      if (!L || n + 2 < 28) wq[(n + 2) % 3][1] = wfrag(n + 2, 1);
      if (F && n == 4) make_fb();
      if (G && n == (F ? 5 : 1)) qa0 = MFMA(fold(ca), fb, pa0);
      if (G && n < 4) ITEM_W(5 + n / 2, n & 1);
      if (F && n == 4) rsW = rsB;
      if (F && n >= 4 && n < 8) ITEM_V((n - 4) / 2, n & 1);
      if (F && (n == 23 || n == 24)) ITEM_V(2, !(n & 1));
      if (K == 3 && n < 6) ITEM_V(n / 2, n & 1);
      if (L && n >= 10 && n < 16) ITEM_V((n - 4) / 2, n & 1);
      if (L && (n == 18 || n == 19)) ITEM_V(6, n & 1);
      if (L && (n == 21 || n == 22)) ITEM_V(7, !(n & 1));
      if (L && n >= 26) ITEM_V(n - 18, 0);
      if (F && n >= 10 && n < 16) ITEM_W((n - 10) / 2, n & 1);
      if (F && (n == 18 || n == 19)) ITEM_W(3, n & 1);
      if (F && (n == 21 || n == 22)) ITEM_W(4, !(n & 1));
      if constexpr (L && n >= 22 && n < 26) LDA2(n - 22);
      if (G) geglu(2 * n);
      SB();
      if (n == 27) pa1 = MFMA(wq[n % 3][1], xf[n], acc1); else acc1 = MFMA(wq[n % 3][1], xf[n], acc1);
      SB();
      if (!L || n + 2 < 28) wq[(n + 2) % 3][0] = wfrag(n + 2, 0);
      if (F && n == 4) gconst();
      if (G && n == (F ? 5 : 1)) qa1 = MFMA(fold(cb), fb, pa1);
      if (F && (n == 0 || n == 7 || n == 17 || n == 20)) st16(rs, ho + (n < 8 ? 8 : 16) * 4096 + (n & 1) * 1024, ONES);
      if (L && n == 17) ALD4(fl, VFL, WS_AFLAG);
      if (n == 7) {
        if (G) { vca += 8 * 2048; vcb += 8 * 2048; }
        else { vca = VCS(!isq, tg * (isq ? 112 : 64) + role * (isq ? 56 : 32) + (isq ? 0 : 1344 + 24 * 64)); vcb = vca + (isq ? 28 * 32 : 512); }
      }
      if (n == 8) ALD16(ca, vca, WS_CS);
      if (n == 9) ALD16(cb, vcb, WS_CS);
      if (L && n >= 26) ITEM_V(n - 18, 1);
      if (G) geglu(2 * n + 1);
      if (n < 27) asm volatile("" :: "v"(acc0), "v"(acc1));
      SB();
    });
    if (!L) { wq[0][0] = wq[1][0]; wq[0][1] = wq[1][1]; wq[1][0] = wq[2][0]; wq[1][1] = wq[2][1]; }
    STAMP(F ? 2 : 3); TK(K == 1 ? 16 : 20); if (K == 1) MARK(3); if (K == 2) MARK(4);
  };

  const float ninf = __uint_as_float(~mlo & 0xff800000u);
  if (!isf) {
    FRESH_LANE();
    ITEM_W(5, 0); ITEM_W(5, 1); ITEM_W(6, 0); ITEM_W(6, 1);
    asm volatile("s_waitcnt vmcnt(4)" : "+v"(ca), "+v"(cb), "+v"(stt)::"memory");
    g_check<true>(rs, ca, cb, stt, vca, vcb, vst, tag, mlo);
    make_fb();
    pa0 = MFMA(fold(ca), fb, pa0); pa1 = MFMA(fold(cb), fb, pa1);
    Sm sm;
    u32x4 kf1 = pack8u(pa0, 1); kf1[2] &= mlo; kf1[3] &= mlo;
    sm.p = MFMA(wf[0], asf(pack8u(pa0, 0)), zero16());
    sm.p = MFMA(wf[1], asf(kf1), sm.p);
    const unsigned wa = 7 * SLOT + (role * 128 + 32 * rb + l31) * 192 + ((hb ^ (l31 >> 1 & 7)) << 3);
#define IMG(f, v) do { *(LAS u32x2*)((wa ^ ((f) & 1) * 32) + ((f) >> 1) * 64) = u32x2{v[0], v[1]}; *(LAS u32x2*)((wa ^ (((f) & 1) * 32 + 16)) + ((f) >> 1) * 64) = u32x2{v[2], v[3]}; } while (0)
    { u32x4 v0 = pack8u(pa1, 0), v1 = pack8u(pa1, 1); v1[2] = (v1[2] & mlo) | (split2(__builtin_amdgcn_rcpf(rstd)) & ~mlo); v1[3] &= mlo; IMG(4, v0); IMG(5, v1); }
    sm_max(sm, ninf); sm_min(sm, ninf); sm_consts(sm, rstd);
    const unsigned oc = WS_AFRAG + (rbg * 144 + usel(isq, 112 + 8 * tg + 4 * role, 96 + 4 * tg + 2 * role)) * 1024 + lane * 16;
#pragma unroll
    for (int i = 0; i < 16; i++) {
      sm_exp(sm, i, ninf);
      if ((i & 3) == 0) { st16(rs, oc + (i < 8 || isq ? (i >> 2) * 1024 : 0xe0000000u), ONES); SB(); }
    }
    st16(rs, (notile ? WS_KVRED + ((tg - 1 + role) * 16 + m) * 1024 + rb * 256 : 0xf0000000u) + (lane < 16 ? lane * 16 : 0x8000000u), ONES);
    sm_sum(sm, rstd);
#pragma unroll
    for (int f = 0; f < 4; f++) { const u32x4 v = sm_norm(sm, f); IMG(f, v); }
    BAR();
    TK(13);
    if (w < 4) {
      unsigned ra = 7 * SLOT + (w >> 1) * 24576 + (8 * hb + q4) * 192 + (((4 * g16 + p4) ^ (4 * hb + (q4 >> 1))) << 3), rc = (ra ^ 16) + 768;
      unsigned rp = ra + (w & 1) * 64, rq = rc + (w & 1) * 64;
      OPAQUE(ra); OPAQUE(rc); OPAQUE(rp); OPAQUE(rq);
      f32x16 kv = zero16();
#pragma unroll
      for (int s = 0; s < 8; s++) kv = MFMA(trfrag(rp + s * 3072, rq + s * 3072), trfrag(ra + 128 + s * 3072, rc + 128 + s * 3072), kv);
      st16(rs, WS_KVPART + ((fi * 4 + w) * 16 + m) * 2048 + lane * 16, pack8u(kv, 0));
      st16(rs, WS_KVPART + ((fi * 4 + w) * 16 + m) * 2048 + 1024 + lane * 16, pack8u(kv, 1));
    }
    STAMP(2); TK(14);
    aW[0] = usel(isq, AW_A(role, 0), AW_F()); aW[1] = usel(isq, AW_A(role, 1), aW[0] ^ 32); aW[2] = aW[0] + 65536; aW[3] = aW[1] + 65536;
    OPAQUE(aW[0]); OPAQUE(aW[1]); OPAQUE(aW[2]); OPAQUE(aW[3]);
    wq[0][1] = wfrag(0, 1); wq[0][0] = wfrag(0, 0); wq[1][1] = wfrag(1, 1); wq[1][0] = wfrag(1, 0);
    BAR();
    TK(15);
    passn(IC<3>{});
    gconst();
#pragma unroll
    for (int i = 0; i < 28; i++) asm volatile("" : "=v"(xf[i]));
    asm volatile("" : "=v"(wq[0][0]), "=v"(wq[0][1]), "=v"(wq[1][0]), "=v"(wq[1][1]), "=v"(stt));
  }
  int tso = ts; asm volatile("" : "+s"(tso));
  if (tso < 2) { passn(IC<1>{}); passn(IC<2>{}); asm volatile("" : "=v"(wfq[0][0]), "=v"(wfq[0][1]), "=v"(wfq[1][0]), "=v"(wfq[1][1])); }

  TK2(0);
  g_check<false>(rs, ca, cb, stt, vca, vcb, vst, tag, mlo);
  pa0 = MFMA(fold(ca), fb, pa0); pa1 = MFMA(fold(cb), fb, pa1);
  const bool early = seen(fl, tag, 0xffull);
  if (tso != 3) {
    FRESH_LANE();
    const unsigned hs = WS_AFRAG + (rbg * 144 + (isf ? 16 + fi : 24 + tg) * 4 + 2 * role) * 1024 + lane * 16;
    u32x4 o[2];
#pragma unroll
    for (int t = 0; t < 2; t++) {
      const f32x16& a = t ? pa1 : pa0;
      float hv[8];
#pragma unroll
      for (int i = 0; i < 8; i++) {
        const float e = __builtin_amdgcn_exp2f(a[i] * fmaf(a[i] * a[i], k1, k0));
        hv[i] = (a[i] * a[i + 8]) * __builtin_amdgcn_rcpf(fmaf(e, ia2, ia2));
      }
      o[t] = u32x4{pk2(hv[0], hv[1]), pk2(hv[2], hv[3]), pk2(hv[4], hv[5]), pk2(hv[6], hv[7])};
      OPAQUE(o[t]); SB();
    }
    st16(rs, hs, o[0]); st16(rs, hs + 1024, o[1]);
  } else {
    FRESH_LANE();
    unsigned cf;
    const unsigned vcf = ((2 * (tg | 1) + 1) * 16 + (lane & 15)) * 64;
    ALD4(cf, vcf, WS_CFLAG);
    u32x4 ph[2][4];
    f32x16 pq[2];
#pragma unroll
    for (int u = 0; u < 2; u++) {
      u32x4 k1 = pack8u(u ? pa1 : pa0, 1); k1[2] &= mlo; k1[3] &= mlo;
      pq[u] = MFMA(wfq[u][0], asf(pack8u(u ? pa1 : pa0, 0)), zero16());
      pq[u] = MFMA(wfq[u][1], asf(k1), pq[u]);
    }
#pragma unroll
    for (int u = 0; u < 2; u++) {
      Sm sm; sm.p = pq[u];
      sm_max(sm, ninf); sm_min(sm, ninf); sm_consts(sm, rstd);
#pragma unroll
    for (int i = 0; i < 16; i++) sm_exp(sm, i, ninf);
      sm_sum(sm, 1.f);
#pragma unroll
      for (int f = 0; f < 4; f++) { ph[u][f] = sm_norm(sm, f); OPAQUE(ph[u][f]); }
    }
    STAMP(7);
    asm volatile("s_waitcnt vmcnt(0)" : "+v"(cf)::"memory");
    if (!seen(cf, tag, ~0ull)) poll(ws, WS_CFLAG + vcf, ~0ull, tag);
    u32x4 kf[8];
#pragma unroll 1
    for (int it = 0; it < SPIN_CAP; ++it) {
      unsigned mx = 0;
#pragma unroll
      for (int i = 0; i < 8; i++) kf[i] = ld16(rs, WS_KVRED + ((4 * tg + 2 * role) * 4 + i) * 1024 + lane * 16);
#pragma unroll
      for (int i = 0; i < 8; i++) mx = umax4(mx, kf[i]);
      if (fresh(mx)) break;
      __builtin_amdgcn_s_sleep(4);
    }
    STAMP(10);
#pragma unroll
    for (int u = 0; u < 2; u++) {
      f32x16 nm = zero16();
      for (int i = 0; i < 4; i++) nm = MFMA(asf(kf[4 * u + i]), asf(ph[u][i]), nm);
      float dl, dh; bcast32(nm[12] + nm[13], dl, dh);
      const float rd = __builtin_amdgcn_rcpf(dh + 1e-6f);
      for (int i = 0; i < 16; i++) nm[i] *= rd;
      u32x4 o1 = pack8u(nm, 1); o1[2] &= mlo; o1[3] &= mlo;
      const unsigned oo = WS_AFRAG + (rbg * 144 + 112 + (4 * tg + 2 * role + u) * 2) * 1024 + lane * 16;
      st16(rs, oo, pack8u(nm, 0));
      st16(rs, oo + 1024, o1);
    }
    STAMP(4);
    if (notile) {
      DUMPTK();
      if (bx == 24 * 8 + 1 && w == 0) {
#pragma unroll 1
        for (int i = 0; i < 8; i++) poll(ws, WS_AFLAG + (2 * i + hb) * 8 * 128 + l31 * 4, 0x00550fff00550fffull, tag);
        st4(rs, WS_EPOCH, tag);
      }
      return;
    }
  }
  TK2(3);
  asm volatile("s_waitcnt vmcnt(6)" : PIN8(0)::"memory");
  TK2(4);
  if (!early) {
    FRESH_LANE();
    poll(ws, WS_AFLAG + VFL, 0xffull, tag); LDA2(0); LDA2(1); LDA2(2); LDA2(3);
    asm volatile("s_waitcnt vmcnt(0)" : PIN8(0)::"memory");
  }

  {
    FRESH_LANE();
    const int tid = w * 64 + lane;
    const unsigned swz = (((lane & 7) ^ rmask(16 * w + rl)) << 4);
    STAMP(13); MARK(5);
    LDA2(4); LDA2(5); LDA2(6);
    const unsigned r0 = 32 * kq + 4 * hb + q4;
    unsigned bV[3][4];
#pragma unroll
    for (int i = 0; i < 12; i++) { bV[i >> 2][i & 3] = ((r0 * 128 + (((2 * g16 + (p4 >> 1)) ^ rmask(r0)) << 4) + (p4 & 1) * 8) ^ ((i & 1) * 64) ^ ((i & 2) * 16)) + (i >> 2) * 65536; OPAQUE(bV[i >> 2][i & 3]); }
    const unsigned voffO = (28 * (w >> 1) + 16 * (w & 1) + rl) * 896 + swz;
    const unsigned voffO2 = voffO + (((w & 1) && rl >= 4) ? 0 : 8 * 896) - 1024;
    const rsrc_t rsO = mkrs(wo + ct * 128, 448 * 896 - ct * 128);
    const size_t go = (size_t)(rt * 64 + (tid >> 3)) * 896 + ct * 128 + (tid & 7) * 16;
    u32x4 xr;
    f32x16 acc0 = zero16(), acc1 = zero16();
    s16x8 wv[2][4];
    auto vread = [&](int c) {
      const int sl = (c + 7) % 10; const unsigned so = (sl & 3) * SLOT;
      for (int i = 0; i < 4; i++) wv[c & 1][i] = trfrag(bV[sl >> 2][i] + so + (i >> 1) * 2048, bV[sl >> 2][i] + so + (i >> 1) * 2048 + 1024);
    };
    vread(0);
    sfor<0, 18>([&](auto cc) {
      constexpr int c = decltype(cc)::value;
      asm volatile("s_waitcnt vmcnt(%3)" : "+v"(af[2 * c]), "+v"(af[2 * c + 1]), "+v"(fl) : "n"(P2::wait(c)) : "memory");
      if constexpr (c >= 4) {
        constexpr int ce = c < 8 ? 8 : c < 12 ? 12 : c < 14 ? 14 : 18, c0 = ce - 4;
#pragma unroll 1
        for (int it = 0; it < SPIN_CAP; ++it) {
          if (__builtin_expect(fresh(umax4(umax4(0u, af[2 * c]), af[2 * c + 1])), 1)) break;
          WAITV(0);
          __builtin_amdgcn_s_sleep(4);
          LDA2(c); if (c < c0 + 3) LDA2(c0 + 3); if (c < c0 + 2) LDA2(c0 + 2); if (c < c0 + 1) LDA2(c0 + 1);
          asm volatile("s_waitcnt vmcnt(0)" : PIN8(c0)::"memory");
        }
        if (c == 8) { STAMP(11); TK(21); } if (c == 14) { STAMP(12); TK(22); }
      }
      if (P2::bar(c)) BAR();
      TK2(5 + c);
      if (c + 1 < 18) vread(c + 1);
      SB();
      acc0 = MFMA(wv[c & 1][0], asf(af[2 * c]), acc0); acc1 = MFMA(wv[c & 1][1], asf(af[2 * c]), acc1);
      if (c == P2::FL2) ALD4(fl, VFL, WS_AFLAG);
      constexpr int j = P2::vj(c);
      if (j >= 0 && j < 4) { ITEM_V(j + 10, 0); ITEM_V(j + 10, 1); }
      if (j >= 4) { DMA1I(voffO, rsO, ldsw, ((j + 7) % 10) * SLOT, (j - 4) * (112 * 896), st0); DMA2(voffO2, rsO, st0); }
      SB();
      acc0 = MFMA(wv[c & 1][2], asf(af[2 * c + 1]), acc0); acc1 = MFMA(wv[c & 1][3], asf(af[2 * c + 1]), acc1);
      if (c == P2::N2) NEED(0xff0f00ull);
      if constexpr (P2::a0(c) < P2::a0(c + 1)) LDA2(P2::a0(c));
      if constexpr (P2::a0(c) + 1 < P2::a0(c + 1)) LDA2(P2::a0(c) + 1);
      if constexpr (P2::a0(c) + 2 < P2::a0(c + 1)) LDA2(P2::a0(c) + 2);
      if constexpr (P2::a0(c) + 3 < P2::a0(c + 1)) LDA2(P2::a0(c) + 3);
      static_assert(P2::a0(c + 1) - P2::a0(c) <= 4, "schedule");
      if (c == 13) asm volatile("global_load_dwordx4 %0, %1, off" : "=v"(xr) : "v"(x + go) : "memory");
      SB();
    });
    STAMP(8);
    const unsigned wa = 5 * SLOT + (kq * 64 + rb2 * 32 + l31) * 272 + hb * 16;
#pragma unroll
    for (int gq = 0; gq < 4; gq++) {
      f32x4 v0 = {acc0[4 * gq], acc0[4 * gq + 1], acc0[4 * gq + 2], acc0[4 * gq + 3]};
      f32x4 v1 = {acc1[4 * gq], acc1[4 * gq + 1], acc1[4 * gq + 2], acc1[4 * gq + 3]};
      *(LAS f32x4*)(wa + gq * 32) = v0; *(LAS f32x4*)(wa + 128 + gq * 32) = v1;
    }
    BAR();
    const unsigned ra = 5 * SLOT + (tid >> 3) * 272 + (tid & 7) * 32;
    f32x4 s0 = *(LAS f32x4*)ra, s1 = *(LAS f32x4*)(ra + 16);
#pragma unroll
    for (int k = 1; k < 4; k++) { s0 += *(LAS f32x4*)(ra + k * 64 * 272); s1 += *(LAS f32x4*)(ra + k * 64 * 272 + 16); }
    asm volatile("s_waitcnt vmcnt(0)" : "+v"(xr)::"memory");
    u32x4 o;
    o[0] = pk2(s0[0] + blo(xr[0]), s0[1] + bhi(xr[0]));
    o[1] = pk2(s0[2] + blo(xr[1]), s0[3] + bhi(xr[1]));
    o[2] = pk2(s1[0] + blo(xr[2]), s1[1] + bhi(xr[2]));
    o[3] = pk2(s1[2] + blo(xr[3]), s1[3] + bhi(xr[3]));
    __builtin_amdgcn_raw_buffer_store_b128(o, mkrs(out, 2048 * 896), (unsigned)go, 0, 16);
    if (isf) MARK(6);
    while (__builtin_amdgcn_s_memrealtime() - tmark < 4000) __builtin_amdgcn_s_sleep(1);
    STAMP(9); STAMPC(15); TK(23); TK2(23); DUMPTK();
  }
}

extern "C" void kernel_launch(void* const* d_in, const int* in_sizes, int n_in, void* d_out, int out_size, void* d_ws, size_t ws_size, hipStream_t stream) {
  (void)hipFuncSetAttribute((const void*)fused_kernel, hipFuncAttributeMaxDynamicSharedMemorySize, LDS_BYTES);
  fused_kernel<<<256, 512, LDS_BYTES, stream>>>((const char*)d_in[0], (const char*)d_in[1], (const char*)d_in[2], (const char*)d_in[3],
                                                (const char*)d_in[5], (const char*)d_in[6], (const char*)d_in[7], (char*)d_out, (char*)d_ws);
}
```

```cpp
#include <hip/hip_runtime.h>
#include <hip/hip_bf16.h>
#include <stdint.h>
#include <type_traits>
#pragma clang diagnostic ignored "-Wint-to-pointer-cast"
#pragma clang diagnostic ignored "-Warray-bounds"

#define DI __device__ __forceinline__
#define AI __attribute__((always_inline))
#define LAS __attribute__((address_space(3)))
#define GAS __attribute__((address_space(1)))
typedef __attribute__((ext_vector_type(8))) short s16x8;
typedef __attribute__((ext_vector_type(4))) short s16x4;
typedef __attribute__((ext_vector_type(16))) float f32x16;
typedef __attribute__((ext_vector_type(4))) float f32x4;
typedef __attribute__((ext_vector_type(2))) float f32x2;
typedef __attribute__((ext_vector_type(4))) unsigned u32x4;
typedef __attribute__((ext_vector_type(2))) unsigned u32x2;
typedef __attribute__((ext_vector_type(2))) __bf16 bf16x2;
typedef GAS unsigned gu32;
typedef __amdgpu_buffer_rsrc_t rsrc_t;
#define RLX_AGENT __ATOMIC_RELAXED, __HIP_MEMORY_SCOPE_AGENT

constexpr unsigned WS_EPOCH = 0;
constexpr unsigned WS_AFLAG = 16384;
constexpr unsigned WS_STAT = WS_AFLAG + 128 * 128;
constexpr unsigned WS_CS = WS_STAT + 2048 * 16;
constexpr unsigned WS_KVRED = 262144;
constexpr unsigned WS_KVPART = WS_KVRED + 131072;
constexpr unsigned WS_CFLAG = WS_KVPART + (1u << 20);
constexpr unsigned WS_AFRAG = WS_KVPART + (2u << 20);
constexpr unsigned WS_STAMP = WS_AFRAG + 64u * 144u * 1024u;
constexpr unsigned WS_BYTES = WS_STAMP;
static_assert(WS_CS + 4928 * 32 <= WS_KVRED, "ws map");

constexpr int LDS_BYTES = 163840;
constexpr unsigned SLOT = 16384;
constexpr unsigned LDS_MISC = 0, LDS_PHI = 2048;
constexpr int CODE_LINES = 240;
constexpr int SPIN_CAP = 1 << 14;

#define HOTA(c) (c)
#define HOTV(c) (c)
#define MFMA(a, b, c) __builtin_amdgcn_mfma_f32_32x32x16_bf16((a), (b), (c), 0, 0, 0)
#define WAITV(n) asm volatile("s_waitcnt vmcnt(" #n ")" ::: "memory")
#define BAR() do { asm volatile("s_waitcnt lgkmcnt(0)" ::: "memory"); __builtin_amdgcn_s_barrier(); asm volatile("" ::: "memory"); } while (0)
#define SB() __builtin_amdgcn_sched_barrier(0)
#define OPAQUE(v) asm volatile("" : "+v"(v))
template <int N> DI void waitv() { asm volatile("s_waitcnt vmcnt(%0)" ::"n"(N) : "memory"); }
template <int N> using IC = std::integral_constant<int, N>;
template <int I, int N, class F> DI void sfor(F&& f) { if constexpr (I < N) { f(IC<I>{}); sfor<I + 1, N>(f); } }

#define STAMP(i)
#define STAMPC(i)
#define TK(i)
#define TK2(i)
#define DUMPTK()

#define DMA1I(voff, rsrc, ldsb, LIMM, SOFF, tmp) asm volatile("s_add_u32 m0, %3, %4\n\ts_mov_b32 %0, %5\n\tbuffer_load_dwordx4 %1, %2, %0 offen lds" \
    : "=&s"(tmp) : "v"(voff), "s"(rsrc), "s"(ldsb), "n"(LIMM), "n"(SOFF) : "memory", "scc")
#define DMA1P(voff, rsrc, ldsb, LIMM, sa, sb, tmp) asm volatile("s_add_u32 m0, %3, %4\n\ts_add_u32 %0, %5, %6\n\tbuffer_load_dwordx4 %1, %2, %0 offen lds" \
    : "=&s"(tmp) : "v"(voff), "s"(rsrc), "s"(ldsb), "n"(LIMM), "s"(sa), "s"(sb) : "memory", "scc")
#define DMA1M(voff, rsrc, ldsb, LIMM, sa, sb, tmp) asm volatile("s_add_u32 m0, %3, %4\n\ts_mul_i32 %0, %6, %5\n\tbuffer_load_dwordx4 %1, %2, %0 offen lds" \
    : "=&s"(tmp) : "v"(voff), "s"(rsrc), "s"(ldsb), "n"(LIMM), "n"(sa), "s"(sb) : "memory", "scc")
#define DMA2(voff, rsrc, soff) asm volatile("buffer_load_dwordx4 %0, %1, %2 offen offset:1024 lds" :: "v"(voff), "s"(rsrc), "s"(soff) : "memory")
#define ALD16(dst, voff, soff) asm volatile("buffer_load_dwordx4 %0, %1, %2, %3 offen sc1" : "=v"(dst) : "v"(voff), "s"(rs), "s"(soff) : "memory")
#define ALD4(dst, voff, soff) asm volatile("buffer_load_dword %0, %1, %2, %3 offen sc1" : "=v"(dst) : "v"(voff), "s"(rs), "s"(soff) : "memory")

#define FRESH_LANE() int lane; asm volatile("v_mbcnt_lo_u32_b32 %0, -1, 0\n\tv_mbcnt_hi_u32_b32 %0, -1, %0" : "=v"(lane)); \
  const int g16 = (lane >> 4) & 1, q4 = (lane & 15) >> 2, p4 = lane & 3, l31 = lane & 31, hb = lane >> 5, rl = lane >> 3; \
  (void)g16; (void)q4; (void)p4; (void)l31; (void)hb; (void)rl
DI unsigned usel(bool c, unsigned a, unsigned b) { const unsigned k = c ? ~0u : 0u; return (a & k) | (b & ~k); }
DI const char* psel(bool c, const char* a, const char* b) { return c ? a : b; }
DI rsrc_t mkoff(const char* p, unsigned off, unsigned bytes) { return __builtin_amdgcn_make_buffer_rsrc((void*)(p + off), 0, bytes - off, 0x00020000); }
DI rsrc_t mkrs(const void* p, unsigned bytes) { return __builtin_amdgcn_make_buffer_rsrc((void*)p, 0, bytes, 0x00020000); }
DI s16x8 trfrag(unsigned a0, unsigned a1) {
  s16x4 lo = __builtin_amdgcn_ds_read_tr16_b64_v4i16((LAS s16x4*)a0);
  s16x4 hi = __builtin_amdgcn_ds_read_tr16_b64_v4i16((LAS s16x4*)a1);
  return __builtin_shufflevector(lo, hi, 0, 1, 2, 3, 4, 5, 6, 7);
}
DI unsigned pk2(float a, float b) { f32x2 p = {a, b}; return __builtin_bit_cast(unsigned, __builtin_convertvector(p, bf16x2)); }
DI float dot2(unsigned a, unsigned b, float c) { return __builtin_amdgcn_fdot2_f32_bf16(__builtin_bit_cast(bf16x2, a), __builtin_bit_cast(bf16x2, b), c, false); }
DI float blo(unsigned u) { return __uint_as_float(u << 16); }
DI float bhi(unsigned u) { return __uint_as_float(u & 0xffff0000u); }
DI float fand(float v, unsigned m) { return __uint_as_float(__float_as_uint(v) & m); }
DI float vmul(float a, float b) { float r = a * b; OPAQUE(r); return r; }
DI float vadd(float a, float b) { float r = a + b; OPAQUE(r); return r; }
DI float max3(float a, float b, float c) { return __builtin_elementwise_maximum(__builtin_elementwise_maximum(a, b), c); }
DI float min3(float a, float b, float c) { return __builtin_elementwise_minimum(__builtin_elementwise_minimum(a, b), c); }
DI float add32(float a, float b) { auto r = __builtin_amdgcn_permlane32_swap(__float_as_uint(a), __float_as_uint(b), false, false); return __uint_as_float(r[0]) + __uint_as_float(r[1]); }
DI float add16(float a, float b) { auto r = __builtin_amdgcn_permlane16_swap(__float_as_uint(a), __float_as_uint(b), false, false); return __uint_as_float(r[0]) + __uint_as_float(r[1]); }
DI void bcast32(float v, float& lo, float& hi) { auto r = __builtin_amdgcn_permlane32_swap(__float_as_uint(v), __float_as_uint(v), false, false); lo = __uint_as_float(r[0]); hi = __uint_as_float(r[1]); }
template <int CTRL> DI float dpp(float x) { return __builtin_bit_cast(float, __builtin_amdgcn_mov_dpp(__builtin_bit_cast(int, x), CTRL, 0xf, 0xf, true)); }
DI float wave_sum(float v) {
  v += dpp<0xB1>(v); v += dpp<0x4E>(v); v += dpp<0x141>(v); v += dpp<0x128>(v);
  v = add16(v, v); return add32(v, v);
}
DI unsigned split2(float v) {
  const unsigned h = pk2(v, 0.f);
  return pk2(__uint_as_float(h << 16), v - __uint_as_float(h << 16));
}
DI u32x4 pack8u(const f32x16& x, int s) {
  u32x4 u = {pk2(x[8 * s], x[8 * s + 1]), pk2(x[8 * s + 2], x[8 * s + 3]), pk2(x[8 * s + 4], x[8 * s + 5]), pk2(x[8 * s + 6], x[8 * s + 7])};
  return u;
}
DI s16x8 asf(u32x4 u) { return __builtin_bit_cast(s16x8, u); }
DI unsigned rmask(unsigned row) { return (((row >> 1) & 1) << 2) | ((row >> 2) & 1) | ((row >> 3) & 2); }
DI f32x16 zero16() { f32x16 z; for (int i = 0; i < 16; i++) z[i] = 0.f; return z; }
DI void st16(rsrc_t rs, unsigned off, u32x4 v) { __builtin_amdgcn_raw_buffer_store_b128(v, rs, off, 0, 16); }
DI void st4(rsrc_t rs, unsigned off, unsigned v) { __builtin_amdgcn_raw_buffer_store_b32(v, rs, off, 0, 16); }
DI u32x4 ld16(rsrc_t rs, unsigned off) { return __builtin_amdgcn_raw_buffer_load_b128(rs, off, 0, 16); }
DI bool seen(unsigned v, unsigned tag, unsigned long long need) { return (__builtin_amdgcn_ballot_w64(v == tag) & need) == need; }
DI void poll(char* ws, unsigned off, unsigned long long need, unsigned tag) {
#pragma unroll 1
  for (int it = 0; it < SPIN_CAP; ++it) {
    const unsigned v = __hip_atomic_load((gu32*)(ws + off), RLX_AGENT);
    if (seen(v, tag, need)) break;
    __builtin_amdgcn_s_sleep(2);
  }
  __builtin_amdgcn_fence(__ATOMIC_ACQUIRE, "wavefront");
}
template <bool ST> DI void g_check(rsrc_t rs, u32x4& a, u32x4& b, u32x4& st, unsigned va, unsigned vb, unsigned vs, unsigned tag, unsigned mlo) {
#pragma unroll 1
  for (int it = 0; it < SPIN_CAP; ++it) {
    unsigned bad = (a[1] ^ tag) | (b[1] ^ tag) | (((a[3] ^ tag) | (b[3] ^ tag)) & mlo);
    if (ST) bad |= (st[1] ^ tag) | (st[3] ^ tag);
    if (__builtin_expect(__builtin_amdgcn_ballot_w64(bad != 0) == 0ull, 1)) break;
    ALD16(a, va, WS_CS); ALD16(b, vb, WS_CS); if (ST) ALD16(st, vs, WS_STAT);
    if (ST) asm volatile("s_waitcnt vmcnt(0)" : "+v"(a), "+v"(b), "+v"(st)::"memory"); else asm volatile("s_waitcnt vmcnt(0)" : "+v"(a), "+v"(b)::"memory");
  }
}

DI unsigned umax4(unsigned m, const u32x4& v) { return max(max(max(m, v[0]), v[1]), max(v[2], v[3])); }
DI bool fresh(unsigned m) { return __builtin_amdgcn_ballot_w64(m == 0xffffffffu) == 0ull; }

struct Sm { float L, cx, cn, mx, mn, s1, s2; f32x16 p, ep, en; };
DI void sm_max(Sm& s, float ninf) {
  const f32x16& p = s.p;
  float a = max3(p[0], p[1], p[2]);
  for (int i = 3; i < 11; i += 2) a = max3(a, p[i], p[i + 1]);
  s.mx = max3(a, p[11], max3(max3(p[12], p[13], p[14]), p[15], p[15]) + ninf);
}
DI void sm_min(Sm& s, float ninf) {
  const f32x16& p = s.p;
  float b = min3(p[0], p[1], p[2]);
  for (int i = 3; i < 11; i += 2) b = min3(b, p[i], p[i + 1]);
  s.mn = min3(b, p[11], min3(min3(p[12], p[13], p[14]), p[15], p[15]) - ninf);
}
DI void sm_consts(Sm& s, float sc) {
  auto r = __builtin_amdgcn_permlane32_swap(__float_as_uint(s.mx), __float_as_uint(-s.mn), false, false);
  const float m = max3(__uint_as_float(r[0]), __uint_as_float(r[1]), __uint_as_float(r[1]));
  float mx, nmn; bcast32(m, mx, nmn);
  s.L = sc * 1.4426950408889634f; s.cx = -mx * s.L; s.cn = -nmn * s.L; s.s1 = 0.f; s.s2 = 0.f;
}
DI void sm_exp(Sm& s, int i, float ninf) {
  if (i == 12) { s.cx += ninf; s.cn += ninf; }
  s.ep[i] = __builtin_amdgcn_exp2f(fmaf(s.p[i], s.L, s.cx)); s.en[i] = __builtin_amdgcn_exp2f(fmaf(s.p[i], -s.L, s.cn));
  if (i > 0) { s.s1 = vadd(s.s1, s.ep[i - 1]); s.s2 = vadd(s.s2, s.en[i - 1]); }
}
DI void sm_sum(Sm& s, float os) {
  const float t = add32(vadd(s.s1, s.ep[15]), vadd(s.s2, s.en[15]));
  bcast32(os * __builtin_amdgcn_rcpf(t), s.s1, s.s2);
}
DI void sm_norm2(const Sm& s, int f, int h, u32x4& u) {
  const f32x16& e = f < 2 ? s.ep : s.en; const float r = f < 2 ? s.s1 : s.s2; const int o = 8 * (f & 1);
  for (int i = 2 * h; i < 2 * h + 2; i++) u[i] = pk2(vmul(e[o + 2 * i], r), vmul(e[o + 2 * i + 1], r));
}
DI u32x4 sm_norm(const Sm& s, int f) {
  const f32x16& e = f < 2 ? s.ep : s.en; const float r = f < 2 ? s.s1 : s.s2; const int o = 8 * (f & 1);
  u32x4 u;
  for (int i = 0; i < 4; i++) u[i] = pk2(vmul(e[o + 2 * i], r), vmul(e[o + 2 * i + 1], r));
  return u;
}

struct P2 {
  static constexpr int a0(int s) { return s <= 1 ? 7 + s : s <= 3 ? 8 : s + 5 < 18 ? s + 5 : 18; }
  static constexpr int vs(int j) { return j + 1; }
  static constexpr int vj(int s) { for (int j = 0; j < 8; j++) if (vs(j) == s) return j; return -1; }
  static constexpr int FL2 = 2, N2 = 7;
  static constexpr bool bar(int c) { return c == 1 || c == 3 || c == 5 || c == 7 || c == 12; }
  static constexpr int vneed(int c) { return c == 5 ? 9 : c == 7 ? 12 : c == 12 ? 17 : -1; }
  static constexpr int na(int s) { return 2 * (a0(s + 1) - a0(s)); }
  static constexpr int nops(int s) { return (vj(s) >= 0 ? 2 : 0) + na(s) + (s == 13 || s == FL2 ? 1 : 0); }
  static constexpr int upto(int from, int c) { int n = 0; for (int s = from; s < c; s++) n += nops(s); return n; }
  static constexpr int wait(int c) {
    int n = 63;
    if (c >= 4 && c < 7) n = 2 * (6 - c) + upto(0, c);
    for (int s = 0; s < c; s++) if (a0(s) <= c && c < a0(s + 1)) n = 2 * (a0(s + 1) - 1 - c) + upto(s + 1, c);
    if (c == N2) { const int f = upto(FL2, c) - 1; if (f < n) n = f; }
    if (vneed(c) >= 10) { const int s = vs(vneed(c) - 10), v = na(s) + upto(s + 1, c); if (v < n) n = v; }
    return n > 63 ? 63 : n;
  }
};

__global__ __launch_bounds__(512, 2) void fused_kernel(const char* __restrict__ x, const char* __restrict__ wqkv, const char* __restrict__ wphi,
                                                       const char* __restrict__ wo, const char* __restrict__ wg, const char* __restrict__ wu,
                                                       const char* __restrict__ wd, char* __restrict__ out, char* ws) {
  const int lane = threadIdx.x & 63, w = __builtin_amdgcn_readfirstlane(threadIdx.x >> 6);
  const int bx = blockIdx.x, xcd = bx & 7, r = bx >> 3;
  const int tg = xcd & 3, m = (xcd >> 2) * 8 + (r & 7), ts = r >> 3;
  const int g16 = (lane >> 4) & 1, q4 = (lane & 15) >> 2, p4 = lane & 3, l31 = lane & 31, hb = lane >> 5, rl = lane >> 3;
  asm volatile("" :: "s"(x), "s"(wqkv), "s"(wphi), "s"(wo), "s"(wg), "s"(wu), "s"(wd), "s"(out), "s"(ws));
  STAMP(0); STAMPC(14); TK(0);
  unsigned st0, st1;
  const unsigned ldsw = w * 2048;
  const unsigned voffX = (16 * w + rl) * 896 + (((lane & 7) ^ rmask(16 * w + rl)) << 4), voffX2 = voffX + 8 * 896 - 1024;
  const rsrc_t rsX = mkrs(x + (size_t)m * 128 * 896, 128 * 896);
#define ITEM_X(c, h) do { if (!(h)) DMA1I(voffX, rsX, ldsw, (7 + ((c) + 2) % 3) * SLOT, (c) * 128, st0); else DMA2(voffX2, rsX, st0); } while (0)
#define ITEM_V(c, h) do { if (!(h)) DMA1I(voffX, rsV, ldsw, (((c) + 7) % 10) * SLOT, (c) * (128 * 896), st0); else DMA2(voffX2, rsV, st0); } while (0)
  ITEM_X(0, 0); ITEM_X(0, 1);

  const bool isf = ts < 2;
  const int fi = 2 * tg + (ts & 1);
  const int rb = w & 3, role = w >> 2;
#define VOFF(f) ((16 * rb + rl) * ((f) ? 3584 : 2688) + (usel(f, (((((lane & 7) >> 1) ^ ((rl >> 1) & 1) ^ (role << 1)) << 1) | (lane & 1)), min((lane & 7) ^ (rl << 1 & 4), 6)) << 4))
  const char* wgu = psel(role, wu, wg);
  unsigned voffW = VOFF(isf), wmul = isf ? 1792 : 1344;
  unsigned voffW2 = voffW + wmul * 16 - 1024;
  rsrc_t rsW = mkoff(psel(isf, wgu, wqkv), fi * (isf ? 128 : 112) + (isf ? 0 : 896 + 896 * role), wmul * 896);
#define ITEM_W(c, h) do { if (!(h)) DMA1M(voffW, rsW, ldsw, (c) * SLOT, (c) * 128, wmul, st1); else DMA2(voffW2, rsW, st1); } while (0)
  ITEM_W(0, 0); ITEM_W(0, 1); ITEM_X(1, 0); ITEM_X(1, 1); ITEM_W(1, 0); ITEM_W(1, 1); ITEM_X(2, 0); ITEM_X(2, 1);
  unsigned pfc;
  {
    unsigned long long pc0; asm volatile("s_getpc_b64 %0" : "=s"(pc0));
    asm volatile("global_load_dword %0, %1, %2" : "=v"(pfc) : "v"(min(r * 8 + w + (lane & 1) * 256, CODE_LINES) * 128), "s"(pc0 & ~127ull) : "memory");
  }

  const rsrc_t rs = mkrs(ws, WS_BYTES);
  unsigned tag;
  const int pu = (xcd >> 2) * 32 + r, li = pu / 3;
  const int la = li >= 6, lj = li - 6 * la, ll = 2 * tg + (li & 1);
  const int lc = li < 12 ? 21 + 28 * (lj & 1) + 8 * (lj >> 1) + 2 * tg + la : li < 14 ? 45 + 28 * (li & 1) + tg : 7 * ((li - 14) >> 1) + ll;
  const int cpart = li >= 14 && (ll > 6 || li > 19) ? 3 : pu - 3 * li;
  const int srow = (xcd * 32 + r) * 8 + w;
  const int tidx = (xcd >> 1) * 56 + (xcd & 1) * 32 + r;
  const bool notile = ts == 3 && (xcd & 1);
  const int rt = notile ? 0 : tidx / 7, ct = notile ? 0 : tidx - 7 * rt;
  const rsrc_t rsV = mkrs(wd + ct * 128, notile ? 0 : 1792 * 896 - ct * 128);
  const rsrc_t rsP = mkrs(wphi + 4 * tg * 1568, isf ? 0 : 4 * 1568);
  const unsigned mlo = lane < 32 ? 0xffffffffu : 0u;
  const unsigned lp = w * 1024;

  const int rbg = m * 4 + rb;
  const int rb2 = w & 1, kq = w >> 1;
#define VFL (((rt * 2 + rb2) * 2 + (kq & 1)) * 128 + l31 * 4)
#define AB ((notile ? 0xe0000000u : ((rt * 2 + rb2) * 144 + 2 * kq) * 1024) + lane * 16)
  u32x4 af[36];
#define LDA2(c) asm volatile("s_mov_b32 %2, %5\n\tbuffer_load_dwordx4 %0, %3, %4, %2 offen sc1\n\tbuffer_load_dwordx4 %1, %3, %4, %2 offen offset:1024 sc1" \
    : "=&v"(af[2 * (c)]), "=&v"(af[2 * (c) + 1]), "=&s"(st1) : "v"(AB), "s"(rs), "n"(WS_AFRAG + (c) * 8192) : "memory")
#define PIN8(c) "+v"(af[2 * (c)]), "+v"(af[2 * (c) + 1]), "+v"(af[2 * (c) + 2]), "+v"(af[2 * (c) + 3]), "+v"(af[2 * (c) + 4]), "+v"(af[2 * (c) + 5]), "+v"(af[2 * (c) + 6]), "+v"(af[2 * (c) + 7])
#define NEED(mask) do { if (!seen(fl, tag, mask)) poll(ws, WS_AFLAG + VFL, mask, tag); } while (0)
#define ONES (u32x4{~0u, ~0u, ~0u, ~0u})
  f32x16 pa0, pa1;
  float rstd;
  unsigned fl = 0;

  const unsigned xrow = 32 * rb + l31;
  const unsigned vst = (m * 128 + xrow) * 16;
  s16x8 xf[28], wq[3][2], fb, wf[2];
  u32x4 stt, ca, cb;
#define AW_F() (g16 * 8192 + (8 * hb + q4) * 128 + (((2 * role) ^ ((q4 >> 1) & 1) ^ (g16 << 1)) * 32) + p4 * 8)
#define AW_A(half, sel) ((half) * 8192 + (8 * hb + q4) * 128 + ((56 * (sel) + 32 * g16 + 8 * p4) ^ (q4 << 5 & 64)))
#define VCS(f, col) ((g16 * ((f) ? 1792 : 16) + (lane & 15)) * 32 + hb * 16 + (col) * 32)
  unsigned aW[4], vca, vcb;
  aW[0] = usel(isf, AW_F(), AW_A(0, role)); aW[1] = aW[0] ^ (isf ? 32 : 8192); aW[2] = aW[0] + 65536; aW[3] = aW[1] + 65536;
  OPAQUE(aW[0]); OPAQUE(aW[1]); OPAQUE(aW[2]); OPAQUE(aW[3]);
  auto fold = [&](u32x4 c) AI { const unsigned c2 = c[2] & mlo; u32x4 u = {c[0], c2, c[0], c2}; return asf(u); };
  auto make_fb = [&]() AI { const unsigned a = stt[0] & 0xffffu, b = stt[0] >> 16; u32x4 u = {a | (a << 16), a | (a << 16), b | (b << 16), b | (b << 16)}; fb = asf(u); rstd = __uint_as_float(stt[2]); };
  auto wfrag = [&](int n, int t, bool narrow = false) AI {
    const int sl = (n >> 2) % 7;
    const unsigned o = (sl & 3) * SLOT + (n & 3) * (narrow ? 1024 : 2048), b = aW[(sl >> 2) * 2 + t];
    return trfrag(b + o, b + o + (narrow ? 256 : 512));
  };

  {
    u32x4 px, pw[3];
    TK(1);
    unsigned aX[4];
#pragma unroll
    for (int ks = 0; ks < 4; ks++) { aX[ks] = 7 * SLOT + xrow * 128 + (((2 * ks + hb) ^ rmask(xrow)) << 4); OPAQUE(aX[ks]); }
    auto xread = [&](int n) AI { xf[n] = *(LAS s16x8*)(aX[n & 3] + (((n >> 2) + 2) % 3) * SLOT); };
    f32x16 acc0 = zero16(), acc1 = zero16();
    const unsigned m56 = lane < 56 ? 0xffffffffu : 0u;
    float cs[8], s = 0.f, q = 0.f; unsigned pfd;
    const bool hw = !isf && role;
    unsigned voffN = usel(hw, (32 * (w & 1) + (lane >> 2)) * 3584 + ((p4 ^ (w & 2)) << 4), voffW); OPAQUE(voffN);
    const char* pcb = psel(lc < 21, wqkv, psel(lc < 49, wg, wu)) + (lc - (lc < 21 ? 0 : lc < 49 ? 21 : 49)) * 128;
    const unsigned ppitch = lc < 21 ? 2688 : 3584;
    rsrc_t rsC = mkoff(pcb, min(152 * cpart + 8 * w, 440) * ppitch, 447 * ppitch + 128);
    unsigned pst = 64 * ppitch;
    const char* pxr = x + (size_t)srow * 896;
    rsrc_t rsN = mkoff(psel(isf, wgu, psel(hw, psel(w & 2, wu, wg), wqkv)), usel(isf, (8 + fi) * 128, usel(hw, 3072 + 64 * fi, 112 * fi)), (isf || hw ? 1792 : 1344) * 896);
    const int u = bx * 8 + w;
    rsrc_t rsF = mkrs(psel(u < 1568, wd, wo) + (u < 1568 ? u : u - 1568) * 1024, u < 1960 ? 1024 : 0);
    unsigned pha = LDS_PHI + (4 * hb + q4) * 56 + g16 * 32 + p4 * 8 + role * 1568;
    asm volatile("" : "+s"(rsC), "+s"(pst), "+s"(pxr), "+s"(rsN), "+s"(rsF)); OPAQUE(pha);
    asm volatile("" :: "s"(rsV), "s"(rt), "s"(ct));
    WAITV(7);
    BAR();
    TK(4);
    xread(0); wq[0][1] = wfrag(0, 1); wq[0][0] = wfrag(0, 0);
    xread(1); wq[1][1] = wfrag(1, 1); wq[1][0] = wfrag(1, 0);
    auto pre = [&](int p) AI {
      if (p == 2) {
        asm volatile("buffer_load_dword %0, off, %1, 0 sc1" : "=v"(tag) : "s"(rs) : "memory");
        asm volatile("global_load_dwordx4 %0, %1, %2" : "=v"(px) : "v"(min(lane, 55) * 16), "s"(pxr) : "memory");
      }
      if (p == 3) {
        unsigned vo = rl * ppitch + (lane & 7) * 16;
        asm volatile("buffer_load_dwordx4 %0, %1, %2, 0 offen" : "=v"(pw[0]) : "v"(vo), "s"(rsC) : "memory"); vo += pst;
        asm volatile("buffer_load_dwordx4 %0, %1, %2, 0 offen" : "=v"(pw[1]) : "v"(vo), "s"(rsC) : "memory"); vo += pst;
        asm volatile("buffer_load_dwordx4 %0, %1, %2, 0 offen" : "=v"(pw[2]) : "v"(vo), "s"(rsC) : "memory");
      }
      if (p == 12) { tag += 1; for (int i = 0; i < 4; i++) px[i] &= m56; }
      if (p == 13) { for (int i = 0; i < 4; i++) { s = dot2(px[i], 0x3f803f80u, s); q = dot2(px[i], px[i], q); } OPAQUE(s); OPAQUE(q); }
      if (p == 14) { s = add32(s, q); s += dpp<0xB1>(s); OPAQUE(s); }
      if (p == 15) { s += dpp<0x4E>(s); s += dpp<0x141>(s); OPAQUE(s); }
      if (p == 16) { s += dpp<0x128>(s); s = add16(s, s); OPAQUE(s); }
      if (p == 17) { bcast32(s * (1.f / 448.f), s, q); q = fmaxf(fmaf(-s, s, q), 0.f) + 1e-5f; OPAQUE(s); OPAQUE(q); }
      if (p == 18) {
        u32x4 o = {split2(-s), tag, __float_as_uint(__builtin_amdgcn_rsqf(q)), tag};
        st16(rs, lane == 0 ? WS_STAT + srow * 16 : 0xfffffff0u, o);
      }
      if (p >= 15 && p < 23) {
        const int i = (p - 15) >> 1;
        if (p & 1) { pw[2][i] &= (w + 16 < (cpart == 2 ? 18 : 19) ? 0xffffffffu : 0u); cs[2 * i] = vadd(vadd(blo(pw[0][i]), blo(pw[1][i])), blo(pw[2][i])); }
        else cs[2 * i + 1] = vadd(vadd(bhi(pw[0][i]), bhi(pw[1][i])), bhi(pw[2][i]));
      }
      if (p == 23 || p == 24) for (int i = 2 * (p - 23); i < 2 * (p - 22); i++) { cs[i] = add32(cs[i], cs[i + 4]); OPAQUE(cs[i]); }
      if (p == 25) for (int i = 0; i < 2; i++) { cs[i] = add16(cs[i], cs[i + 2]); OPAQUE(cs[i]); }
      if (p == 26) for (int i = 0; i < 2; i++) { cs[i] += dpp<0x128>(cs[i]); OPAQUE(cs[i]); }
      if (p == 27) {
        *(LAS f32x2*)(LDS_MISC + w * 256 + ((lane & 7) * 8 + hb * 4 + g16 * 2) * 4) = f32x2{cs[0], cs[1]};
        *(LAS u32x2*)(LDS_PHI + 4 * 1568 + lane * 8) = u32x2{0u, 0u};
      }
    };
    sfor<0, 28>([&](auto nn) AI {
      constexpr int n = decltype(nn)::value;
      constexpr int c = (n + 2) >> 2, j = (n + 2) & 3;
      if (j == 0) {
        if (c == 2) asm volatile("s_waitcnt vmcnt(7)" : "+v"(tag), "+v"(px), "+v"(pw[0]), "+v"(pw[1]), "+v"(pw[2]) : "v"(pfc) : "memory");
        if (c == 1) WAITV(10); if (c == 3) WAITV(7); if (c == 4) WAITV(6); if (c == 5 || c == 6) WAITV(9);
        if (c == 7) asm volatile("s_waitcnt vmcnt(4)" :: "v"(pfd) : "memory");
        BAR();
        TK(4 + c);
      }
      if (n == 14 && w == 7 && cpart < 3) {
        float t = 0.f;
        for (int i = 0; i < 8; i++) t += *(LAS float*)(LDS_MISC + i * 256 + lane * 4);
        u32x2 o = {split2(t), tag};
        __builtin_amdgcn_raw_buffer_store_b64(o, rs, WS_CS + (lc * 64 + lane) * 32 + cpart * 8, 0, 16);
      }
#define P0_ISSUE(h) do { \
      if (n == 0 || n == 3) ITEM_W(n == 0 ? 2 : 3, h); \
      if (c >= 1 && c <= 4 && j == 0) ITEM_X(c + 2, h); \
      if (c >= 1 && c <= 3 && j == 2) ITEM_W(c + 3, h); \
      if (c == 4 && j >= 2) ITEM_W(j - 1, h); \
      if (c == 5 && j == 0) ITEM_W(0, h); \
      if (c == 5 && j == 2) ITEM_W(3, h); \
      if (c == 6 && j == 0) ITEM_W(4, h); } while (0)
      SB();
      if (n == 27) pa0 = MFMA(wq[n % 3][0], xf[n], acc0); else acc0 = MFMA(wq[n % 3][0], xf[n], acc0);
      SB();
      if (n + 2 < 28) xread(n + 2);
      wq[(n + 2) % 3][1] = wfrag(n + 2, 1);
      pre(2 * n);
      P0_ISSUE(0);
      if (n == 5) DMA1I(lp + lane * 16, rsP, lp, LDS_PHI, 0, st0);
      if (n == 15) {
        asm volatile("buffer_load_dword %0, %1, %2, 0 offen" : "=v"(pfd) : "v"((lane & 7) * 128), "s"(rsF) : "memory");
      }
      if (n == 23) { vca = VCS(isf, fi * (isf ? 64 : 56) + role * (isf ? 32 : 28) + (isf ? 1344 : 448)); vcb = vca + (isf ? 512 : 448 * 32); }
      SB();
      if (n == 27) pa1 = MFMA(wq[n % 3][1], xf[n], acc1); else acc1 = MFMA(wq[n % 3][1], xf[n], acc1);
      SB();
      wq[(n + 2) % 3][0] = wfrag(n + 2, 0);
      pre(2 * n + 1);
      P0_ISSUE(1);
      if (n == 14) {
        wmul = isf || hw ? 1792 : 1344;
        rsW = rsN;
        voffW = voffN; voffW2 = voffW + (hw ? 16 * 3584 : wmul * 16) - 1024;
      }
      if (n == 15) {
        const unsigned pa = pha + (ts & 1) * 3136;
        wf[0] = trfrag(pa, pa + 448); wf[1] = trfrag(pa + 896, pa + 1344);
      }
      SB();
    });
    wq[0][0] = wq[1][0]; wq[0][1] = wq[1][1]; wq[1][0] = wq[2][0]; wq[1][1] = wq[2][1];
    STAMP(1); TK(12);
  }

  float k0, k1, ia2;
  auto gconst = [&]() AI {
    const float c0 = -2.f * 0.7978845608028654f * 1.4426950408889634f;
    k0 = vmul(c0, rstd); k1 = vmul(c0 * 0.044715f * rstd * rstd, rstd); ia2 = __builtin_amdgcn_rcpf(rstd * rstd);
  };
  auto passn = [&](auto kind) AI {
    constexpr int K = decltype(kind)::value;
    constexpr bool F = K == 1, L = K != 1, G = K != 3;
    FRESH_LANE();
    const unsigned hb0 = WS_AFRAG + (rbg * 144 + 2 * role) * 1024 + lane * 16;
    const unsigned ho = hb0 + (F ? fi : 8 + fi) * 4096;
    unsigned fo = 0;
    if (K == 3) {
      const unsigned f0 = WS_CFLAG + ((fi * 8 + w) * 16 + m) * 64, fa = WS_AFLAG + rbg * 256 + (8 + 2 * fi + role) * 4, fb2 = fa + 128;
      fo = lane == 0 ? f0 : 0xffffff00u; fo = lane == 1 ? fa : fo; fo = lane == 2 ? fb2 : fo;
      OPAQUE(fo);
    }
    const rsrc_t rsB = mkoff(wgu, (16 + fi) * 128, 448 * 3584);
    f32x16 acc0 = zero16(), acc1 = zero16();
    f32x16 qa0, qa1;
    float gt[2], gu[2], hprev; u32x4 hq;
    auto geglu = [&](int hs) AI {
#pragma unroll
      for (int e = 0; e < 16; e++) {
        const int d = hs - (F ? 14 + 9 * e / 4 : 4 + 12 * e / 5);
        const float Gv = e < 8 ? qa0[e & 7] : qa1[e & 7], U = e < 8 ? qa0[(e & 7) + 8] : qa1[(e & 7) + 8];
        if (d == 0) gt[e & 1] = vmul(Gv, fmaf(Gv * Gv, k1, k0));
        if (d == 1) { gt[e & 1] = __builtin_amdgcn_exp2f(gt[e & 1]); gu[e & 1] = vmul(Gv, U); }
        if (d == 2) gt[e & 1] = __builtin_amdgcn_rcpf(fmaf(gt[e & 1], ia2, ia2));
        if (d == 3) {
          if (e & 1) hq[(e >> 1) & 3] = pk2(hprev, gu[1] * gt[1]); else hprev = vmul(gu[0], gt[0]);
          if ((e & 7) == 7) st16(rs, ho + (e >> 3) * 1024, hq);
        }
      }
    };
    if (F) { ALD16(ca, vca, WS_CS); ALD16(cb, vcb, WS_CS); ALD16(stt, vst, WS_STAT); }
    sfor<0, 28>([&](auto nn) AI {
      constexpr int n = decltype(nn)::value;
      if (K == 2 && n == 10) { WAITV(6); st4(rs, lane == 0 ? WS_AFLAG + (rbg * 2 + role) * 128 + fi * 4 : 0xffffff00u, tag); }
      if (K == 3 && n == 5) { WAITV(5); st4(rs, fo, tag); }
      if (n == 10) { BAR(); TK(K == 1 ? 13 : 17); }
      if (n == 18) {
        asm volatile("s_waitcnt vmcnt(%2)" : "+v"(ca), "+v"(cb) : "n"(K == 3 ? 6 : 8) : "memory"); BAR(); TK(K == 1 ? 14 : 18);
        if (K == 3 && notile) {
          const int gg = tg - 1 + role;
          poll(ws, WS_CFLAG + (((2 * gg + (m >> 3)) * 8 + (m >> 1 & 3)) * 16 + (lane & 15)) * 64, ~0ull, tag);
          const unsigned pb = WS_KVPART + ((gg * 8 + (m >> 1)) * 16 + (lane >> 4)) * 2048 + (m & 1) * 1024 + (rb * 16 + (lane & 15)) * 16;
          u32x4 pv[4];
#pragma unroll
          for (int i = 0; i < 4; i++) pv[i] = ld16(rs, pb + i * 8192);
          float t[8];
#pragma unroll
          for (int j = 0; j < 4; j++) {
            t[2 * j] = (blo(pv[0][j]) + blo(pv[1][j])) + (blo(pv[2][j]) + blo(pv[3][j]));
            t[2 * j + 1] = (bhi(pv[0][j]) + bhi(pv[1][j])) + (bhi(pv[2][j]) + bhi(pv[3][j]));
          }
#pragma unroll
          for (int j = 0; j < 8; j++) { t[j] = add32(t[j], t[j]); t[j] = add16(t[j], t[j]); }
          st16(rs, lane < 16 ? WS_KVRED + (gg * 16 + m) * 1024 + (rb * 16 + lane) * 16 : 0xf0000000u, u32x4{pk2(t[0], t[1]), pk2(t[2], t[3]), pk2(t[4], t[5]), pk2(t[6], t[7])});
          STAMP(6);
        }
      }
      if (L && n == 19) g_check<false>(rs, ca, cb, stt, vca, vcb, vst, tag, mlo);
      if (L && n == 23) asm volatile("s_waitcnt vmcnt(%1)" : "+v"(fl) : "n"(K == 3 ? 2 : 3) : "memory");
      if (n == 26) { if (F) WAITV(10); BAR(); TK(K == 1 ? 15 : 19); }
      if (G && n == (F ? 5 : 0)) {
        if (F) asm volatile("s_waitcnt vmcnt(6)" : "+v"(ca), "+v"(cb), "+v"(stt)::"memory");
        g_check<F>(rs, ca, cb, stt, vca, vcb, vst, tag, mlo);
      }
      SB();
      if (n == 27) pa0 = MFMA(wq[n % 3][0], xf[n], acc0); else acc0 = MFMA(wq[n % 3][0], xf[n], acc0);
      SB();
      if (!L || n + 2 < 28) wq[(n + 2) % 3][1] = wfrag(n + 2, 1, K == 3);
      if (F && n == 5) make_fb();
      if (G && n == (F ? 6 : 1)) qa0 = MFMA(fold(ca), fb, pa0);
      if (L && n == 20) acc0 = MFMA(fold(ca), fb, acc0);
      if (G && n < 4) ITEM_W(5 + n / 2, n & 1);
      if (F && n == 4) rsW = rsB;
      if (F && n >= 4 && n < 8) ITEM_V((n - 4) / 2, n & 1);
      if (F && (n == 23 || n == 24)) ITEM_V(2, !(n & 1));
      if (K == 3 && n < 6) ITEM_V(n / 2, n & 1);
      if (L && n >= 10 && n < 16) ITEM_V((n - 4) / 2, n & 1);
      if (L && (n == 18 || n == 19)) ITEM_V(6, n & 1);
      if (L && (n == 21 || n == 22)) ITEM_V(7, !(n & 1));
      if (L && n >= 26) ITEM_V(n - 18, 0);
      if (F && n >= 10 && n < 16) ITEM_W((n - 10) / 2, n & 1);
      if (F && (n == 18 || n == 19)) ITEM_W(3, n & 1);
      if (F && (n == 21 || n == 22)) ITEM_W(4, !(n & 1));
      if constexpr (K == 2 && n >= 23 && n < 26) LDA2(n - 23);
      if (G) geglu(2 * n);
      SB();
      if (n == 27) pa1 = MFMA(wq[n % 3][1], xf[n], acc1); else acc1 = MFMA(wq[n % 3][1], xf[n], acc1);
      SB();
      if (!L || n + 2 < 28) wq[(n + 2) % 3][0] = wfrag(n + 2, 0);
      if (F && n == 5) gconst();
      if (G && n == (F ? 6 : 1)) qa1 = MFMA(fold(cb), fb, pa1);
      if (L && n == 20) acc1 = MFMA(fold(cb), fb, acc1);
      if (F && (n == 0 || n == 7 || n == 17 || n == 20)) st16(rs, ho + (n < 8 ? 8 : 16) * 4096 + (n & 1) * 1024, ONES);
      if (L && n == 19) ALD4(fl, VFL, WS_AFLAG);
      if (n == 7) {
        if (G) { vca += 8 * 2048; vcb += 8 * 2048; }
        else { vca -= 448 * 32; vcb = VCS(true, 1344 + 1536 + 32 * fi + 16 * role); }
      }
      if (n == 8) ALD16(ca, vca, WS_CS);
      if (n == 9) ALD16(cb, vcb, WS_CS);
      if constexpr (K == 2 && n == 25) LDA2(3);
      if (L && n >= 26) ITEM_V(n - 18, 1);
      if (G) geglu(2 * n + 1);
      if (n < 27) asm volatile("" :: "v"(acc0), "v"(acc1));
      SB();
    });
    if (!L) { wq[0][0] = wq[1][0]; wq[0][1] = wq[1][1]; wq[1][0] = wq[2][0]; wq[1][1] = wq[2][1]; }
    STAMP(F ? 2 : 3); TK(K == 1 ? 16 : 20);
  };

  auto phase2 = [&]() AI {
    FRESH_LANE();
    const int tid = w * 64 + lane;
    const unsigned swz = (((lane & 7) ^ rmask(16 * w + rl)) << 4);
    STAMP(13);
    LDA2(4); LDA2(5); LDA2(6);
    const unsigned r0 = 32 * kq + 4 * hb + q4;
    unsigned bV[3][4];
#pragma unroll
    for (int i = 0; i < 12; i++) { bV[i >> 2][i & 3] = ((r0 * 128 + (((2 * g16 + (p4 >> 1)) ^ rmask(r0)) << 4) + (p4 & 1) * 8) ^ ((i & 1) * 64) ^ ((i & 2) * 16)) + (i >> 2) * 65536; OPAQUE(bV[i >> 2][i & 3]); }
    const unsigned voffO = (28 * (w >> 1) + 16 * (w & 1) + rl) * 896 + swz;
    const unsigned voffO2 = voffO + (((w & 1) && rl >= 4) ? 0 : 8 * 896) - 1024;
    const rsrc_t rsO = mkrs(wo + ct * 128, 448 * 896 - ct * 128);
    const size_t go = (size_t)(rt * 64 + (tid >> 3)) * 896 + ct * 128 + (tid & 7) * 16;
    u32x4 xr;
    f32x16 acc0 = zero16(), acc1 = zero16();
    s16x8 wv[2][4];
    auto vread = [&](int c) AI {
      const int sl = (c + 7) % 10; const unsigned so = (sl & 3) * SLOT;
      for (int i = 0; i < 4; i++) wv[c & 1][i] = trfrag(bV[sl >> 2][i] + so + (i >> 1) * 2048, bV[sl >> 2][i] + so + (i >> 1) * 2048 + 1024);
    };
    vread(0);
    sfor<0, 18>([&](auto cc) AI {
      constexpr int c = decltype(cc)::value;
      asm volatile("s_waitcnt vmcnt(%3)" : "+v"(af[2 * c]), "+v"(af[2 * c + 1]), "+v"(fl) : "n"(P2::wait(c)) : "memory");
      if constexpr (c >= 4) {
        constexpr int ce = c < 8 ? 8 : c < 12 ? 12 : c < 14 ? 14 : 18, c0 = ce - 4;
#pragma unroll 1
        for (int it = 0; it < SPIN_CAP; ++it) {
          if (__builtin_expect(fresh(umax4(umax4(0u, af[2 * c]), af[2 * c + 1])), 1)) break;
          WAITV(0);
          __builtin_amdgcn_s_sleep(4);
          LDA2(c); if (c < c0 + 3) LDA2(c0 + 3); if (c < c0 + 2) LDA2(c0 + 2); if (c < c0 + 1) LDA2(c0 + 1);
          asm volatile("s_waitcnt vmcnt(0)" : PIN8(c0)::"memory");
        }
        if (c == 8) { STAMP(11); TK(21); } if (c == 14) { STAMP(12); TK(22); }
      }
      if (P2::bar(c)) BAR();
      TK2(5 + c);
      if (c + 1 < 18) vread(c + 1);
      SB();
      acc0 = MFMA(wv[c & 1][0], asf(af[2 * c]), acc0); acc1 = MFMA(wv[c & 1][1], asf(af[2 * c]), acc1);
      if (c == P2::FL2) ALD4(fl, VFL, WS_AFLAG);
      constexpr int j = P2::vj(c);
      if (j >= 0 && j < 4) { ITEM_V(j + 10, 0); ITEM_V(j + 10, 1); }
      if (j >= 4) { DMA1I(voffO, rsO, ldsw, ((j + 7) % 10) * SLOT, (j - 4) * (112 * 896), st0); DMA2(voffO2, rsO, st0); }
      SB();
      acc0 = MFMA(wv[c & 1][2], asf(af[2 * c + 1]), acc0); acc1 = MFMA(wv[c & 1][3], asf(af[2 * c + 1]), acc1);
      if (c == P2::N2) NEED(0xffff00ull);
      if constexpr (P2::a0(c) < P2::a0(c + 1)) LDA2(P2::a0(c));
      if constexpr (P2::a0(c) + 1 < P2::a0(c + 1)) LDA2(P2::a0(c) + 1);
      if constexpr (P2::a0(c) + 2 < P2::a0(c + 1)) LDA2(P2::a0(c) + 2);
      if constexpr (P2::a0(c) + 3 < P2::a0(c + 1)) LDA2(P2::a0(c) + 3);
      static_assert(P2::a0(c + 1) - P2::a0(c) <= 4, "schedule");
      if (c == 13) asm volatile("global_load_dwordx4 %0, %1, off" : "=v"(xr) : "v"(x + go) : "memory");
      SB();
    });
    STAMP(8);
    const unsigned wa = 5 * SLOT + (kq * 64 + rb2 * 32 + l31) * 272 + hb * 16;
#pragma unroll
    for (int gq = 0; gq < 4; gq++) {
      f32x4 v0 = {acc0[4 * gq], acc0[4 * gq + 1], acc0[4 * gq + 2], acc0[4 * gq + 3]};
      f32x4 v1 = {acc1[4 * gq], acc1[4 * gq + 1], acc1[4 * gq + 2], acc1[4 * gq + 3]};
      *(LAS f32x4*)(wa + gq * 32) = v0; *(LAS f32x4*)(wa + 128 + gq * 32) = v1;
    }
    BAR();
    const unsigned ra = 5 * SLOT + (tid >> 3) * 272 + (tid & 7) * 32;
    f32x4 s0 = *(LAS f32x4*)ra, s1 = *(LAS f32x4*)(ra + 16);
#pragma unroll
    for (int k = 1; k < 4; k++) { s0 += *(LAS f32x4*)(ra + k * 64 * 272); s1 += *(LAS f32x4*)(ra + k * 64 * 272 + 16); }
    asm volatile("s_waitcnt vmcnt(0)" : "+v"(xr)::"memory");
    u32x4 o;
    o[0] = pk2(s0[0] + blo(xr[0]), s0[1] + bhi(xr[0]));
    o[1] = pk2(s0[2] + blo(xr[1]), s0[3] + bhi(xr[1]));
    o[2] = pk2(s1[0] + blo(xr[2]), s1[1] + bhi(xr[2]));
    o[3] = pk2(s1[2] + blo(xr[3]), s1[3] + bhi(xr[3]));
    __builtin_amdgcn_raw_buffer_store_b128(o, mkrs(out, 2048 * 896), (unsigned)go, 0, 16);
    STAMP(9); STAMPC(15); TK(23); TK2(23); DUMPTK();
  };

  auto ffn_tail = [&](auto spec) AI {
    FRESH_LANE();
    TK2(0);
    const bool early = seen(fl, tag, 0xffull);
    if (!decltype(spec)::value) { LDA2(0); LDA2(1); LDA2(2); LDA2(3); }
    const unsigned hs = WS_AFRAG + (rbg * 144 + (decltype(spec)::value ? 16 + fi : 24 + tg) * 4 + 2 * role) * 1024 + lane * 16;
    u32x4 o[2];
#pragma unroll
    for (int t = 0; t < 2; t++) {
      const f32x16& a = t ? pa1 : pa0;
      float hv[8];
#pragma unroll
      for (int i = 0; i < 8; i++) {
        const float e = __builtin_amdgcn_exp2f(a[i] * fmaf(a[i] * a[i], k1, k0));
        hv[i] = (a[i] * a[i + 8]) * __builtin_amdgcn_rcpf(fmaf(e, ia2, ia2));
      }
      o[t] = u32x4{pk2(hv[0], hv[1]), pk2(hv[2], hv[3]), pk2(hv[4], hv[5]), pk2(hv[6], hv[7])};
      OPAQUE(o[t]); SB();
    }
    st16(rs, hs, o[0]); st16(rs, hs + 1024, o[1]);
    TK2(3);
    asm volatile("s_waitcnt vmcnt(%8)" : PIN8(0) : "n"(decltype(spec)::value ? 6 : 2) : "memory");
    TK2(4);
    if (!early) {
      poll(ws, WS_AFLAG + VFL, 0xffull, tag); LDA2(0); LDA2(1); LDA2(2); LDA2(3);
      asm volatile("s_waitcnt vmcnt(0)" : PIN8(0)::"memory");
    }
  };

  const float ninf = __uint_as_float(~mlo & 0xff800000u);
  if (!isf) {
    FRESH_LANE();
    ALD16(ca, vca, WS_CS); ALD16(cb, vcb, WS_CS); ALD16(stt, vst, WS_STAT);
    ITEM_W(5, 0); ITEM_W(5, 1); ITEM_W(6, 0); ITEM_W(6, 1);
    asm volatile("s_waitcnt vmcnt(4)" : "+v"(ca), "+v"(cb), "+v"(stt)::"memory");
    g_check<true>(rs, ca, cb, stt, vca, vcb, vst, tag, mlo);
    make_fb();
    pa0 = MFMA(fold(ca), fb, pa0); pa1 = MFMA(fold(cb), fb, pa1);
    Sm sm;
    u32x4 kf1 = pack8u(pa0, 1); kf1[2] &= mlo; kf1[3] &= mlo;
    sm.p = MFMA(wf[0], asf(pack8u(pa0, 0)), zero16());
    sm.p = MFMA(wf[1], asf(kf1), sm.p);
    const unsigned wa = 7 * SLOT + (role * 128 + 32 * rb + l31) * 192 + ((hb ^ (l31 >> 1 & 7)) << 3);
#define IMG(f, v) do { *(LAS u32x2*)((wa ^ ((f) & 1) * 32) + ((f) >> 1) * 64) = u32x2{v[0], v[1]}; *(LAS u32x2*)((wa ^ (((f) & 1) * 32 + 16)) + ((f) >> 1) * 64) = u32x2{v[2], v[3]}; } while (0)
    { u32x4 v0 = pack8u(pa1, 0), v1 = pack8u(pa1, 1); v1[2] = (v1[2] & mlo) | (split2(__builtin_amdgcn_rcpf(rstd)) & ~mlo); v1[3] &= mlo; IMG(4, v0); IMG(5, v1); }
    sm_max(sm, ninf); sm_min(sm, ninf); sm_consts(sm, rstd);
    const unsigned oc = WS_AFRAG + rbg * 144 * 1024 + lane * 16;
#pragma unroll
    for (int i = 0; i < 16; i++) {
      sm_exp(sm, i, ninf);
      if (i % 5 == 0 && i < 15) { st16(rs, oc + (i == 0 ? 96 + 2 * fi + role : 112 + 4 * fi + 2 * role + (i == 5)) * 1024, ONES); SB(); }
    }
    st16(rs, (notile ? WS_KVRED + ((tg - 1 + role) * 16 + m) * 1024 + rb * 256 : 0xf0000000u) + (lane < 16 ? lane * 16 : 0x8000000u), ONES);
    sm_sum(sm, rstd);
#pragma unroll
    for (int f = 0; f < 4; f++) { const u32x4 v = sm_norm(sm, f); IMG(f, v); }
    BAR();
    TK(13);
    if (w < 4) {
      unsigned ra = 7 * SLOT + (w >> 1) * 24576 + (8 * hb + q4) * 192 + (((4 * g16 + p4) ^ (4 * hb + (q4 >> 1))) << 3), rc = (ra ^ 16) + 768;
      unsigned rp = ra + (w & 1) * 64, rq = rc + (w & 1) * 64;
      OPAQUE(ra); OPAQUE(rc); OPAQUE(rp); OPAQUE(rq);
      f32x16 kv = zero16();
#pragma unroll
      for (int s = 0; s < 8; s++) kv = MFMA(trfrag(rp + s * 3072, rq + s * 3072), trfrag(ra + 128 + s * 3072, rc + 128 + s * 3072), kv);
      st16(rs, WS_KVPART + ((fi * 4 + w) * 16 + m) * 2048 + lane * 16, pack8u(kv, 0));
      st16(rs, WS_KVPART + ((fi * 4 + w) * 16 + m) * 2048 + 1024 + lane * 16, pack8u(kv, 1));
    }
    STAMP(2); TK(14);
    aW[1] = 8192 + g16 * 4096 + (8 * hb + q4) * 64 + ((role ^ g16) << 5) + p4 * 8; aW[3] = aW[1] + 65536;
    OPAQUE(aW[0]); OPAQUE(aW[1]); OPAQUE(aW[2]); OPAQUE(aW[3]);
    wq[0][1] = wfrag(0, 1, true); wq[0][0] = wfrag(0, 0); wq[1][1] = wfrag(1, 1, true); wq[1][0] = wfrag(1, 0);
    BAR();
    TK(15);
    passn(IC<3>{});
    {
      FRESH_LANE();
      TK2(0);
      unsigned cf;
      const unsigned vcf = (((2 * (tg | 1) + 1) * 8 + (tg & 1) * 4 + (lane >> 2 & 3)) * 16 + 8 * (ts & 1) + 4 * role + p4) * 64;
      ALD4(cf, vcf, WS_CFLAG);
      const bool early = seen(fl, tag, 0xffull);
      Sm sq;
      u32x4 q1 = pack8u(pa0, 1); q1[2] &= mlo; q1[3] &= mlo;
      sq.p = MFMA(wf[0], asf(pack8u(pa0, 0)), zero16());
      sq.p = MFMA(wf[1], asf(q1), sq.p);
      gconst();
      float hv[8];
#pragma unroll
      for (int i = 0; i < 8; i++) {
        const float e = __builtin_amdgcn_exp2f(pa1[i] * fmaf(pa1[i] * pa1[i], k1, k0));
        hv[i] = (pa1[i] * pa1[i + 8]) * __builtin_amdgcn_rcpf(fmaf(e, ia2, ia2));
      }
      const unsigned ob = WS_AFRAG + rbg * 144 * 1024 + lane * 16;
      asm volatile("s_waitcnt vmcnt(0)" : "+v"(cf)::"memory");
      st16(rs, ob + (96 + 2 * fi + role) * 1024, u32x4{pk2(hv[0], hv[1]), pk2(hv[2], hv[3]), pk2(hv[4], hv[5]), pk2(hv[6], hv[7])});
      if (!seen(cf, tag, ~0ull)) poll(ws, WS_CFLAG + vcf, ~0ull, tag);
      u32x4 kf[4], ph[4];
      const unsigned vk = (2 * fi + role) * 4096 + lane * 16;
#define LDK() do { ALD16(kf[0], vk, WS_KVRED); ALD16(kf[1], vk, WS_KVRED + 1024); ALD16(kf[2], vk, WS_KVRED + 2048); ALD16(kf[3], vk, WS_KVRED + 3072); } while (0)
#define PINK "+v"(kf[0]), "+v"(kf[1]), "+v"(kf[2]), "+v"(kf[3])
      sm_max(sq, ninf); sm_min(sq, ninf); sm_consts(sq, rstd);
#pragma unroll
      for (int i = 0; i < 16; i += 4) {
        sm_exp(sq, i, ninf); sm_exp(sq, i + 1, ninf); sm_exp(sq, i + 2, ninf); sm_exp(sq, i + 3, ninf);
        if (i == 0) LDA2(0); if (i == 4) LDA2(1); if (i == 8) LDA2(2); if (i == 12) LDA2(3);
        SB();
      }
      LDK();
      sm_sum(sq, 1.f);
#pragma unroll
      for (int f = 0; f < 4; f++) { ph[f] = sm_norm(sq, f); OPAQUE(ph[f]); }
      STAMP(7);
      asm volatile("s_waitcnt vmcnt(0)" : PINK, PIN8(0)::"memory");
#pragma unroll 1
      for (int it = 0; it < SPIN_CAP; ++it) {
        if (fresh(umax4(umax4(umax4(umax4(0u, kf[0]), kf[1]), kf[2]), kf[3]))) break;
        __builtin_amdgcn_s_sleep(8);
        LDK();
        asm volatile("s_waitcnt vmcnt(0)" : PINK::"memory");
      }
      STAMP(10);
      f32x16 nm = zero16();
#pragma unroll
      for (int i = 0; i < 4; i++) nm = MFMA(asf(kf[i]), asf(ph[i]), nm);
      float dl, dh; bcast32(nm[12] + nm[13], dl, dh);
      const float rd = __builtin_amdgcn_rcpf(dh + 1e-6f);
#pragma unroll
      for (int i = 0; i < 16; i++) nm[i] *= rd;
      u32x4 o1 = pack8u(nm, 1); o1[2] &= mlo; o1[3] &= mlo;
      st16(rs, ob + (112 + 4 * fi + 2 * role) * 1024, pack8u(nm, 0));
      st16(rs, ob + (113 + 4 * fi + 2 * role) * 1024, o1);
      STAMP(4); TK2(3);
      if (notile) {
        DUMPTK();
        if (bx == 24 * 8 + 1 && w == 0) {
#pragma unroll 1
          for (int i = 0; i < 8; i++) poll(ws, WS_AFLAG + (2 * i + hb) * 8 * 128 + l31 * 4, 0x00ffffff00ffffffull, tag);
          st4(rs, WS_EPOCH, tag);
        }
        __builtin_amdgcn_endpgm();
      }
      if (!early) {
        poll(ws, WS_AFLAG + VFL, 0xffull, tag); LDA2(0); LDA2(1); LDA2(2); LDA2(3);
        asm volatile("s_waitcnt vmcnt(0)" : PIN8(0)::"memory");
      }
    }
#pragma unroll
    for (int i = 0; i < 28; i++) asm volatile("" : "=v"(xf[i]));
    asm volatile("" : "=v"(wq[0][0]), "=v"(wq[0][1]), "=v"(wq[1][0]), "=v"(wq[1][1]), "=v"(pa0), "=v"(pa1));
  }
  int tso = ts; asm volatile("" : "+s"(tso));
  if (tso < 2) { passn(IC<1>{}); passn(IC<2>{}); ffn_tail(std::true_type{}); }
  phase2();
}

extern "C" void kernel_launch(void* const* d_in, const int* in_sizes, int n_in, void* d_out, int out_size, void* d_ws, size_t ws_size, hipStream_t stream) {
  (void)hipFuncSetAttribute((const void*)fused_kernel, hipFuncAttributeMaxDynamicSharedMemorySize, LDS_BYTES);
  fused_kernel<<<256, 512, LDS_BYTES, stream>>>((const char*)d_in[0], (const char*)d_in[1], (const char*)d_in[2], (const char*)d_in[3],
                                                (const char*)d_in[5], (const char*)d_in[6], (const char*)d_in[7], (char*)d_out, (char*)d_ws);
}
```

```cpp
#include <hip/hip_runtime.h>
#include <hip/hip_bf16.h>
#include <stdint.h>
#include <type_traits>
#pragma clang diagnostic ignored "-Wint-to-pointer-cast"
#pragma clang diagnostic ignored "-Warray-bounds"

#define DI __device__ __forceinline__
#define AI __attribute__((always_inline))
#define LAS __attribute__((address_space(3)))
#define GAS __attribute__((address_space(1)))
typedef __attribute__((ext_vector_type(8))) short s16x8;
typedef __attribute__((ext_vector_type(4))) short s16x4;
typedef __attribute__((ext_vector_type(16))) float f32x16;
typedef __attribute__((ext_vector_type(4))) float f32x4;
typedef __attribute__((ext_vector_type(2))) float f32x2;
typedef __attribute__((ext_vector_type(4))) unsigned u32x4;
typedef __attribute__((ext_vector_type(2))) unsigned u32x2;
typedef __attribute__((ext_vector_type(2))) __bf16 bf16x2;
typedef GAS unsigned gu32;
typedef __amdgpu_buffer_rsrc_t rsrc_t;
#define RLX_AGENT __ATOMIC_RELAXED, __HIP_MEMORY_SCOPE_AGENT

constexpr unsigned WS_EPOCH = 0;
constexpr unsigned WS_AFLAG = 16384;
constexpr unsigned WS_STAT = WS_AFLAG + 128 * 128;
constexpr unsigned WS_CS = WS_STAT + 2048 * 16;
constexpr unsigned WS_KVRED = 262144;
constexpr unsigned WS_KVPART = WS_KVRED + 131072;
constexpr unsigned WS_CFLAG = WS_KVPART + (1u << 20);
constexpr unsigned WS_AFRAG = WS_KVPART + (2u << 20);
constexpr unsigned WS_STAMP = WS_AFRAG + 64u * 144u * 1024u;
constexpr unsigned WS_BYTES = WS_STAMP;
static_assert(WS_CS + 4928 * 32 <= WS_KVRED, "ws map");

constexpr int LDS_BYTES = 163840;
constexpr unsigned SLOT = 16384;
constexpr unsigned LDS_MISC = 0, LDS_PHI = 2048;
constexpr int CODE_LINES = 240;
constexpr int SPIN_CAP = 1 << 14;

#define HOTA(c) (c)
#define HOTV(c) (c)
#define MFMA(a, b, c) __builtin_amdgcn_mfma_f32_32x32x16_bf16((a), (b), (c), 0, 0, 0)
#define WAITV(n) asm volatile("s_waitcnt vmcnt(" #n ")" ::: "memory")
#define BAR() do { asm volatile("s_waitcnt lgkmcnt(0)" ::: "memory"); __builtin_amdgcn_s_barrier(); asm volatile("" ::: "memory"); } while (0)
#define SB() __builtin_amdgcn_sched_barrier(0)
#define OPAQUE(v) asm volatile("" : "+v"(v))
template <int N> DI void waitv() { asm volatile("s_waitcnt vmcnt(%0)" ::"n"(N) : "memory"); }
template <int N> using IC = std::integral_constant<int, N>;
template <int I, int N, class F> DI void sfor(F&& f) { if constexpr (I < N) { f(IC<I>{}); sfor<I + 1, N>(f); } }

#define STAMP(i)
#define STAMPC(i)
#define TK(i)
#define TK2(i)
#define DUMPTK()

#define DMA1I(voff, rsrc, ldsb, LIMM, SOFF, tmp) asm volatile("s_add_u32 m0, %3, %4\n\ts_mov_b32 %0, %5\n\tbuffer_load_dwordx4 %1, %2, %0 offen lds" \
    : "=&s"(tmp) : "v"(voff), "s"(rsrc), "s"(ldsb), "n"(LIMM), "n"(SOFF) : "memory", "scc")
#define DMA1P(voff, rsrc, ldsb, LIMM, sa, sb, tmp) asm volatile("s_add_u32 m0, %3, %4\n\ts_add_u32 %0, %5, %6\n\tbuffer_load_dwordx4 %1, %2, %0 offen lds" \
    : "=&s"(tmp) : "v"(voff), "s"(rsrc), "s"(ldsb), "n"(LIMM), "s"(sa), "s"(sb) : "memory", "scc")
#define DMA1M(voff, rsrc, ldsb, LIMM, sa, sb, tmp) asm volatile("s_add_u32 m0, %3, %4\n\ts_mul_i32 %0, %6, %5\n\tbuffer_load_dwordx4 %1, %2, %0 offen lds" \
    : "=&s"(tmp) : "v"(voff), "s"(rsrc), "s"(ldsb), "n"(LIMM), "n"(sa), "s"(sb) : "memory", "scc")
#define DMA2(voff, rsrc, soff) asm volatile("buffer_load_dwordx4 %0, %1, %2 offen offset:1024 lds" :: "v"(voff), "s"(rsrc), "s"(soff) : "memory")
#define ALD16(dst, voff, soff) asm volatile("buffer_load_dwordx4 %0, %1, %2, %3 offen sc1" : "=v"(dst) : "v"(voff), "s"(rs), "s"(soff) : "memory")
#define ALD4(dst, voff, soff) asm volatile("buffer_load_dword %0, %1, %2, %3 offen sc1" : "=v"(dst) : "v"(voff), "s"(rs), "s"(soff) : "memory")

#define FRESH_LANE() int lane; asm volatile("v_mbcnt_lo_u32_b32 %0, -1, 0\n\tv_mbcnt_hi_u32_b32 %0, -1, %0" : "=v"(lane)); \
  const int g16 = (lane >> 4) & 1, q4 = (lane & 15) >> 2, p4 = lane & 3, l31 = lane & 31, hb = lane >> 5, rl = lane >> 3; \
  (void)g16; (void)q4; (void)p4; (void)l31; (void)hb; (void)rl
DI unsigned usel(bool c, unsigned a, unsigned b) { const unsigned k = c ? ~0u : 0u; return (a & k) | (b & ~k); }
DI const char* psel(bool c, const char* a, const char* b) { return c ? a : b; }
DI rsrc_t mkoff(const char* p, unsigned off, unsigned bytes) { return __builtin_amdgcn_make_buffer_rsrc((void*)(p + off), 0, bytes - off, 0x00020000); }
DI rsrc_t mkrs(const void* p, unsigned bytes) { return __builtin_amdgcn_make_buffer_rsrc((void*)p, 0, bytes, 0x00020000); }
DI s16x8 trfrag(unsigned a0, unsigned a1) {
  s16x4 lo = __builtin_amdgcn_ds_read_tr16_b64_v4i16((LAS s16x4*)a0);
  s16x4 hi = __builtin_amdgcn_ds_read_tr16_b64_v4i16((LAS s16x4*)a1);
  return __builtin_shufflevector(lo, hi, 0, 1, 2, 3, 4, 5, 6, 7);
}
DI unsigned pk2(float a, float b) { f32x2 p = {a, b}; return __builtin_bit_cast(unsigned, __builtin_convertvector(p, bf16x2)); }
DI float dot2(unsigned a, unsigned b, float c) { return __builtin_amdgcn_fdot2_f32_bf16(__builtin_bit_cast(bf16x2, a), __builtin_bit_cast(bf16x2, b), c, false); }
DI float blo(unsigned u) { return __uint_as_float(u << 16); }
DI float bhi(unsigned u) { return __uint_as_float(u & 0xffff0000u); }
DI float fand(float v, unsigned m) { return __uint_as_float(__float_as_uint(v) & m); }
DI float vmul(float a, float b) { float r = a * b; OPAQUE(r); return r; }
DI float vadd(float a, float b) { float r = a + b; OPAQUE(r); return r; }
DI float max3(float a, float b, float c) { return __builtin_elementwise_maximum(__builtin_elementwise_maximum(a, b), c); }
DI float min3(float a, float b, float c) { return __builtin_elementwise_minimum(__builtin_elementwise_minimum(a, b), c); }
DI float add32(float a, float b) { auto r = __builtin_amdgcn_permlane32_swap(__float_as_uint(a), __float_as_uint(b), false, false); return __uint_as_float(r[0]) + __uint_as_float(r[1]); }
DI float add16(float a, float b) { auto r = __builtin_amdgcn_permlane16_swap(__float_as_uint(a), __float_as_uint(b), false, false); return __uint_as_float(r[0]) + __uint_as_float(r[1]); }
DI void bcast32(float v, float& lo, float& hi) { auto r = __builtin_amdgcn_permlane32_swap(__float_as_uint(v), __float_as_uint(v), false, false); lo = __uint_as_float(r[0]); hi = __uint_as_float(r[1]); }
template <int CTRL> DI float dpp(float x) { return __builtin_bit_cast(float, __builtin_amdgcn_mov_dpp(__builtin_bit_cast(int, x), CTRL, 0xf, 0xf, true)); }
DI float wave_sum(float v) {
  v += dpp<0xB1>(v); v += dpp<0x4E>(v); v += dpp<0x141>(v); v += dpp<0x128>(v);
  v = add16(v, v); return add32(v, v);
}
DI unsigned split2(float v) {
  const unsigned h = pk2(v, 0.f);
  return pk2(__uint_as_float(h << 16), v - __uint_as_float(h << 16));
}
DI u32x4 pack8u(const f32x16& x, int s) {
  u32x4 u = {pk2(x[8 * s], x[8 * s + 1]), pk2(x[8 * s + 2], x[8 * s + 3]), pk2(x[8 * s + 4], x[8 * s + 5]), pk2(x[8 * s + 6], x[8 * s + 7])};
  return u;
}
DI s16x8 asf(u32x4 u) { return __builtin_bit_cast(s16x8, u); }
DI unsigned rmask(unsigned row) { return (((row >> 1) & 1) << 2) | ((row >> 2) & 1) | ((row >> 3) & 2); }
DI f32x16 zero16() { f32x16 z; for (int i = 0; i < 16; i++) z[i] = 0.f; return z; }
DI void st16(rsrc_t rs, unsigned off, u32x4 v) { __builtin_amdgcn_raw_buffer_store_b128(v, rs, off, 0, 16); }
DI void st4(rsrc_t rs, unsigned off, unsigned v) { __builtin_amdgcn_raw_buffer_store_b32(v, rs, off, 0, 16); }
DI u32x4 ld16(rsrc_t rs, unsigned off) { return __builtin_amdgcn_raw_buffer_load_b128(rs, off, 0, 16); }
DI bool seen(unsigned v, unsigned tag, unsigned long long need) { return (__builtin_amdgcn_ballot_w64(v == tag) & need) == need; }
DI void poll(char* ws, unsigned off, unsigned long long need, unsigned tag) {
#pragma unroll 1
  for (int it = 0; it < SPIN_CAP; ++it) {
    const unsigned v = __hip_atomic_load((gu32*)(ws + off), RLX_AGENT);
    if (seen(v, tag, need)) break;
    __builtin_amdgcn_s_sleep(2);
  }
  __builtin_amdgcn_fence(__ATOMIC_ACQUIRE, "wavefront");
}
template <bool ST> DI void g_check(rsrc_t rs, u32x4& a, u32x4& b, u32x4& st, unsigned va, unsigned vb, unsigned vs, unsigned tag, unsigned mlo) {
#pragma unroll 1
  for (int it = 0; it < SPIN_CAP; ++it) {
    unsigned bad = (a[1] ^ tag) | (b[1] ^ tag) | (((a[3] ^ tag) | (b[3] ^ tag)) & mlo);
    if (ST) bad |= (st[1] ^ tag) | (st[3] ^ tag);
    if (__builtin_expect(__builtin_amdgcn_ballot_w64(bad != 0) == 0ull, 1)) break;
    ALD16(a, va, WS_CS); ALD16(b, vb, WS_CS); if (ST) ALD16(st, vs, WS_STAT);
    if (ST) asm volatile("s_waitcnt vmcnt(0)" : "+v"(a), "+v"(b), "+v"(st)::"memory"); else asm volatile("s_waitcnt vmcnt(0)" : "+v"(a), "+v"(b)::"memory");
  }
}

DI unsigned umax4(unsigned m, const u32x4& v) { return max(max(max(m, v[0]), v[1]), max(v[2], v[3])); }
DI bool fresh(unsigned m) { return __builtin_amdgcn_ballot_w64(m == 0xffffffffu) == 0ull; }

struct Sm { float L, cx, cn, mx, mn, s1, s2; f32x16 p, ep, en; };
DI void sm_max(Sm& s, float ninf) {
  const f32x16& p = s.p;
  float a = max3(p[0], p[1], p[2]);
  for (int i = 3; i < 11; i += 2) a = max3(a, p[i], p[i + 1]);
  s.mx = max3(a, p[11], max3(max3(p[12], p[13], p[14]), p[15], p[15]) + ninf);
}
DI void sm_min(Sm& s, float ninf) {
  const f32x16& p = s.p;
  float b = min3(p[0], p[1], p[2]);
  for (int i = 3; i < 11; i += 2) b = min3(b, p[i], p[i + 1]);
  s.mn = min3(b, p[11], min3(min3(p[12], p[13], p[14]), p[15], p[15]) - ninf);
}
DI void sm_consts(Sm& s, float sc) {
  auto r = __builtin_amdgcn_permlane32_swap(__float_as_uint(s.mx), __float_as_uint(-s.mn), false, false);
  const float m = max3(__uint_as_float(r[0]), __uint_as_float(r[1]), __uint_as_float(r[1]));
  float mx, nmn; bcast32(m, mx, nmn);
  s.L = sc * 1.4426950408889634f; s.cx = -mx * s.L; s.cn = -nmn * s.L; s.s1 = 0.f; s.s2 = 0.f;
}
DI void sm_exp(Sm& s, int i, float ninf) {
  if (i == 12) { s.cx += ninf; s.cn += ninf; }
  s.ep[i] = __builtin_amdgcn_exp2f(fmaf(s.p[i], s.L, s.cx)); s.en[i] = __builtin_amdgcn_exp2f(fmaf(s.p[i], -s.L, s.cn));
  if (i > 0) { s.s1 = vadd(s.s1, s.ep[i - 1]); s.s2 = vadd(s.s2, s.en[i - 1]); }
}
DI void sm_sum(Sm& s, float os) {
  const float t = add32(vadd(s.s1, s.ep[15]), vadd(s.s2, s.en[15]));
  bcast32(os * __builtin_amdgcn_rcpf(t), s.s1, s.s2);
}
DI void sm_norm2(const Sm& s, int f, int h, u32x4& u) {
  const f32x16& e = f < 2 ? s.ep : s.en; const float r = f < 2 ? s.s1 : s.s2; const int o = 8 * (f & 1);
  for (int i = 2 * h; i < 2 * h + 2; i++) u[i] = pk2(vmul(e[o + 2 * i], r), vmul(e[o + 2 * i + 1], r));
}
DI u32x4 sm_norm(const Sm& s, int f) {
  const f32x16& e = f < 2 ? s.ep : s.en; const float r = f < 2 ? s.s1 : s.s2; const int o = 8 * (f & 1);
  u32x4 u;
  for (int i = 0; i < 4; i++) u[i] = pk2(vmul(e[o + 2 * i], r), vmul(e[o + 2 * i + 1], r));
  return u;
}

struct P2 {
  static constexpr int a0(int s) { return s <= 1 ? 7 + s : s <= 3 ? 8 : s + 5 < 18 ? s + 5 : 18; }
  static constexpr int vs(int j) { return j + 1; }
  static constexpr int vj(int s) { for (int j = 0; j < 8; j++) if (vs(j) == s) return j; return -1; }
  static constexpr int FL2 = 2, N2 = 7;
  static constexpr bool bar(int c) { return c == 1 || c == 3 || c == 5 || c == 7 || c == 12; }
  static constexpr int vneed(int c) { return c == 5 ? 9 : c == 7 ? 12 : c == 12 ? 17 : -1; }
  static constexpr int na(int s) { return 2 * (a0(s + 1) - a0(s)); }
  static constexpr int nops(int s) { return (vj(s) >= 0 ? 2 : 0) + na(s) + (s == 13 || s == FL2 ? 1 : 0); }
  static constexpr int upto(int from, int c) { int n = 0; for (int s = from; s < c; s++) n += nops(s); return n; }
  static constexpr int wait(int c) {
    int n = 63;
    if (c >= 4 && c < 7) n = 2 * (6 - c) + upto(0, c);
    for (int s = 0; s < c; s++) if (a0(s) <= c && c < a0(s + 1)) n = 2 * (a0(s + 1) - 1 - c) + upto(s + 1, c);
    if (c == N2) { const int f = upto(FL2, c) - 1; if (f < n) n = f; }
    if (vneed(c) >= 10) { const int s = vs(vneed(c) - 10), v = na(s) + upto(s + 1, c); if (v < n) n = v; }
    return n > 63 ? 63 : n;
  }
};

__global__ __launch_bounds__(512, 2) void fused_kernel(const char* __restrict__ x, const char* __restrict__ wqkv, const char* __restrict__ wphi,
                                                       const char* __restrict__ wo, const char* __restrict__ wg, const char* __restrict__ wu,
                                                       const char* __restrict__ wd, char* __restrict__ out, char* ws) {
  const int lane = threadIdx.x & 63, w = __builtin_amdgcn_readfirstlane(threadIdx.x >> 6);
  const int bx = blockIdx.x, xcd = bx & 7, r = bx >> 3;
  const int tg = xcd & 3, m = (xcd >> 2) * 8 + (r & 7), ts = r >> 3;
  const int g16 = (lane >> 4) & 1, q4 = (lane & 15) >> 2, p4 = lane & 3, l31 = lane & 31, hb = lane >> 5, rl = lane >> 3;
  asm volatile("" :: "s"(x), "s"(wqkv), "s"(wphi), "s"(wo), "s"(wg), "s"(wu), "s"(wd), "s"(out), "s"(ws));
  STAMP(0); STAMPC(14); TK(0);
  unsigned st0, st1;
  const unsigned ldsw = w * 2048;
  const unsigned voffX = (16 * w + rl) * 896 + (((lane & 7) ^ rmask(16 * w + rl)) << 4), voffX2 = voffX + 8 * 896 - 1024;
  const rsrc_t rsX = mkrs(x + (size_t)m * 128 * 896, 128 * 896);
#define ITEM_X(c, h) do { if (!(h)) DMA1I(voffX, rsX, ldsw, (7 + ((c) + 2) % 3) * SLOT, (c) * 128, st0); else DMA2(voffX2, rsX, st0); } while (0)
#define ITEM_V(c, h) do { if (!(h)) DMA1I(voffX, rsV, ldsw, (((c) + 7) % 10) * SLOT, (c) * (128 * 896), st0); else DMA2(voffX2, rsV, st0); } while (0)
  ITEM_X(0, 0); ITEM_X(0, 1);

  const bool isf = ts < 2;
  const int fi = 2 * tg + (ts & 1);
  const int rb = w & 3, role = w >> 2;
#define VOFF(f) ((16 * rb + rl) * ((f) ? 3584 : 2688) + (usel(f, (((((lane & 7) >> 1) ^ ((rl >> 1) & 1) ^ (role << 1)) << 1) | (lane & 1)), min((lane & 7) ^ (rl << 1 & 4), 6)) << 4))
  const char* wgu = psel(role, wu, wg);
  unsigned voffW = VOFF(isf), wmul = isf ? 1792 : 1344;
  unsigned voffW2 = voffW + wmul * 16 - 1024;
  rsrc_t rsW = mkoff(psel(isf, wgu, wqkv), fi * (isf ? 128 : 112) + (isf ? 0 : 896 + 896 * role), wmul * 896);
#define ITEM_W(c, h) do { if (!(h)) DMA1M(voffW, rsW, ldsw, (c) * SLOT, (c) * 128, wmul, st1); else DMA2(voffW2, rsW, st1); } while (0)
  ITEM_W(0, 0); ITEM_W(0, 1); ITEM_X(1, 0); ITEM_X(1, 1); ITEM_W(1, 0); ITEM_W(1, 1); ITEM_X(2, 0); ITEM_X(2, 1);
  unsigned pfc;
  {
    unsigned long long pc0; asm volatile("s_getpc_b64 %0" : "=s"(pc0));
    asm volatile("global_load_dword %0, %1, %2" : "=v"(pfc) : "v"(min(r * 8 + w + (lane & 1) * 256, CODE_LINES) * 128), "s"(pc0 & ~127ull) : "memory");
  }

  const rsrc_t rs = mkrs(ws, WS_BYTES);
  unsigned tag;
  const int pu = (xcd >> 2) * 32 + r, li = pu / 3;
  const int la = li >= 6, lj = li - 6 * la, ll = 2 * tg + (li & 1);
  const int lc = li < 12 ? 21 + 28 * (lj & 1) + 8 * (lj >> 1) + 2 * tg + la : li < 14 ? 45 + 28 * (li & 1) + tg : 7 * ((li - 14) >> 1) + ll;
  const int cpart = li >= 14 && (ll > 6 || li > 19) ? 3 : pu - 3 * li;
  const int srow = (xcd * 32 + r) * 8 + w;
  const int tidx = (xcd >> 1) * 56 + (xcd & 1) * 32 + r;
  const bool notile = ts == 3 && (xcd & 1);
  const int rt = notile ? 0 : tidx / 7, ct = notile ? 0 : tidx - 7 * rt;
  const rsrc_t rsV = mkrs(wd + ct * 128, notile ? 0 : 1792 * 896 - ct * 128);
  const rsrc_t rsP = mkrs(wphi + 4 * tg * 1568, isf ? 0 : 4 * 1568);
  const unsigned mlo = lane < 32 ? 0xffffffffu : 0u;
  const unsigned lp = w * 1024;

  const int rbg = m * 4 + rb;
  const int rb2 = w & 1, kq = w >> 1;
#define VFL (((rt * 2 + rb2) * 2 + (kq & 1)) * 128 + l31 * 4)
#define AB ((notile ? 0xe0000000u : ((rt * 2 + rb2) * 144 + 2 * kq) * 1024) + lane * 16)
  u32x4 af[36];
#define LDA2(c) asm volatile("s_mov_b32 %2, %5\n\tbuffer_load_dwordx4 %0, %3, %4, %2 offen sc1\n\tbuffer_load_dwordx4 %1, %3, %4, %2 offen offset:1024 sc1" \
    : "=&v"(af[2 * (c)]), "=&v"(af[2 * (c) + 1]), "=&s"(st1) : "v"(AB), "s"(rs), "n"(WS_AFRAG + (c) * 8192) : "memory")
#define PIN8(c) "+v"(af[2 * (c)]), "+v"(af[2 * (c) + 1]), "+v"(af[2 * (c) + 2]), "+v"(af[2 * (c) + 3]), "+v"(af[2 * (c) + 4]), "+v"(af[2 * (c) + 5]), "+v"(af[2 * (c) + 6]), "+v"(af[2 * (c) + 7])
#define NEED(mask) do { if (!seen(fl, tag, mask)) poll(ws, WS_AFLAG + VFL, mask, tag); } while (0)
#define ONES (u32x4{~0u, ~0u, ~0u, ~0u})
  f32x16 pa0, pa1;
  float rstd;
  unsigned fl = 0;

  const unsigned xrow = 32 * rb + l31;
  const unsigned vst = (m * 128 + xrow) * 16;
  s16x8 xf[28], wq[3][2], fb, wf[2];
  u32x4 stt, ca, cb;
#define AW_F() (g16 * 8192 + (8 * hb + q4) * 128 + (((2 * role) ^ ((q4 >> 1) & 1) ^ (g16 << 1)) * 32) + p4 * 8)
#define AW_A(half, sel) ((half) * 8192 + (8 * hb + q4) * 128 + ((56 * (sel) + 32 * g16 + 8 * p4) ^ (q4 << 5 & 64)))
#define VCS(f, col) ((g16 * ((f) ? 1792 : 16) + (lane & 15)) * 32 + hb * 16 + (col) * 32)
  unsigned aW[4], vca, vcb;
  aW[0] = usel(isf, AW_F(), AW_A(0, role)); aW[1] = aW[0] ^ (isf ? 32 : 8192); aW[2] = aW[0] + 65536; aW[3] = aW[1] + 65536;
  OPAQUE(aW[0]); OPAQUE(aW[1]); OPAQUE(aW[2]); OPAQUE(aW[3]);
  auto fold = [&](u32x4 c) AI { const unsigned c2 = c[2] & mlo; u32x4 u = {c[0], c2, c[0], c2}; return asf(u); };
  auto make_fb = [&]() AI { const unsigned a = stt[0] & 0xffffu, b = stt[0] >> 16; u32x4 u = {a | (a << 16), a | (a << 16), b | (b << 16), b | (b << 16)}; fb = asf(u); rstd = __uint_as_float(stt[2]); };
  auto wfrag = [&](int n, int t, bool narrow = false) AI {
    const int sl = (n >> 2) % 7;
    const unsigned o = (sl & 3) * SLOT + (n & 3) * (narrow ? 1024 : 2048), b = aW[(sl >> 2) * 2 + t];
    return trfrag(b + o, b + o + (narrow ? 256 : 512));
  };

  {
    u32x4 px, pw[3];
    TK(1);
    unsigned aX[4];
#pragma unroll
    for (int ks = 0; ks < 4; ks++) { aX[ks] = 7 * SLOT + xrow * 128 + (((2 * ks + hb) ^ rmask(xrow)) << 4); OPAQUE(aX[ks]); }
    auto xread = [&](int n) AI { xf[n] = *(LAS s16x8*)(aX[n & 3] + (((n >> 2) + 2) % 3) * SLOT); };
    f32x16 acc0 = zero16(), acc1 = zero16();
    const unsigned m56 = lane < 56 ? 0xffffffffu : 0u;
    float cs[8], s = 0.f, q = 0.f; unsigned pfd;
    const bool hw = !isf && role;
    unsigned voffN = usel(hw, (32 * (w & 1) + (lane >> 2)) * 3584 + ((p4 ^ (w & 2)) << 4), voffW); OPAQUE(voffN);
    const char* pcb = psel(lc < 21, wqkv, psel(lc < 49, wg, wu)) + (lc - (lc < 21 ? 0 : lc < 49 ? 21 : 49)) * 128;
    const unsigned ppitch = lc < 21 ? 2688 : 3584;
    rsrc_t rsC = mkoff(pcb, min(152 * cpart + 8 * w, 440) * ppitch, 447 * ppitch + 128);
    unsigned pst = 64 * ppitch;
    const char* pxr = x + (size_t)srow * 896;
    rsrc_t rsN = mkoff(psel(isf, wgu, psel(hw, psel(w & 2, wu, wg), wqkv)), usel(isf, (8 + fi) * 128, usel(hw, 3072 + 64 * fi, 112 * fi)), (isf || hw ? 1792 : 1344) * 896);
    const int u = bx * 8 + w;
    rsrc_t rsF = mkrs(psel(u < 1568, wd, wo) + (u < 1568 ? u : u - 1568) * 1024, u < 1960 ? 1024 : 0);
    unsigned pha = LDS_PHI + (4 * hb + q4) * 56 + g16 * 32 + p4 * 8 + role * 1568;
    asm volatile("" : "+s"(rsC), "+s"(pst), "+s"(pxr), "+s"(rsN), "+s"(rsF)); OPAQUE(pha);
    asm volatile("" :: "s"(rsV), "s"(rt), "s"(ct));
    WAITV(7);
    BAR();
    TK(4);
    xread(0); wq[0][1] = wfrag(0, 1); wq[0][0] = wfrag(0, 0);
    xread(1); wq[1][1] = wfrag(1, 1); wq[1][0] = wfrag(1, 0);
    auto pre = [&](int p) AI {
      if (p == 2) {
        asm volatile("buffer_load_dword %0, off, %1, 0 sc1" : "=v"(tag) : "s"(rs) : "memory");
        asm volatile("global_load_dwordx4 %0, %1, %2" : "=v"(px) : "v"(min(lane, 55) * 16), "s"(pxr) : "memory");
      }
      if (p == 3) {
        unsigned vo = rl * ppitch + (lane & 7) * 16;
        asm volatile("buffer_load_dwordx4 %0, %1, %2, 0 offen" : "=v"(pw[0]) : "v"(vo), "s"(rsC) : "memory"); vo += pst;
        asm volatile("buffer_load_dwordx4 %0, %1, %2, 0 offen" : "=v"(pw[1]) : "v"(vo), "s"(rsC) : "memory"); vo += pst;
        asm volatile("buffer_load_dwordx4 %0, %1, %2, 0 offen" : "=v"(pw[2]) : "v"(vo), "s"(rsC) : "memory");
      }
      if (p == 12) { tag += 1; for (int i = 0; i < 4; i++) px[i] &= m56; }
      if (p == 13) { for (int i = 0; i < 4; i++) { s = dot2(px[i], 0x3f803f80u, s); q = dot2(px[i], px[i], q); } OPAQUE(s); OPAQUE(q); }
      if (p == 14) { s = add32(s, q); s += dpp<0xB1>(s); OPAQUE(s); }
      if (p == 15) { s += dpp<0x4E>(s); s += dpp<0x141>(s); OPAQUE(s); }
      if (p == 16) { s += dpp<0x128>(s); s = add16(s, s); OPAQUE(s); }
      if (p == 17) { bcast32(s * (1.f / 448.f), s, q); q = fmaxf(fmaf(-s, s, q), 0.f) + 1e-5f; OPAQUE(s); OPAQUE(q); }
      if (p == 18) {
        u32x4 o = {split2(-s), tag, __float_as_uint(__builtin_amdgcn_rsqf(q)), tag};
        st16(rs, lane == 0 ? WS_STAT + srow * 16 : 0xfffffff0u, o);
      }
      if (p >= 15 && p < 23) {
        const int i = (p - 15) >> 1;
        if (p & 1) { pw[2][i] &= (w + 16 < (cpart == 2 ? 18 : 19) ? 0xffffffffu : 0u); cs[2 * i] = vadd(vadd(blo(pw[0][i]), blo(pw[1][i])), blo(pw[2][i])); }
        else cs[2 * i + 1] = vadd(vadd(bhi(pw[0][i]), bhi(pw[1][i])), bhi(pw[2][i]));
      }
      if (p == 23 || p == 24) for (int i = 2 * (p - 23); i < 2 * (p - 22); i++) { cs[i] = add32(cs[i], cs[i + 4]); OPAQUE(cs[i]); }
      if (p == 25) for (int i = 0; i < 2; i++) { cs[i] = add16(cs[i], cs[i + 2]); OPAQUE(cs[i]); }
      if (p == 26) for (int i = 0; i < 2; i++) { cs[i] += dpp<0x128>(cs[i]); OPAQUE(cs[i]); }
      if (p == 27) {
        *(LAS f32x2*)(LDS_MISC + w * 256 + ((lane & 7) * 8 + hb * 4 + g16 * 2) * 4) = f32x2{cs[0], cs[1]};
        *(LAS u32x2*)(LDS_PHI + 4 * 1568 + lane * 8) = u32x2{0u, 0u};
      }
    };
    sfor<0, 28>([&](auto nn) AI {
      constexpr int n = decltype(nn)::value;
      constexpr int c = (n + 2) >> 2, j = (n + 2) & 3;
      if (j == 0) {
        if (c == 2) asm volatile("s_waitcnt vmcnt(7)" : "+v"(tag), "+v"(px), "+v"(pw[0]), "+v"(pw[1]), "+v"(pw[2]) : "v"(pfc) : "memory");
        if (c == 1) WAITV(10); if (c == 3) WAITV(7); if (c == 4) WAITV(6); if (c == 5 || c == 6) WAITV(9);
        if (c == 7) asm volatile("s_waitcnt vmcnt(4)" :: "v"(pfd) : "memory");
        BAR();
        TK(4 + c);
      }
      if (n == 14 && w == 7 && cpart < 3) {
        float t = 0.f;
        for (int i = 0; i < 8; i++) t += *(LAS float*)(LDS_MISC + i * 256 + lane * 4);
        u32x2 o = {split2(t), tag};
        __builtin_amdgcn_raw_buffer_store_b64(o, rs, WS_CS + (lc * 64 + lane) * 32 + cpart * 8, 0, 16);
      }
#define P0_ISSUE(h) do { \
      if (n == 0 || n == 3) ITEM_W(n == 0 ? 2 : 3, h); \
      if (c >= 1 && c <= 4 && j == 0) ITEM_X(c + 2, h); \
      if (c >= 1 && c <= 3 && j == 2) ITEM_W(c + 3, h); \
      if (c == 4 && j >= 2) ITEM_W(j - 1, h); \
      if (c == 5 && j == 0) ITEM_W(0, h); \
      if (c == 5 && j == 2) ITEM_W(3, h); \
      if (c == 6 && j == 0) ITEM_W(4, h); } while (0)
      SB();
      if (n == 27) pa0 = MFMA(wq[n % 3][0], xf[n], acc0); else acc0 = MFMA(wq[n % 3][0], xf[n], acc0);
      SB();
      if (n + 2 < 28) xread(n + 2);
      wq[(n + 2) % 3][1] = wfrag(n + 2, 1);
      pre(2 * n);
      P0_ISSUE(0);
      if (n == 5) DMA1I(lp + lane * 16, rsP, lp, LDS_PHI, 0, st0);
      if (n == 15) {
        asm volatile("buffer_load_dword %0, %1, %2, 0 offen" : "=v"(pfd) : "v"((lane & 7) * 128), "s"(rsF) : "memory");
      }
      if (n == 23) { vca = VCS(isf, fi * (isf ? 64 : 56) + role * (isf ? 32 : 28) + (isf ? 1344 : 448)); vcb = vca + (isf ? 512 : 448 * 32); }
      SB();
      if (n == 27) pa1 = MFMA(wq[n % 3][1], xf[n], acc1); else acc1 = MFMA(wq[n % 3][1], xf[n], acc1);
      SB();
      wq[(n + 2) % 3][0] = wfrag(n + 2, 0);
      pre(2 * n + 1);
      P0_ISSUE(1);
      if (n == 14) {
        wmul = isf || hw ? 1792 : 1344;
        rsW = rsN;
        voffW = voffN; voffW2 = voffW + (hw ? 16 * 3584 : wmul * 16) - 1024;
      }
      if (n == 15) {
        const unsigned pa = pha + (ts & 1) * 3136;
        wf[0] = trfrag(pa, pa + 448); wf[1] = trfrag(pa + 896, pa + 1344);
      }
      SB();
    });
    wq[0][0] = wq[1][0]; wq[0][1] = wq[1][1]; wq[1][0] = wq[2][0]; wq[1][1] = wq[2][1];
    STAMP(1); TK(12);
  }

  float k0, k1, ia2;
  auto gconst = [&]() AI {
    const float c0 = -2.f * 0.7978845608028654f * 1.4426950408889634f;
    k0 = vmul(c0, rstd); k1 = vmul(c0 * 0.044715f * rstd * rstd, rstd); ia2 = __builtin_amdgcn_rcpf(rstd * rstd);
  };
  auto passn = [&](auto kind) AI {
    constexpr int K = decltype(kind)::value;
    constexpr bool F = K == 1, L = K != 1, G = K != 3;
    FRESH_LANE();
    const unsigned hb0 = WS_AFRAG + (rbg * 144 + 2 * role) * 1024 + lane * 16;
    const unsigned ho = hb0 + (F ? fi : 8 + fi) * 4096;
    unsigned fo = 0;
    if (K == 3) {
      const unsigned f0 = WS_CFLAG + ((fi * 8 + w) * 16 + m) * 64, fa = WS_AFLAG + rbg * 256 + (8 + 2 * fi + role) * 4, fb2 = fa + 128;
      fo = lane == 0 ? f0 : 0xffffff00u; fo = lane == 1 ? fa : fo; fo = lane == 2 ? fb2 : fo;
      OPAQUE(fo);
    }
    const rsrc_t rsB = mkoff(wgu, (16 + fi) * 128, 448 * 3584);
    f32x16 acc0 = zero16(), acc1 = zero16();
    f32x16 qa0, qa1;
    float gt[2], gu[2], hprev; u32x4 hq;
    auto geglu = [&](int hs) AI {
#pragma unroll
      for (int e = 0; e < 16; e++) {
        const int d = hs - (F ? 14 + 9 * e / 4 : 4 + 12 * e / 5);
        const float Gv = e < 8 ? qa0[e & 7] : qa1[e & 7], U = e < 8 ? qa0[(e & 7) + 8] : qa1[(e & 7) + 8];
        if (d == 0) gt[e & 1] = vmul(Gv, fmaf(Gv * Gv, k1, k0));
        if (d == 1) { gt[e & 1] = __builtin_amdgcn_exp2f(gt[e & 1]); gu[e & 1] = vmul(Gv, U); }
        if (d == 2) gt[e & 1] = __builtin_amdgcn_rcpf(fmaf(gt[e & 1], ia2, ia2));
        if (d == 3) {
          if (e & 1) hq[(e >> 1) & 3] = pk2(hprev, gu[1] * gt[1]); else hprev = vmul(gu[0], gt[0]);
          if ((e & 7) == 7) st16(rs, ho + (e >> 3) * 1024, hq);
        }
      }
    };
    if (F) { ALD16(ca, vca, WS_CS); ALD16(cb, vcb, WS_CS); ALD16(stt, vst, WS_STAT); }
    sfor<0, 28>([&](auto nn) AI {
      constexpr int n = decltype(nn)::value;
      if (K == 2 && n == 10) { WAITV(6); st4(rs, lane == 0 ? WS_AFLAG + (rbg * 2 + role) * 128 + fi * 4 : 0xffffff00u, tag); }
      if (K == 3 && n == 5) { WAITV(5); st4(rs, fo, tag); }
      if (n == 10) { BAR(); TK(K == 1 ? 13 : 17); }
      if (n == 18) {
        asm volatile("s_waitcnt vmcnt(%2)" : "+v"(ca), "+v"(cb) : "n"(K == 3 ? 6 : 8) : "memory"); BAR(); TK(K == 1 ? 14 : 18);
        if (K == 3 && notile) {
          const int gg = tg - 1 + role;
          poll(ws, WS_CFLAG + (((2 * gg + (m >> 3)) * 8 + (m >> 1 & 3)) * 16 + (lane & 15)) * 64, ~0ull, tag);
          const unsigned pb = WS_KVPART + ((gg * 8 + (m >> 1)) * 16 + (lane >> 4)) * 2048 + (m & 1) * 1024 + (rb * 16 + (lane & 15)) * 16;
          u32x4 pv[4];
#pragma unroll
          for (int i = 0; i < 4; i++) pv[i] = ld16(rs, pb + i * 8192);
          float t[8];
#pragma unroll
          for (int j = 0; j < 4; j++) {
            t[2 * j] = (blo(pv[0][j]) + blo(pv[1][j])) + (blo(pv[2][j]) + blo(pv[3][j]));
            t[2 * j + 1] = (bhi(pv[0][j]) + bhi(pv[1][j])) + (bhi(pv[2][j]) + bhi(pv[3][j]));
          }
#pragma unroll
          for (int j = 0; j < 8; j++) { t[j] = add32(t[j], t[j]); t[j] = add16(t[j], t[j]); }
          st16(rs, lane < 16 ? WS_KVRED + (gg * 16 + m) * 1024 + (rb * 16 + lane) * 16 : 0xf0000000u, u32x4{pk2(t[0], t[1]), pk2(t[2], t[3]), pk2(t[4], t[5]), pk2(t[6], t[7])});
          STAMP(6);
        }
      }
      if (L && n == 19) g_check<false>(rs, ca, cb, stt, vca, vcb, vst, tag, mlo);
      if (n == 26) { if (F) WAITV(10); BAR(); TK(K == 1 ? 15 : 19); }
      if (G && n == (F ? 5 : 0)) {
        if (F) asm volatile("s_waitcnt vmcnt(6)" : "+v"(ca), "+v"(cb), "+v"(stt)::"memory");
        g_check<F>(rs, ca, cb, stt, vca, vcb, vst, tag, mlo);
      }
      SB();
      if (n == 27) pa0 = MFMA(wq[n % 3][0], xf[n], acc0); else acc0 = MFMA(wq[n % 3][0], xf[n], acc0);
      SB();
      if (!L || n + 2 < 28) wq[(n + 2) % 3][1] = wfrag(n + 2, 1, K == 3);
      if (F && n == 5) make_fb();
      if (G && n == (F ? 6 : 1)) qa0 = MFMA(fold(ca), fb, pa0);
      if (L && n == 20) acc0 = MFMA(fold(ca), fb, acc0);
      if (G && n < 4) ITEM_W(5 + n / 2, n & 1);
      if (F && n == 4) rsW = rsB;
      if (F && n >= 4 && n < 8) ITEM_V((n - 4) / 2, n & 1);
      if (F && (n == 23 || n == 24)) ITEM_V(2, !(n & 1));
      if (K == 3 && n < 6) ITEM_V(n / 2, n & 1);
      if (L && n >= 10 && n < 16) ITEM_V((n - 4) / 2, n & 1);
      if (L && (n == 18 || n == 19)) ITEM_V(6, n & 1);
      if (L && (n == 21 || n == 22)) ITEM_V(7, !(n & 1));
      if (L && n >= 26) ITEM_V(n - 18, 0);
      if (F && n >= 10 && n < 16) ITEM_W((n - 10) / 2, n & 1);
      if (F && (n == 18 || n == 19)) ITEM_W(3, n & 1);
      if (F && (n == 21 || n == 22)) ITEM_W(4, !(n & 1));
      if (G) geglu(2 * n);
      SB();
      if (n == 27) pa1 = MFMA(wq[n % 3][1], xf[n], acc1); else acc1 = MFMA(wq[n % 3][1], xf[n], acc1);
      SB();
      if (!L || n + 2 < 28) wq[(n + 2) % 3][0] = wfrag(n + 2, 0);
      if (F && n == 5) gconst();
      if (G && n == (F ? 6 : 1)) qa1 = MFMA(fold(cb), fb, pa1);
      if (L && n == 20) acc1 = MFMA(fold(cb), fb, acc1);
      if (F && (n == 0 || n == 7 || n == 17 || n == 20)) st16(rs, ho + (n < 8 ? 8 : 16) * 4096 + (n & 1) * 1024, ONES);
      if (K == 2 && n == 21) ALD4(fl, VFL, WS_AFLAG);
      if (n == 7) {
        if (G) { vca += 8 * 2048; vcb += 8 * 2048; }
        else { vca -= 448 * 32; vcb = VCS(true, 1344 + 1536 + 32 * fi + 16 * role); }
      }
      if (n == 8) ALD16(ca, vca, WS_CS);
      if (n == 9) ALD16(cb, vcb, WS_CS);
      if (L && n >= 26) ITEM_V(n - 18, 1);
      if (G) geglu(2 * n + 1);
      if (n < 27) asm volatile("" :: "v"(acc0), "v"(acc1));
      SB();
    });
    if (!L) { wq[0][0] = wq[1][0]; wq[0][1] = wq[1][1]; wq[1][0] = wq[2][0]; wq[1][1] = wq[2][1]; }
    STAMP(F ? 2 : 3); TK(K == 1 ? 16 : 20);
  };

  auto phase2 = [&]() AI {
    FRESH_LANE();
    const int tid = w * 64 + lane;
    const unsigned swz = (((lane & 7) ^ rmask(16 * w + rl)) << 4);
    STAMP(13);
    LDA2(4); LDA2(5); LDA2(6);
    const unsigned r0 = 32 * kq + 4 * hb + q4;
    unsigned bV[3][4];
#pragma unroll
    for (int i = 0; i < 12; i++) { bV[i >> 2][i & 3] = ((r0 * 128 + (((2 * g16 + (p4 >> 1)) ^ rmask(r0)) << 4) + (p4 & 1) * 8) ^ ((i & 1) * 64) ^ ((i & 2) * 16)) + (i >> 2) * 65536; OPAQUE(bV[i >> 2][i & 3]); }
    const unsigned voffO = (28 * (w >> 1) + 16 * (w & 1) + rl) * 896 + swz;
    const unsigned voffO2 = voffO + (((w & 1) && rl >= 4) ? 0 : 8 * 896) - 1024;
    const rsrc_t rsO = mkrs(wo + ct * 128, 448 * 896 - ct * 128);
    const size_t go = (size_t)(rt * 64 + (tid >> 3)) * 896 + ct * 128 + (tid & 7) * 16;
    u32x4 xr;
    f32x16 acc0 = zero16(), acc1 = zero16();
    s16x8 wv[2][4];
    auto vread = [&](int c) AI {
      const int sl = (c + 7) % 10; const unsigned so = (sl & 3) * SLOT;
      for (int i = 0; i < 4; i++) wv[c & 1][i] = trfrag(bV[sl >> 2][i] + so + (i >> 1) * 2048, bV[sl >> 2][i] + so + (i >> 1) * 2048 + 1024);
    };
    vread(0);
    sfor<0, 18>([&](auto cc) AI {
      constexpr int c = decltype(cc)::value;
      asm volatile("s_waitcnt vmcnt(%3)" : "+v"(af[2 * c]), "+v"(af[2 * c + 1]), "+v"(fl) : "n"(P2::wait(c)) : "memory");
      if constexpr (c >= 4) {
        constexpr int ce = c < 8 ? 8 : c < 12 ? 12 : c < 14 ? 14 : 18, c0 = ce - 4;
#pragma unroll 1
        for (int it = 0; it < SPIN_CAP; ++it) {
          if (__builtin_expect(fresh(umax4(umax4(0u, af[2 * c]), af[2 * c + 1])), 1)) break;
          WAITV(0);
          __builtin_amdgcn_s_sleep(4);
          LDA2(c); if (c < c0 + 3) LDA2(c0 + 3); if (c < c0 + 2) LDA2(c0 + 2); if (c < c0 + 1) LDA2(c0 + 1);
          asm volatile("s_waitcnt vmcnt(0)" : PIN8(c0)::"memory");
        }
        if (c == 8) { STAMP(11); TK(21); } if (c == 14) { STAMP(12); TK(22); }
      }
      if (P2::bar(c)) BAR();
      TK2(5 + c);
      if (c + 1 < 18) vread(c + 1);
      SB();
      acc0 = MFMA(wv[c & 1][0], asf(af[2 * c]), acc0); acc1 = MFMA(wv[c & 1][1], asf(af[2 * c]), acc1);
      if (c == P2::FL2) ALD4(fl, VFL, WS_AFLAG);
      constexpr int j = P2::vj(c);
      if (j >= 0 && j < 4) { ITEM_V(j + 10, 0); ITEM_V(j + 10, 1); }
      if (j >= 4) { DMA1I(voffO, rsO, ldsw, ((j + 7) % 10) * SLOT, (j - 4) * (112 * 896), st0); DMA2(voffO2, rsO, st0); }
      SB();
      acc0 = MFMA(wv[c & 1][2], asf(af[2 * c + 1]), acc0); acc1 = MFMA(wv[c & 1][3], asf(af[2 * c + 1]), acc1);
      if (c == P2::N2) NEED(0xffff00ull);
      if constexpr (P2::a0(c) < P2::a0(c + 1)) LDA2(P2::a0(c));
      if constexpr (P2::a0(c) + 1 < P2::a0(c + 1)) LDA2(P2::a0(c) + 1);
      if constexpr (P2::a0(c) + 2 < P2::a0(c + 1)) LDA2(P2::a0(c) + 2);
      if constexpr (P2::a0(c) + 3 < P2::a0(c + 1)) LDA2(P2::a0(c) + 3);
      static_assert(P2::a0(c + 1) - P2::a0(c) <= 4, "schedule");
      if (c == 13) asm volatile("global_load_dwordx4 %0, %1, off" : "=v"(xr) : "v"(x + go) : "memory");
      SB();
    });
    STAMP(8);
    const unsigned wa = 5 * SLOT + (kq * 64 + rb2 * 32 + l31) * 272 + hb * 16;
#pragma unroll
    for (int gq = 0; gq < 4; gq++) {
      f32x4 v0 = {acc0[4 * gq], acc0[4 * gq + 1], acc0[4 * gq + 2], acc0[4 * gq + 3]};
      f32x4 v1 = {acc1[4 * gq], acc1[4 * gq + 1], acc1[4 * gq + 2], acc1[4 * gq + 3]};
      *(LAS f32x4*)(wa + gq * 32) = v0; *(LAS f32x4*)(wa + 128 + gq * 32) = v1;
    }
    BAR();
    const unsigned ra = 5 * SLOT + (tid >> 3) * 272 + (tid & 7) * 32;
    f32x4 s0 = *(LAS f32x4*)ra, s1 = *(LAS f32x4*)(ra + 16);
#pragma unroll
    for (int k = 1; k < 4; k++) { s0 += *(LAS f32x4*)(ra + k * 64 * 272); s1 += *(LAS f32x4*)(ra + k * 64 * 272 + 16); }
    asm volatile("s_waitcnt vmcnt(0)" : "+v"(xr)::"memory");
    u32x4 o;
    o[0] = pk2(s0[0] + blo(xr[0]), s0[1] + bhi(xr[0]));
    o[1] = pk2(s0[2] + blo(xr[1]), s0[3] + bhi(xr[1]));
    o[2] = pk2(s1[0] + blo(xr[2]), s1[1] + bhi(xr[2]));
    o[3] = pk2(s1[2] + blo(xr[3]), s1[3] + bhi(xr[3]));
    __builtin_amdgcn_raw_buffer_store_b128(o, mkrs(out, 2048 * 896), (unsigned)go, 0, 16);
    STAMP(9); STAMPC(15); TK(23); TK2(23); DUMPTK();
  };

  auto ffn_tail = [&]() AI {
    FRESH_LANE();
    TK2(0);
    asm volatile("s_waitcnt vmcnt(6)" : "+v"(fl)::"memory");
    const bool early = seen(fl, tag, 0xffull);
    const unsigned hs = WS_AFRAG + (rbg * 144 + (16 + fi) * 4 + 2 * role) * 1024 + lane * 16;
    u32x4 o[2];
#pragma unroll
    for (int t = 0; t < 4; t++) {
      const f32x16& a = t >> 1 ? pa1 : pa0;
      if (t == 0) LDA2(0); if (t == 1) LDA2(1); if (t == 2) LDA2(2); if (t == 3) LDA2(3);
      float hv[4];
#pragma unroll
      for (int j = 0; j < 4; j++) {
        const int i = (t & 1) * 4 + j;
        const float e = __builtin_amdgcn_exp2f(a[i] * fmaf(a[i] * a[i], k1, k0));
        hv[j] = (a[i] * a[i + 8]) * __builtin_amdgcn_rcpf(fmaf(e, ia2, ia2));
      }
      o[t >> 1][2 * (t & 1)] = pk2(hv[0], hv[1]); o[t >> 1][2 * (t & 1) + 1] = pk2(hv[2], hv[3]);
      asm volatile("" :: "v"(o[t >> 1][2 * (t & 1)]), "v"(o[t >> 1][2 * (t & 1) + 1])); SB();
    }
    st16(rs, hs, o[0]); st16(rs, hs + 1024, o[1]);
    TK2(3);
    asm volatile("s_waitcnt vmcnt(2)" : PIN8(0)::"memory");
    TK2(4);
    if (!early) {
      poll(ws, WS_AFLAG + VFL, 0xffull, tag); LDA2(0); LDA2(1); LDA2(2); LDA2(3);
      asm volatile("s_waitcnt vmcnt(0)" : PIN8(0)::"memory");
    }
  };

  const float ninf = __uint_as_float(~mlo & 0xff800000u);
  if (!isf) {
    FRESH_LANE();
    ALD16(ca, vca, WS_CS); ALD16(cb, vcb, WS_CS); ALD16(stt, vst, WS_STAT);
    ITEM_W(5, 0); ITEM_W(5, 1); ITEM_W(6, 0); ITEM_W(6, 1);
    asm volatile("s_waitcnt vmcnt(4)" : "+v"(ca), "+v"(cb), "+v"(stt)::"memory");
    g_check<true>(rs, ca, cb, stt, vca, vcb, vst, tag, mlo);
    make_fb();
    pa0 = MFMA(fold(ca), fb, pa0); pa1 = MFMA(fold(cb), fb, pa1);
    Sm sm;
    u32x4 kf1 = pack8u(pa0, 1); kf1[2] &= mlo; kf1[3] &= mlo;
    sm.p = MFMA(wf[0], asf(pack8u(pa0, 0)), zero16());
    sm.p = MFMA(wf[1], asf(kf1), sm.p);
    const unsigned wa = 7 * SLOT + (role * 128 + 32 * rb + l31) * 192 + ((hb ^ (l31 >> 1 & 7)) << 3);
#define IMG(f, v) do { *(LAS u32x2*)((wa ^ ((f) & 1) * 32) + ((f) >> 1) * 64) = u32x2{v[0], v[1]}; *(LAS u32x2*)((wa ^ (((f) & 1) * 32 + 16)) + ((f) >> 1) * 64) = u32x2{v[2], v[3]}; } while (0)
    { u32x4 v0 = pack8u(pa1, 0), v1 = pack8u(pa1, 1); v1[2] = (v1[2] & mlo) | (split2(__builtin_amdgcn_rcpf(rstd)) & ~mlo); v1[3] &= mlo; IMG(4, v0); IMG(5, v1); }
    sm_max(sm, ninf); sm_min(sm, ninf); sm_consts(sm, rstd);
    const unsigned oc = WS_AFRAG + rbg * 144 * 1024 + lane * 16;
#pragma unroll
    for (int i = 0; i < 16; i++) {
      sm_exp(sm, i, ninf);
      if (i % 5 == 0 && i < 15) { st16(rs, oc + (i == 0 ? 96 + 2 * fi + role : 112 + 4 * fi + 2 * role + (i == 5)) * 1024, ONES); SB(); }
    }
    st16(rs, (notile ? WS_KVRED + ((tg - 1 + role) * 16 + m) * 1024 + rb * 256 : 0xf0000000u) + (lane < 16 ? lane * 16 : 0x8000000u), ONES);
    sm_sum(sm, rstd);
#pragma unroll
    for (int f = 0; f < 4; f++) { const u32x4 v = sm_norm(sm, f); IMG(f, v); }
    BAR();
    TK(13);
    if (w < 4) {
      unsigned ra = 7 * SLOT + (w >> 1) * 24576 + (8 * hb + q4) * 192 + (((4 * g16 + p4) ^ (4 * hb + (q4 >> 1))) << 3), rc = (ra ^ 16) + 768;
      unsigned rp = ra + (w & 1) * 64, rq = rc + (w & 1) * 64;
      OPAQUE(ra); OPAQUE(rc); OPAQUE(rp); OPAQUE(rq);
      f32x16 kv = zero16();
#pragma unroll
      for (int s = 0; s < 8; s++) kv = MFMA(trfrag(rp + s * 3072, rq + s * 3072), trfrag(ra + 128 + s * 3072, rc + 128 + s * 3072), kv);
      st16(rs, WS_KVPART + ((fi * 4 + w) * 16 + m) * 2048 + lane * 16, pack8u(kv, 0));
      st16(rs, WS_KVPART + ((fi * 4 + w) * 16 + m) * 2048 + 1024 + lane * 16, pack8u(kv, 1));
    }
    STAMP(2); TK(14);
    aW[1] = 8192 + g16 * 4096 + (8 * hb + q4) * 64 + ((role ^ g16) << 5) + p4 * 8; aW[3] = aW[1] + 65536;
    OPAQUE(aW[0]); OPAQUE(aW[1]); OPAQUE(aW[2]); OPAQUE(aW[3]);
    wq[0][1] = wfrag(0, 1, true); wq[0][0] = wfrag(0, 0); wq[1][1] = wfrag(1, 1, true); wq[1][0] = wfrag(1, 0);
    BAR();
    TK(15);
    passn(IC<3>{});
    {
      FRESH_LANE();
      TK2(0);
      unsigned cf;
      const unsigned vcf = (((2 * (tg | 1) + 1) * 8 + (tg & 1) * 4 + (lane >> 2 & 3)) * 16 + 8 * (ts & 1) + 4 * role + p4) * 64;
      ALD4(cf, vcf, WS_CFLAG);
      ALD4(fl, VFL, WS_AFLAG);
      Sm sq;
      u32x4 q1 = pack8u(pa0, 1); q1[2] &= mlo; q1[3] &= mlo;
      sq.p = MFMA(wf[0], asf(pack8u(pa0, 0)), zero16());
      sq.p = MFMA(wf[1], asf(q1), sq.p);
      gconst();
      float hv[8];
#pragma unroll
      for (int i = 0; i < 8; i++) {
        const float e = __builtin_amdgcn_exp2f(pa1[i] * fmaf(pa1[i] * pa1[i], k1, k0));
        hv[i] = (pa1[i] * pa1[i + 8]) * __builtin_amdgcn_rcpf(fmaf(e, ia2, ia2));
      }
      const unsigned ob = WS_AFRAG + rbg * 144 * 1024 + lane * 16;
      asm volatile("s_waitcnt vmcnt(0)" : "+v"(cf), "+v"(fl)::"memory");
      const bool early = seen(fl, tag, 0xffull);
      st16(rs, ob + (96 + 2 * fi + role) * 1024, u32x4{pk2(hv[0], hv[1]), pk2(hv[2], hv[3]), pk2(hv[4], hv[5]), pk2(hv[6], hv[7])});
      if (!seen(cf, tag, ~0ull)) poll(ws, WS_CFLAG + vcf, ~0ull, tag);
      u32x4 kf[4], ph[4];
      const unsigned vk = (2 * fi + role) * 4096 + lane * 16;
#define LDK() do { ALD16(kf[0], vk, WS_KVRED); ALD16(kf[1], vk, WS_KVRED + 1024); ALD16(kf[2], vk, WS_KVRED + 2048); ALD16(kf[3], vk, WS_KVRED + 3072); } while (0)
#define PINK "+v"(kf[0]), "+v"(kf[1]), "+v"(kf[2]), "+v"(kf[3])
      sm_max(sq, ninf); sm_min(sq, ninf); sm_consts(sq, rstd);
#pragma unroll
      for (int i = 0; i < 16; i += 4) {
        sm_exp(sq, i, ninf); sm_exp(sq, i + 1, ninf); sm_exp(sq, i + 2, ninf); sm_exp(sq, i + 3, ninf);
        if (i == 0) LDA2(0); if (i == 4) LDA2(1); if (i == 8) LDA2(2); if (i == 12) LDA2(3);
        SB();
      }
      LDK();
      sm_sum(sq, 1.f);
#pragma unroll
      for (int f = 0; f < 4; f++) { ph[f] = sm_norm(sq, f); OPAQUE(ph[f]); }
      STAMP(7);
      asm volatile("s_waitcnt vmcnt(0)" : PINK, PIN8(0)::"memory");
#pragma unroll 1
      for (int it = 0; it < SPIN_CAP; ++it) {
        if (fresh(umax4(umax4(umax4(umax4(0u, kf[0]), kf[1]), kf[2]), kf[3]))) break;
        __builtin_amdgcn_s_sleep(8);
        LDK();
        asm volatile("s_waitcnt vmcnt(0)" : PINK::"memory");
      }
      STAMP(10);
      f32x16 nm = zero16();
#pragma unroll
      for (int i = 0; i < 4; i++) nm = MFMA(asf(kf[i]), asf(ph[i]), nm);
      float dl, dh; bcast32(nm[12] + nm[13], dl, dh);
      const float rd = __builtin_amdgcn_rcpf(dh + 1e-6f);
#pragma unroll
      for (int i = 0; i < 16; i++) nm[i] *= rd;
      u32x4 o1 = pack8u(nm, 1); o1[2] &= mlo; o1[3] &= mlo;
      st16(rs, ob + (112 + 4 * fi + 2 * role) * 1024, pack8u(nm, 0));
      st16(rs, ob + (113 + 4 * fi + 2 * role) * 1024, o1);
      STAMP(4); TK2(3);
      if (notile) {
        DUMPTK();
        if (bx == 24 * 8 + 1 && w == 0) {
#pragma unroll 1
          for (int i = 0; i < 8; i++) poll(ws, WS_AFLAG + (2 * i + hb) * 8 * 128 + l31 * 4, 0x00ffffff00ffffffull, tag);
          st4(rs, WS_EPOCH, tag);
        }
        __builtin_amdgcn_endpgm();
      }
      if (!early) {
        poll(ws, WS_AFLAG + VFL, 0xffull, tag); LDA2(0); LDA2(1); LDA2(2); LDA2(3);
        asm volatile("s_waitcnt vmcnt(0)" : PIN8(0)::"memory");
      }
    }
#pragma unroll
    for (int i = 0; i < 28; i++) asm volatile("" : "=v"(xf[i]));
    asm volatile("" : "=v"(wq[0][0]), "=v"(wq[0][1]), "=v"(wq[1][0]), "=v"(wq[1][1]), "=v"(pa0), "=v"(pa1));
  }
  int tso = ts; asm volatile("" : "+s"(tso));
  if (tso < 2) { passn(IC<1>{}); passn(IC<2>{}); ffn_tail(); }
  phase2();
}

extern "C" void kernel_launch(void* const* d_in, const int* in_sizes, int n_in, void* d_out, int out_size, void* d_ws, size_t ws_size, hipStream_t stream) {
  (void)hipFuncSetAttribute((const void*)fused_kernel, hipFuncAttributeMaxDynamicSharedMemorySize, LDS_BYTES);
  fused_kernel<<<256, 512, LDS_BYTES, stream>>>((const char*)d_in[0], (const char*)d_in[1], (const char*)d_in[2], (const char*)d_in[3],
                                                (const char*)d_in[5], (const char*)d_in[6], (const char*)d_in[7], (char*)d_out, (char*)d_ws);
}
```

```cpp
#include <hip/hip_runtime.h>
#include <hip/hip_bf16.h>
#include <stdint.h>
#include <type_traits>
#pragma clang diagnostic ignored "-Wint-to-pointer-cast"
#pragma clang diagnostic ignored "-Warray-bounds"

#define DI __device__ __forceinline__
#define AI __attribute__((always_inline))
#define LAS __attribute__((address_space(3)))
#define GAS __attribute__((address_space(1)))
typedef __attribute__((ext_vector_type(8))) short s16x8;
typedef __attribute__((ext_vector_type(4))) short s16x4;
typedef __attribute__((ext_vector_type(16))) float f32x16;
typedef __attribute__((ext_vector_type(4))) float f32x4;
typedef __attribute__((ext_vector_type(2))) float f32x2;
typedef __attribute__((ext_vector_type(4))) unsigned u32x4;
typedef __attribute__((ext_vector_type(2))) unsigned u32x2;
typedef __attribute__((ext_vector_type(2))) __bf16 bf16x2;
typedef GAS unsigned gu32;
typedef __amdgpu_buffer_rsrc_t rsrc_t;
#define RLX_AGENT __ATOMIC_RELAXED, __HIP_MEMORY_SCOPE_AGENT

constexpr unsigned WS_EPOCH = 0;
constexpr unsigned WS_AFLAG = 16384;
constexpr unsigned WS_STAT = WS_AFLAG + 128 * 128;
constexpr unsigned WS_CS = WS_STAT + 2048 * 16;
constexpr unsigned WS_KVRED = 262144;
constexpr unsigned WS_KVPART = WS_KVRED + 131072;
constexpr unsigned WS_CFLAG = WS_KVPART + (1u << 20);
constexpr unsigned WS_AFRAG = WS_KVPART + (2u << 20);
constexpr unsigned WS_STAMP = WS_AFRAG + 64u * 144u * 1024u;
constexpr unsigned WS_BYTES = WS_STAMP;
static_assert(WS_CS + 4928 * 32 <= WS_KVRED, "ws map");

constexpr int LDS_BYTES = 163840;
constexpr unsigned SLOT = 16384;
constexpr unsigned LDS_MISC = 0, LDS_PHI = 2048;
constexpr int CODE_LINES = 250;
constexpr int SPIN_CAP = 1 << 14;

#define HOTA(c) (c)
#define HOTV(c) (c)
#define MFMA(a, b, c) __builtin_amdgcn_mfma_f32_32x32x16_bf16((a), (b), (c), 0, 0, 0)
#define WAITV(n) asm volatile("s_waitcnt vmcnt(" #n ")" ::: "memory")
#define BAR() do { asm volatile("s_waitcnt lgkmcnt(0)" ::: "memory"); __builtin_amdgcn_s_barrier(); asm volatile("" ::: "memory"); } while (0)
#define SB() __builtin_amdgcn_sched_barrier(0)
#define OPAQUE(v) asm volatile("" : "+v"(v))
template <int N> DI void waitv() { asm volatile("s_waitcnt vmcnt(%0)" ::"n"(N) : "memory"); }
template <int N> using IC = std::integral_constant<int, N>;
template <int I, int N, class F> DI void sfor(F&& f) { if constexpr (I < N) { f(IC<I>{}); sfor<I + 1, N>(f); } }

#define STAMP(i)
#define STAMPC(i)
#define TK(i)
#define TK2(i)
#define DUMPTK()

#define DMA1I(voff, rsrc, ldsb, LIMM, SOFF, tmp) asm volatile("s_add_u32 m0, %3, %4\n\ts_mov_b32 %0, %5\n\tbuffer_load_dwordx4 %1, %2, %0 offen lds" \
    : "=&s"(tmp) : "v"(voff), "s"(rsrc), "s"(ldsb), "n"(LIMM), "n"(SOFF) : "memory", "scc")
#define DMA1P(voff, rsrc, ldsb, LIMM, sa, sb, tmp) asm volatile("s_add_u32 m0, %3, %4\n\ts_add_u32 %0, %5, %6\n\tbuffer_load_dwordx4 %1, %2, %0 offen lds" \
    : "=&s"(tmp) : "v"(voff), "s"(rsrc), "s"(ldsb), "n"(LIMM), "s"(sa), "s"(sb) : "memory", "scc")
#define DMA1M(voff, rsrc, ldsb, LIMM, sa, sb, tmp) asm volatile("s_add_u32 m0, %3, %4\n\ts_mul_i32 %0, %6, %5\n\tbuffer_load_dwordx4 %1, %2, %0 offen lds" \
    : "=&s"(tmp) : "v"(voff), "s"(rsrc), "s"(ldsb), "n"(LIMM), "n"(sa), "s"(sb) : "memory", "scc")
#define DMA2(voff, rsrc, soff) asm volatile("buffer_load_dwordx4 %0, %1, %2 offen offset:1024 lds" :: "v"(voff), "s"(rsrc), "s"(soff) : "memory")
#define ALD16(dst, voff, soff) asm volatile("buffer_load_dwordx4 %0, %1, %2, %3 offen sc1" : "=v"(dst) : "v"(voff), "s"(rs), "s"(soff) : "memory")
#define ALD4(dst, voff, soff) asm volatile("buffer_load_dword %0, %1, %2, %3 offen sc1" : "=v"(dst) : "v"(voff), "s"(rs), "s"(soff) : "memory")

#define FRESH_LANE() int lane; asm volatile("v_mbcnt_lo_u32_b32 %0, -1, 0\n\tv_mbcnt_hi_u32_b32 %0, -1, %0" : "=v"(lane)); \
  const int g16 = (lane >> 4) & 1, q4 = (lane & 15) >> 2, p4 = lane & 3, l31 = lane & 31, hb = lane >> 5, rl = lane >> 3; \
  (void)g16; (void)q4; (void)p4; (void)l31; (void)hb; (void)rl
DI unsigned usel(bool c, unsigned a, unsigned b) { const unsigned k = c ? ~0u : 0u; return (a & k) | (b & ~k); }
DI const char* psel(bool c, const char* a, const char* b) { return c ? a : b; }
DI rsrc_t mkoff(const char* p, unsigned off, unsigned bytes) { return __builtin_amdgcn_make_buffer_rsrc((void*)(p + off), 0, bytes - off, 0x00020000); }
DI rsrc_t mkrs(const void* p, unsigned bytes) { return __builtin_amdgcn_make_buffer_rsrc((void*)p, 0, bytes, 0x00020000); }
DI s16x8 trfrag(unsigned a0, unsigned a1) {
  s16x4 lo = __builtin_amdgcn_ds_read_tr16_b64_v4i16((LAS s16x4*)a0);
  s16x4 hi = __builtin_amdgcn_ds_read_tr16_b64_v4i16((LAS s16x4*)a1);
  return __builtin_shufflevector(lo, hi, 0, 1, 2, 3, 4, 5, 6, 7);
}
DI unsigned pk2(float a, float b) { f32x2 p = {a, b}; return __builtin_bit_cast(unsigned, __builtin_convertvector(p, bf16x2)); }
DI float dot2(unsigned a, unsigned b, float c) { return __builtin_amdgcn_fdot2_f32_bf16(__builtin_bit_cast(bf16x2, a), __builtin_bit_cast(bf16x2, b), c, false); }
DI float blo(unsigned u) { return __uint_as_float(u << 16); }
DI float bhi(unsigned u) { return __uint_as_float(u & 0xffff0000u); }
DI float fand(float v, unsigned m) { return __uint_as_float(__float_as_uint(v) & m); }
DI float vmul(float a, float b) { float r = a * b; OPAQUE(r); return r; }
DI float vadd(float a, float b) { float r = a + b; OPAQUE(r); return r; }
DI float max3(float a, float b, float c) { return __builtin_elementwise_maximum(__builtin_elementwise_maximum(a, b), c); }
DI float min3(float a, float b, float c) { return __builtin_elementwise_minimum(__builtin_elementwise_minimum(a, b), c); }
DI float add32(float a, float b) { auto r = __builtin_amdgcn_permlane32_swap(__float_as_uint(a), __float_as_uint(b), false, false); return __uint_as_float(r[0]) + __uint_as_float(r[1]); }
DI float add16(float a, float b) { auto r = __builtin_amdgcn_permlane16_swap(__float_as_uint(a), __float_as_uint(b), false, false); return __uint_as_float(r[0]) + __uint_as_float(r[1]); }
DI void bcast32(float v, float& lo, float& hi) { auto r = __builtin_amdgcn_permlane32_swap(__float_as_uint(v), __float_as_uint(v), false, false); lo = __uint_as_float(r[0]); hi = __uint_as_float(r[1]); }
template <int CTRL> DI float dpp(float x) { return __builtin_bit_cast(float, __builtin_amdgcn_mov_dpp(__builtin_bit_cast(int, x), CTRL, 0xf, 0xf, true)); }
DI float wave_sum(float v) {
  v += dpp<0xB1>(v); v += dpp<0x4E>(v); v += dpp<0x141>(v); v += dpp<0x128>(v);
  v = add16(v, v); return add32(v, v);
}
DI unsigned split2(float v) {
  const unsigned h = pk2(v, 0.f);
  return pk2(__uint_as_float(h << 16), v - __uint_as_float(h << 16));
}
DI u32x4 pack8u(const f32x16& x, int s) {
  u32x4 u = {pk2(x[8 * s], x[8 * s + 1]), pk2(x[8 * s + 2], x[8 * s + 3]), pk2(x[8 * s + 4], x[8 * s + 5]), pk2(x[8 * s + 6], x[8 * s + 7])};
  return u;
}
DI s16x8 asf(u32x4 u) { return __builtin_bit_cast(s16x8, u); }
DI unsigned rmask(unsigned row) { return (((row >> 1) & 1) << 2) | ((row >> 2) & 1) | ((row >> 3) & 2); }
DI f32x16 zero16() { f32x16 z; for (int i = 0; i < 16; i++) z[i] = 0.f; return z; }
DI void st16(rsrc_t rs, unsigned off, u32x4 v) { __builtin_amdgcn_raw_buffer_store_b128(v, rs, off, 0, 16); }
DI void st4(rsrc_t rs, unsigned off, unsigned v) { __builtin_amdgcn_raw_buffer_store_b32(v, rs, off, 0, 16); }
DI u32x4 ld16(rsrc_t rs, unsigned off) { return __builtin_amdgcn_raw_buffer_load_b128(rs, off, 0, 16); }
DI bool seen(unsigned v, unsigned tag, unsigned long long need) { return (__builtin_amdgcn_ballot_w64(v == tag) & need) == need; }
DI void poll(char* ws, unsigned off, unsigned long long need, unsigned tag) {
#pragma unroll 1
  for (int it = 0; it < SPIN_CAP; ++it) {
    const unsigned v = __hip_atomic_load((gu32*)(ws + off), RLX_AGENT);
    if (seen(v, tag, need)) break;
    __builtin_amdgcn_s_sleep(2);
  }
  __builtin_amdgcn_fence(__ATOMIC_ACQUIRE, "wavefront");
}
template <bool ST> DI void g_check(rsrc_t rs, u32x4& a, u32x4& b, u32x4& st, unsigned va, unsigned vb, unsigned vs, unsigned tag, unsigned mlo) {
#pragma unroll 1
  for (int it = 0; it < SPIN_CAP; ++it) {
    unsigned bad = (a[1] ^ tag) | (b[1] ^ tag) | (((a[3] ^ tag) | (b[3] ^ tag)) & mlo);
    if (ST) bad |= (st[1] ^ tag) | (st[3] ^ tag);
    if (__builtin_expect(__builtin_amdgcn_ballot_w64(bad != 0) == 0ull, 1)) break;
    ALD16(a, va, WS_CS); ALD16(b, vb, WS_CS); if (ST) ALD16(st, vs, WS_STAT);
    if (ST) asm volatile("s_waitcnt vmcnt(0)" : "+v"(a), "+v"(b), "+v"(st)::"memory"); else asm volatile("s_waitcnt vmcnt(0)" : "+v"(a), "+v"(b)::"memory");
  }
}

DI unsigned umax4(unsigned m, const u32x4& v) { return max(max(max(m, v[0]), v[1]), max(v[2], v[3])); }
DI bool fresh(unsigned m) { return __builtin_amdgcn_ballot_w64(m == 0xffffffffu) == 0ull; }

struct Sm { float L, cx, cn, mx, mn, s1, s2; f32x16 p, ep, en; };
DI void sm_max(Sm& s, float ninf) {
  const f32x16& p = s.p;
  float a = max3(p[0], p[1], p[2]);
  for (int i = 3; i < 11; i += 2) a = max3(a, p[i], p[i + 1]);
  s.mx = max3(a, p[11], max3(max3(p[12], p[13], p[14]), p[15], p[15]) + ninf);
}
DI void sm_min(Sm& s, float ninf) {
  const f32x16& p = s.p;
  float b = min3(p[0], p[1], p[2]);
  for (int i = 3; i < 11; i += 2) b = min3(b, p[i], p[i + 1]);
  s.mn = min3(b, p[11], min3(min3(p[12], p[13], p[14]), p[15], p[15]) - ninf);
}
DI void sm_consts(Sm& s, float sc) {
  auto r = __builtin_amdgcn_permlane32_swap(__float_as_uint(s.mx), __float_as_uint(-s.mn), false, false);
  const float m = max3(__uint_as_float(r[0]), __uint_as_float(r[1]), __uint_as_float(r[1]));
  float mx, nmn; bcast32(m, mx, nmn);
  s.L = sc * 1.4426950408889634f; s.cx = -mx * s.L; s.cn = -nmn * s.L; s.s1 = 0.f; s.s2 = 0.f;
}
DI void sm_exp(Sm& s, int i, float ninf) {
  if (i == 12) { s.cx += ninf; s.cn += ninf; }
  s.ep[i] = __builtin_amdgcn_exp2f(fmaf(s.p[i], s.L, s.cx)); s.en[i] = __builtin_amdgcn_exp2f(fmaf(s.p[i], -s.L, s.cn));
  if (i > 0) { s.s1 = vadd(s.s1, s.ep[i - 1]); s.s2 = vadd(s.s2, s.en[i - 1]); }
}
DI void sm_sum(Sm& s, float os) {
  const float t = add32(vadd(s.s1, s.ep[15]), vadd(s.s2, s.en[15]));
  bcast32(os * __builtin_amdgcn_rcpf(t), s.s1, s.s2);
}
DI void sm_norm2(const Sm& s, int f, int h, u32x4& u) {
  const f32x16& e = f < 2 ? s.ep : s.en; const float r = f < 2 ? s.s1 : s.s2; const int o = 8 * (f & 1);
  for (int i = 2 * h; i < 2 * h + 2; i++) u[i] = pk2(vmul(e[o + 2 * i], r), vmul(e[o + 2 * i + 1], r));
}
DI u32x4 sm_norm(const Sm& s, int f) {
  const f32x16& e = f < 2 ? s.ep : s.en; const float r = f < 2 ? s.s1 : s.s2; const int o = 8 * (f & 1);
  u32x4 u;
  for (int i = 0; i < 4; i++) u[i] = pk2(vmul(e[o + 2 * i], r), vmul(e[o + 2 * i + 1], r));
  return u;
}

struct P2 {
  static constexpr int a0(int s) { return s <= 1 ? 7 + s : s <= 3 ? 8 : s + 5 < 18 ? s + 5 : 18; }
  static constexpr int vs(int j) { return j + 1; }
  static constexpr int vj(int s) { for (int j = 0; j < 8; j++) if (vs(j) == s) return j; return -1; }
  static constexpr int FL2 = 2, N2 = 7;
  static constexpr bool bar(int c) { return c == 1 || c == 3 || c == 5 || c == 7 || c == 12; }
  static constexpr int vneed(int c) { return c == 5 ? 9 : c == 7 ? 12 : c == 12 ? 17 : -1; }
  static constexpr int na(int s) { return 2 * (a0(s + 1) - a0(s)); }
  static constexpr int nops(int s) { return (vj(s) >= 0 ? 2 : 0) + na(s) + (s == 13 || s == FL2 ? 1 : 0); }
  static constexpr int upto(int from, int c) { int n = 0; for (int s = from; s < c; s++) n += nops(s); return n; }
  static constexpr int wait(int c) {
    int n = 63;
    if (c >= 4 && c < 7) n = 2 * (6 - c) + upto(0, c);
    for (int s = 0; s < c; s++) if (a0(s) <= c && c < a0(s + 1)) n = 2 * (a0(s + 1) - 1 - c) + upto(s + 1, c);
    if (c == N2) { const int f = upto(FL2, c) - 1; if (f < n) n = f; }
    if (vneed(c) >= 10) { const int s = vs(vneed(c) - 10), v = na(s) + upto(s + 1, c); if (v < n) n = v; }
    return n > 63 ? 63 : n;
  }
};

__global__ __launch_bounds__(512, 2) void fused_kernel(const char* __restrict__ x, const char* __restrict__ wqkv, const char* __restrict__ wphi,
                                                       const char* __restrict__ wo, const char* __restrict__ wg, const char* __restrict__ wu,
                                                       const char* __restrict__ wd, char* __restrict__ out, char* ws) {
  const int lane = threadIdx.x & 63, w = __builtin_amdgcn_readfirstlane(threadIdx.x >> 6);
  const int bx = blockIdx.x, xcd = bx & 7, r = bx >> 3;
  const int tg = xcd & 3, m = (xcd >> 2) * 8 + (r & 7), ts = r >> 3;
  const int g16 = (lane >> 4) & 1, q4 = (lane & 15) >> 2, p4 = lane & 3, l31 = lane & 31, hb = lane >> 5, rl = lane >> 3;
  asm volatile("" :: "s"(x), "s"(wqkv), "s"(wphi), "s"(wo), "s"(wg), "s"(wu), "s"(wd), "s"(out), "s"(ws));
  STAMP(0); STAMPC(14); TK(0);
  unsigned st0, st1;
  const unsigned ldsw = w * 2048;
  const unsigned voffX = (16 * w + rl) * 896 + (((lane & 7) ^ rmask(16 * w + rl)) << 4), voffX2 = voffX + 8 * 896 - 1024;
  const rsrc_t rsX = mkrs(x + (size_t)m * 128 * 896, 128 * 896);
#define ITEM_X(c, h) do { if (!(h)) DMA1I(voffX, rsX, ldsw, (7 + ((c) + 2) % 3) * SLOT, (c) * 128, st0); else DMA2(voffX2, rsX, st0); } while (0)
#define ITEM_V(c, h) do { if (!(h)) DMA1I(voffX, rsV, ldsw, (((c) + 7) % 10) * SLOT, (c) * (128 * 896), st0); else DMA2(voffX2, rsV, st0); } while (0)
  ITEM_X(0, 0); ITEM_X(0, 1);

  const bool isf = ts < 2;
  const int fi = 2 * tg + (ts & 1);
  const int rb = w & 3, role = w >> 2;
#define VOFF(f) ((16 * rb + rl) * ((f) ? 3584 : 2688) + (usel(f, (((((lane & 7) >> 1) ^ ((rl >> 1) & 1) ^ (role << 1)) << 1) | (lane & 1)), min((lane & 7) ^ (rl << 1 & 4), 6)) << 4))
  const char* wgu = psel(role, wu, wg);
  unsigned voffW = VOFF(isf), wmul = isf ? 1792 : 1344;
  unsigned voffW2 = voffW + wmul * 16 - 1024;
  rsrc_t rsW = mkoff(psel(isf, wgu, wqkv), fi * (isf ? 128 : 112) + (isf ? 0 : 896 + 896 * role), wmul * 896);
#define ITEM_W(c, h) do { if (!(h)) DMA1M(voffW, rsW, ldsw, (c) * SLOT, (c) * 128, wmul, st1); else DMA2(voffW2, rsW, st1); } while (0)
  ITEM_W(0, 0); ITEM_W(0, 1); ITEM_X(1, 0); ITEM_X(1, 1); ITEM_W(1, 0); ITEM_W(1, 1); ITEM_X(2, 0); ITEM_X(2, 1);
  unsigned pfc;
  {
    unsigned long long pc0; asm volatile("s_getpc_b64 %0" : "=s"(pc0));
    asm volatile("global_load_dword %0, %1, %2" : "=v"(pfc) : "v"(min(r * 8 + w + (lane & 1) * 256, CODE_LINES) * 128), "s"(pc0 & ~127ull) : "memory");
  }

  const rsrc_t rs = mkrs(ws, WS_BYTES);
  unsigned tag;
  const int pu = (xcd >> 2) * 32 + r, li = pu / 3;
  const int la = li >= 6, lj = li - 6 * la, ll = 2 * tg + (li & 1);
  const int lc = li < 12 ? 21 + 28 * (lj & 1) + 8 * (lj >> 1) + 2 * tg + la : li < 14 ? 45 + 28 * (li & 1) + tg : 7 * ((li - 14) >> 1) + ll;
  const int cpart = li >= 14 && (ll > 6 || li > 19) ? 3 : pu - 3 * li;
  const int srow = (xcd * 32 + r) * 8 + w;
  const int tidx = (xcd >> 1) * 56 + (xcd & 1) * 32 + r;
  const bool notile = ts == 3 && (xcd & 1);
  const int rt = notile ? 0 : tidx / 7, ct = notile ? 0 : tidx - 7 * rt;
  const rsrc_t rsV = mkrs(wd + ct * 128, notile ? 0 : 1792 * 896 - ct * 128);
  const rsrc_t rsP = mkrs(wphi + 4 * tg * 1568, isf ? 0 : 4 * 1568);
  const unsigned mlo = lane < 32 ? 0xffffffffu : 0u;
  const unsigned lp = w * 1024;

  const int rbg = m * 4 + rb;
  const int rb2 = w & 1, kq = w >> 1;
#define VFL (((rt * 2 + rb2) * 2 + (kq & 1)) * 128 + l31 * 4)
#define AB ((notile ? 0xe0000000u : ((rt * 2 + rb2) * 144 + 2 * kq) * 1024) + lane * 16)
  u32x4 af[36];
#define LDA2(c) asm volatile("s_mov_b32 %2, %5\n\tbuffer_load_dwordx4 %0, %3, %4, %2 offen sc1\n\tbuffer_load_dwordx4 %1, %3, %4, %2 offen offset:1024 sc1" \
    : "=&v"(af[2 * (c)]), "=&v"(af[2 * (c) + 1]), "=&s"(st1) : "v"(AB), "s"(rs), "n"(WS_AFRAG + (c) * 8192) : "memory")
#define PIN8(c) "+v"(af[2 * (c)]), "+v"(af[2 * (c) + 1]), "+v"(af[2 * (c) + 2]), "+v"(af[2 * (c) + 3]), "+v"(af[2 * (c) + 4]), "+v"(af[2 * (c) + 5]), "+v"(af[2 * (c) + 6]), "+v"(af[2 * (c) + 7])
#define NEED(mask) do { if (!seen(fl, tag, mask)) poll(ws, WS_AFLAG + VFL, mask, tag); } while (0)
#define ONES (u32x4{~0u, ~0u, ~0u, ~0u})
  f32x16 pa0, pa1;
  float rstd;
  unsigned fl = 0;

  const unsigned xrow = 32 * rb + l31;
  const unsigned vst = (m * 128 + xrow) * 16;
  s16x8 xf[28], wq[3][2], fb, wf[2];
  u32x4 stt, ca, cb;
#define AW_F() (g16 * 8192 + (8 * hb + q4) * 128 + (((2 * role) ^ ((q4 >> 1) & 1) ^ (g16 << 1)) * 32) + p4 * 8)
#define AW_A(half, sel) ((half) * 8192 + (8 * hb + q4) * 128 + ((56 * (sel) + 32 * g16 + 8 * p4) ^ (q4 << 5 & 64)))
#define VCS(f, col) ((g16 * ((f) ? 1792 : 16) + (lane & 15)) * 32 + hb * 16 + (col) * 32)
  unsigned aW[4], vca, vcb;
  aW[0] = usel(isf, AW_F(), AW_A(0, role)); aW[1] = aW[0] ^ (isf ? 32 : 8192); aW[2] = aW[0] + 65536; aW[3] = aW[1] + 65536;
  OPAQUE(aW[0]); OPAQUE(aW[1]); OPAQUE(aW[2]); OPAQUE(aW[3]);
  auto fold = [&](u32x4 c) AI { const unsigned c2 = c[2] & mlo; u32x4 u = {c[0], c2, c[0], c2}; return asf(u); };
  auto make_fb = [&]() AI { const unsigned a = stt[0] & 0xffffu, b = stt[0] >> 16; u32x4 u = {a | (a << 16), a | (a << 16), b | (b << 16), b | (b << 16)}; fb = asf(u); rstd = __uint_as_float(stt[2]); };
  auto wfrag = [&](int n, int t, bool narrow = false) AI {
    const int sl = (n >> 2) % 7;
    const unsigned o = (sl & 3) * SLOT + (n & 3) * (narrow ? 1024 : 2048), b = aW[(sl >> 2) * 2 + t];
    return trfrag(b + o, b + o + (narrow ? 256 : 512));
  };

  {
    u32x4 px, pw[3];
    TK(1);
    unsigned aX[4];
#pragma unroll
    for (int ks = 0; ks < 4; ks++) { aX[ks] = 7 * SLOT + xrow * 128 + (((2 * ks + hb) ^ rmask(xrow)) << 4); OPAQUE(aX[ks]); }
    auto xread = [&](int n) AI { xf[n] = *(LAS s16x8*)(aX[n & 3] + (((n >> 2) + 2) % 3) * SLOT); };
    f32x16 acc0 = zero16(), acc1 = zero16();
    const unsigned m56 = lane < 56 ? 0xffffffffu : 0u;
    float cs[8], s = 0.f, q = 0.f; unsigned pfd, pfo;
    const bool hw = !isf && role;
    unsigned voffN = usel(hw, (32 * (w & 1) + (lane >> 2)) * 3584 + ((p4 ^ (w & 2)) << 4), voffW); OPAQUE(voffN);
    const char* pcb = psel(lc < 21, wqkv, psel(lc < 49, wg, wu)) + (lc - (lc < 21 ? 0 : lc < 49 ? 21 : 49)) * 128;
    const unsigned ppitch = lc < 21 ? 2688 : 3584;
    rsrc_t rsC = mkoff(pcb, min(152 * cpart + 8 * w, 440) * ppitch, 447 * ppitch + 128);
    unsigned pst = 64 * ppitch;
    const char* pxr = x + (size_t)srow * 896;
    rsrc_t rsN = mkoff(psel(isf, wgu, psel(hw, psel(w & 2, wu, wg), wqkv)), usel(isf, (8 + fi) * 128, usel(hw, 3072 + 64 * fi, 112 * fi)), (isf || hw ? 1792 : 1344) * 896);
    const int u = bx * 8 + w;
    rsrc_t rsF = mkrs(psel(u < 1568, wd, wo) + (u < 1568 ? u : u - 1568) * 1024, u < 1960 ? 1024 : 0);
    unsigned pha = LDS_PHI + (4 * hb + q4) * 56 + g16 * 32 + p4 * 8 + role * 1568;
    asm volatile("" : "+s"(rsC), "+s"(pst), "+s"(pxr), "+s"(rsN), "+s"(rsF)); OPAQUE(pha);
    asm volatile("" :: "s"(rsV), "s"(rt), "s"(ct));
    WAITV(7);
    BAR();
    TK(4);
    xread(0); wq[0][1] = wfrag(0, 1); wq[0][0] = wfrag(0, 0);
    xread(1); wq[1][1] = wfrag(1, 1); wq[1][0] = wfrag(1, 0);
    auto pre = [&](int p) AI {
      if (p == 2) {
        asm volatile("buffer_load_dword %0, off, %1, 0 sc1" : "=v"(tag) : "s"(rs) : "memory");
        asm volatile("global_load_dwordx4 %0, %1, %2" : "=v"(px) : "v"(min(lane, 55) * 16), "s"(pxr) : "memory");
      }
      if (p == 3) {
        unsigned vo = rl * ppitch + (lane & 7) * 16;
        asm volatile("buffer_load_dwordx4 %0, %1, %2, 0 offen" : "=v"(pw[0]) : "v"(vo), "s"(rsC) : "memory"); vo += pst;
        asm volatile("buffer_load_dwordx4 %0, %1, %2, 0 offen" : "=v"(pw[1]) : "v"(vo), "s"(rsC) : "memory"); vo += pst;
        asm volatile("buffer_load_dwordx4 %0, %1, %2, 0 offen" : "=v"(pw[2]) : "v"(vo), "s"(rsC) : "memory");
      }
      if (p == 12) { tag += 1; for (int i = 0; i < 4; i++) px[i] &= m56; }
      if (p == 13) { for (int i = 0; i < 4; i++) { s = dot2(px[i], 0x3f803f80u, s); q = dot2(px[i], px[i], q); } OPAQUE(s); OPAQUE(q); }
      if (p == 14) { s = add32(s, q); s += dpp<0xB1>(s); OPAQUE(s); }
      if (p == 15) { s += dpp<0x4E>(s); s += dpp<0x141>(s); OPAQUE(s); }
      if (p == 16) { s += dpp<0x128>(s); s = add16(s, s); OPAQUE(s); }
      if (p == 17) { bcast32(s * (1.f / 448.f), s, q); q = fmaxf(fmaf(-s, s, q), 0.f) + 1e-5f; OPAQUE(s); OPAQUE(q); }
      if (p == 18) {
        u32x4 o = {split2(-s), tag, __float_as_uint(__builtin_amdgcn_rsqf(q)), tag};
        st16(rs, lane == 0 ? WS_STAT + srow * 16 : 0xfffffff0u, o);
      }
      if (p >= 15 && p < 23) {
        const int i = (p - 15) >> 1;
        if (p & 1) { pw[2][i] &= (w + 16 < (cpart == 2 ? 18 : 19) ? 0xffffffffu : 0u); cs[2 * i] = vadd(vadd(blo(pw[0][i]), blo(pw[1][i])), blo(pw[2][i])); }
        else cs[2 * i + 1] = vadd(vadd(bhi(pw[0][i]), bhi(pw[1][i])), bhi(pw[2][i]));
      }
      if (p == 23 || p == 24) for (int i = 2 * (p - 23); i < 2 * (p - 22); i++) { cs[i] = add32(cs[i], cs[i + 4]); OPAQUE(cs[i]); }
      if (p == 25) for (int i = 0; i < 2; i++) { cs[i] = add16(cs[i], cs[i + 2]); OPAQUE(cs[i]); }
      if (p == 26) for (int i = 0; i < 2; i++) { cs[i] += dpp<0x128>(cs[i]); OPAQUE(cs[i]); }
      if (p == 27) {
        *(LAS f32x2*)(LDS_MISC + w * 256 + ((lane & 7) * 8 + hb * 4 + g16 * 2) * 4) = f32x2{cs[0], cs[1]};
        *(LAS u32x2*)(LDS_PHI + 4 * 1568 + lane * 8) = u32x2{0u, 0u};
      }
    };
    sfor<0, 28>([&](auto nn) AI {
      constexpr int n = decltype(nn)::value;
      constexpr int c = (n + 2) >> 2, j = (n + 2) & 3;
      if (j == 0) {
        if (c == 2) asm volatile("s_waitcnt vmcnt(7)" : "+v"(tag), "+v"(px), "+v"(pw[0]), "+v"(pw[1]), "+v"(pw[2]) : "v"(pfc) : "memory");
        if (c == 1) WAITV(10); if (c == 3) WAITV(7); if (c == 4) WAITV(6); if (c == 5) WAITV(9); if (c == 6) WAITV(10);
        if (c == 7) asm volatile("s_waitcnt vmcnt(4)" :: "v"(pfd), "v"(pfo) : "memory");
        BAR();
        TK(4 + c);
      }
      if (n == 14 && w == 7 && cpart < 3) {
        float t = 0.f;
        for (int i = 0; i < 8; i++) t += *(LAS float*)(LDS_MISC + i * 256 + lane * 4);
        u32x2 o = {split2(t), tag};
        __builtin_amdgcn_raw_buffer_store_b64(o, rs, WS_CS + (lc * 64 + lane) * 32 + cpart * 8, 0, 16);
      }
#define P0_ISSUE(h) do { \
      if (n == 0 || n == 3) ITEM_W(n == 0 ? 2 : 3, h); \
      if (c >= 1 && c <= 4 && j == 0) ITEM_X(c + 2, h); \
      if (c >= 1 && c <= 3 && j == 2) ITEM_W(c + 3, h); \
      if (c == 4 && j >= 2) ITEM_W(j - 1, h); \
      if (c == 5 && j == 0) ITEM_W(0, h); \
      if (c == 5 && j == 2) ITEM_W(3, h); \
      if (c == 6 && j == 0) ITEM_W(4, h); } while (0)
      SB();
      if (n == 27) pa0 = MFMA(wq[n % 3][0], xf[n], acc0); else acc0 = MFMA(wq[n % 3][0], xf[n], acc0);
      SB();
      if (n + 2 < 28) xread(n + 2);
      wq[(n + 2) % 3][1] = wfrag(n + 2, 1);
      pre(2 * n);
      P0_ISSUE(0);
      if (n == 5) DMA1I(lp + lane * 16, rsP, lp, LDS_PHI, 0, st0);
      if (n == 15) {
        asm volatile("buffer_load_dword %0, %1, %2, 0 offen" : "=v"(pfd) : "v"((lane & 7) * 128), "s"(rsF) : "memory");
      }
      if (n == 19) asm volatile("global_load_dword %0, %1, %2" : "=v"(pfo) : "v"(0), "s"(out + (size_t)(rt * 64 + 8 * w) * 896) : "memory");
      if (n == 23) { vca = VCS(isf, fi * (isf ? 64 : 56) + role * (isf ? 32 : 28) + (isf ? 1344 : 448)); vcb = vca + (isf ? 512 : 448 * 32); }
      SB();
      if (n == 27) pa1 = MFMA(wq[n % 3][1], xf[n], acc1); else acc1 = MFMA(wq[n % 3][1], xf[n], acc1);
      SB();
      wq[(n + 2) % 3][0] = wfrag(n + 2, 0);
      pre(2 * n + 1);
      P0_ISSUE(1);
      if (n == 14) {
        wmul = isf || hw ? 1792 : 1344;
        rsW = rsN;
        voffW = voffN; voffW2 = voffW + (hw ? 16 * 3584 : wmul * 16) - 1024;
      }
      if (n == 15) {
        const unsigned pa = pha + (ts & 1) * 3136;
        wf[0] = trfrag(pa, pa + 448); wf[1] = trfrag(pa + 896, pa + 1344);
      }
      SB();
    });
    wq[0][0] = wq[1][0]; wq[0][1] = wq[1][1]; wq[1][0] = wq[2][0]; wq[1][1] = wq[2][1];
    STAMP(1); TK(12);
  }

  float k0, k1, ia2;
  auto gconst = [&]() AI {
    const float c0 = -2.f * 0.7978845608028654f * 1.4426950408889634f;
    k0 = vmul(c0, rstd); k1 = vmul(c0 * 0.044715f * rstd * rstd, rstd); ia2 = __builtin_amdgcn_rcpf(rstd * rstd);
  };
  auto passn = [&](auto kind) AI {
    constexpr int K = decltype(kind)::value;
    constexpr bool F = K == 1, L = K != 1, G = K != 3;
    FRESH_LANE();
    const unsigned hb0 = WS_AFRAG + (rbg * 144 + 2 * role) * 1024 + lane * 16;
    const unsigned ho = hb0 + (F ? fi : 8 + fi) * 4096;
    unsigned fo = 0;
    if (K == 3) {
      const unsigned f0 = WS_CFLAG + ((fi * 8 + w) * 16 + m) * 64, fa = WS_AFLAG + rbg * 256 + (8 + 2 * fi + role) * 4, fb2 = fa + 128;
      fo = lane == 0 ? f0 : 0xffffff00u; fo = lane == 1 ? fa : fo; fo = lane == 2 ? fb2 : fo;
      OPAQUE(fo);
    }
    const rsrc_t rsB = mkoff(wgu, (16 + fi) * 128, 448 * 3584);
    f32x16 acc0 = zero16(), acc1 = zero16();
    f32x16 qa0, qa1;
    float gt[2], gu[2], hprev; u32x4 hq;
    auto geglu = [&](int hs) AI {
#pragma unroll
      for (int e = 0; e < 16; e++) {
        const int d = hs - (F ? 14 + 9 * e / 4 : 4 + 12 * e / 5);
        const float Gv = e < 8 ? qa0[e & 7] : qa1[e & 7], U = e < 8 ? qa0[(e & 7) + 8] : qa1[(e & 7) + 8];
        if (d == 0) gt[e & 1] = vmul(Gv, fmaf(Gv * Gv, k1, k0));
        if (d == 1) { gt[e & 1] = __builtin_amdgcn_exp2f(gt[e & 1]); gu[e & 1] = vmul(Gv, U); }
        if (d == 2) gt[e & 1] = __builtin_amdgcn_rcpf(fmaf(gt[e & 1], ia2, ia2));
        if (d == 3) {
          if (e & 1) hq[(e >> 1) & 3] = pk2(hprev, gu[1] * gt[1]); else hprev = vmul(gu[0], gt[0]);
          if ((e & 7) == 7) st16(rs, ho + (e >> 3) * 1024, hq);
        }
      }
    };
    if (F) { ALD16(ca, vca, WS_CS); ALD16(cb, vcb, WS_CS); ALD16(stt, vst, WS_STAT); }
    sfor<0, 28>([&](auto nn) AI {
      constexpr int n = decltype(nn)::value;
      if (K == 2 && n == 10) { WAITV(6); st4(rs, lane == 0 ? WS_AFLAG + (rbg * 2 + role) * 128 + fi * 4 : 0xffffff00u, tag); }
      if (K == 3 && n == 5) { WAITV(5); st4(rs, fo, tag); }
      if (n == 10) { BAR(); TK(K == 1 ? 13 : 17); }
      if (n == 18) {
        asm volatile("s_waitcnt vmcnt(%2)" : "+v"(ca), "+v"(cb) : "n"(K == 3 ? 6 : 8) : "memory"); BAR(); TK(K == 1 ? 14 : 18);
        if (K == 3 && notile) {
          const int gg = tg - 1 + role;
          poll(ws, WS_CFLAG + (((2 * gg + (m >> 3)) * 8 + (m >> 1 & 3)) * 16 + (lane & 15)) * 64, ~0ull, tag);
          const unsigned pb = WS_KVPART + ((gg * 8 + (m >> 1)) * 16 + (lane >> 4)) * 2048 + (m & 1) * 1024 + (rb * 16 + (lane & 15)) * 16;
          u32x4 pv[4];
#pragma unroll
          for (int i = 0; i < 4; i++) pv[i] = ld16(rs, pb + i * 8192);
          float t[8];
#pragma unroll
          for (int j = 0; j < 4; j++) {
            t[2 * j] = (blo(pv[0][j]) + blo(pv[1][j])) + (blo(pv[2][j]) + blo(pv[3][j]));
            t[2 * j + 1] = (bhi(pv[0][j]) + bhi(pv[1][j])) + (bhi(pv[2][j]) + bhi(pv[3][j]));
          }
#pragma unroll
          for (int j = 0; j < 8; j++) { t[j] = add32(t[j], t[j]); t[j] = add16(t[j], t[j]); }
          st16(rs, lane < 16 ? WS_KVRED + (gg * 16 + m) * 1024 + (rb * 16 + lane) * 16 : 0xf0000000u, u32x4{pk2(t[0], t[1]), pk2(t[2], t[3]), pk2(t[4], t[5]), pk2(t[6], t[7])});
          STAMP(6);
        }
      }
      if (L && n == 19) g_check<false>(rs, ca, cb, stt, vca, vcb, vst, tag, mlo);
      if (n == 26) { if (F) WAITV(10); BAR(); TK(K == 1 ? 15 : 19); }
      if (G && n == (F ? 5 : 0)) {
        if (F) asm volatile("s_waitcnt vmcnt(6)" : "+v"(ca), "+v"(cb), "+v"(stt)::"memory");
        g_check<F>(rs, ca, cb, stt, vca, vcb, vst, tag, mlo);
      }
      SB();
      if (n == 27) pa0 = MFMA(wq[n % 3][0], xf[n], acc0); else acc0 = MFMA(wq[n % 3][0], xf[n], acc0);
      SB();
      if (!L || n + 2 < 28) wq[(n + 2) % 3][1] = wfrag(n + 2, 1, K == 3);
      if (F && n == 5) make_fb();
      if (G && n == (F ? 6 : 1)) qa0 = MFMA(fold(ca), fb, pa0);
      if (L && n == 20) acc0 = MFMA(fold(ca), fb, acc0);
      if (G && n < 4) ITEM_W(5 + n / 2, n & 1);
      if (F && n == 4) rsW = rsB;
      if (F && n >= 4 && n < 8) ITEM_V((n - 4) / 2, n & 1);
      if (F && (n == 23 || n == 24)) ITEM_V(2, !(n & 1));
      if (K == 3 && n < 6) ITEM_V(n / 2, n & 1);
      if (L && n >= 10 && n < 16) ITEM_V((n - 4) / 2, n & 1);
      if (L && (n == 18 || n == 19)) ITEM_V(6, n & 1);
      if (L && (n == 21 || n == 22)) ITEM_V(7, !(n & 1));
      if (L && n >= 26) ITEM_V(n - 18, 0);
      if (F && n >= 10 && n < 16) ITEM_W((n - 10) / 2, n & 1);
      if (F && (n == 18 || n == 19)) ITEM_W(3, n & 1);
      if (F && (n == 21 || n == 22)) ITEM_W(4, !(n & 1));
      if (G) geglu(2 * n);
      SB();
      if (n == 27) pa1 = MFMA(wq[n % 3][1], xf[n], acc1); else acc1 = MFMA(wq[n % 3][1], xf[n], acc1);
      SB();
      if (!L || n + 2 < 28) wq[(n + 2) % 3][0] = wfrag(n + 2, 0);
      if (F && n == 5) gconst();
      if (G && n == (F ? 6 : 1)) qa1 = MFMA(fold(cb), fb, pa1);
      if (L && n == 20) acc1 = MFMA(fold(cb), fb, acc1);
      if (F && (n == 0 || n == 7 || n == 17 || n == 20)) st16(rs, ho + (n < 8 ? 8 : 16) * 4096 + (n & 1) * 1024, ONES);
      if (K == 2 && n == 21) ALD4(fl, VFL, WS_AFLAG);
      if (n == 7) {
        if (G) { vca += 8 * 2048; vcb += 8 * 2048; }
        else { vca -= 448 * 32; vcb = VCS(true, 1344 + 1536 + 32 * fi + 16 * role); }
      }
      if (n == 8) ALD16(ca, vca, WS_CS);
      if (n == 9) ALD16(cb, vcb, WS_CS);
      if (L && n >= 26) ITEM_V(n - 18, 1);
      if (G) geglu(2 * n + 1);
      if (n < 27) asm volatile("" :: "v"(acc0), "v"(acc1));
      SB();
    });
    if (!L) { wq[0][0] = wq[1][0]; wq[0][1] = wq[1][1]; wq[1][0] = wq[2][0]; wq[1][1] = wq[2][1]; }
    STAMP(F ? 2 : 3); TK(K == 1 ? 16 : 20);
  };

  auto phase2 = [&]() AI {
    FRESH_LANE();
    const int tid = w * 64 + lane;
    const unsigned swz = (((lane & 7) ^ rmask(16 * w + rl)) << 4);
    STAMP(13);
    LDA2(4); LDA2(5); LDA2(6);
    const unsigned r0 = 32 * kq + 4 * hb + q4;
    unsigned bV[3][4];
#pragma unroll
    for (int i = 0; i < 12; i++) { bV[i >> 2][i & 3] = ((r0 * 128 + (((2 * g16 + (p4 >> 1)) ^ rmask(r0)) << 4) + (p4 & 1) * 8) ^ ((i & 1) * 64) ^ ((i & 2) * 16)) + (i >> 2) * 65536; OPAQUE(bV[i >> 2][i & 3]); }
    const unsigned voffO = (28 * (w >> 1) + 16 * (w & 1) + rl) * 896 + swz;
    const unsigned voffO2 = voffO + (((w & 1) && rl >= 4) ? 0 : 8 * 896) - 1024;
    const rsrc_t rsO = mkrs(wo + ct * 128, 448 * 896 - ct * 128);
    const size_t go = (size_t)(rt * 64 + (tid >> 3)) * 896 + ct * 128 + (tid & 7) * 16;
    u32x4 xr;
    f32x16 acc0 = zero16(), acc1 = zero16();
    s16x8 wv[2][4];
    auto vread = [&](int c) AI {
      const int sl = (c + 7) % 10; const unsigned so = (sl & 3) * SLOT;
      for (int i = 0; i < 4; i++) wv[c & 1][i] = trfrag(bV[sl >> 2][i] + so + (i >> 1) * 2048, bV[sl >> 2][i] + so + (i >> 1) * 2048 + 1024);
    };
    vread(0);
    sfor<0, 18>([&](auto cc) AI {
      constexpr int c = decltype(cc)::value;
      asm volatile("s_waitcnt vmcnt(%3)" : "+v"(af[2 * c]), "+v"(af[2 * c + 1]), "+v"(fl) : "n"(P2::wait(c)) : "memory");
      if constexpr (c >= 4) {
        constexpr int ce = c < 8 ? 8 : c < 12 ? 12 : c < 14 ? 14 : 18, c0 = ce - 4;
#pragma unroll 1
        for (int it = 0; it < SPIN_CAP; ++it) {
          if (__builtin_expect(fresh(umax4(umax4(0u, af[2 * c]), af[2 * c + 1])), 1)) break;
          WAITV(0);
          __builtin_amdgcn_s_sleep(4);
          LDA2(c); if (c < c0 + 3) LDA2(c0 + 3); if (c < c0 + 2) LDA2(c0 + 2); if (c < c0 + 1) LDA2(c0 + 1);
          asm volatile("s_waitcnt vmcnt(0)" : PIN8(c0)::"memory");
        }
        if (c == 8) { STAMP(11); TK(21); } if (c == 14) { STAMP(12); TK(22); }
      }
      if (P2::bar(c)) BAR();
      TK2(5 + c);
      if (c + 1 < 18) vread(c + 1);
      SB();
      acc0 = MFMA(wv[c & 1][0], asf(af[2 * c]), acc0); acc1 = MFMA(wv[c & 1][1], asf(af[2 * c]), acc1);
      if (c == P2::FL2) ALD4(fl, VFL, WS_AFLAG);
      constexpr int j = P2::vj(c);
      if (j >= 0 && j < 4) { ITEM_V(j + 10, 0); ITEM_V(j + 10, 1); }
      if (j >= 4) { DMA1I(voffO, rsO, ldsw, ((j + 7) % 10) * SLOT, (j - 4) * (112 * 896), st0); DMA2(voffO2, rsO, st0); }
      SB();
      acc0 = MFMA(wv[c & 1][2], asf(af[2 * c + 1]), acc0); acc1 = MFMA(wv[c & 1][3], asf(af[2 * c + 1]), acc1);
      if (c == P2::N2) NEED(0xffff00ull);
      if constexpr (P2::a0(c) < P2::a0(c + 1)) LDA2(P2::a0(c));
      if constexpr (P2::a0(c) + 1 < P2::a0(c + 1)) LDA2(P2::a0(c) + 1);
      if constexpr (P2::a0(c) + 2 < P2::a0(c + 1)) LDA2(P2::a0(c) + 2);
      if constexpr (P2::a0(c) + 3 < P2::a0(c + 1)) LDA2(P2::a0(c) + 3);
      static_assert(P2::a0(c + 1) - P2::a0(c) <= 4, "schedule");
      if (c == 13) asm volatile("global_load_dwordx4 %0, %1, off" : "=v"(xr) : "v"(x + go) : "memory");
      SB();
    });
    STAMP(8);
    const unsigned wa = 5 * SLOT + (kq * 64 + rb2 * 32 + l31) * 272 + hb * 16;
#pragma unroll
    for (int gq = 0; gq < 4; gq++) {
      f32x4 v0 = {acc0[4 * gq], acc0[4 * gq + 1], acc0[4 * gq + 2], acc0[4 * gq + 3]};
      f32x4 v1 = {acc1[4 * gq], acc1[4 * gq + 1], acc1[4 * gq + 2], acc1[4 * gq + 3]};
      *(LAS f32x4*)(wa + gq * 32) = v0; *(LAS f32x4*)(wa + 128 + gq * 32) = v1;
    }
    BAR();
    const unsigned ra = 5 * SLOT + (tid >> 3) * 272 + (tid & 7) * 32;
    f32x4 s0 = *(LAS f32x4*)ra, s1 = *(LAS f32x4*)(ra + 16);
#pragma unroll
    for (int k = 1; k < 4; k++) { s0 += *(LAS f32x4*)(ra + k * 64 * 272); s1 += *(LAS f32x4*)(ra + k * 64 * 272 + 16); }
    asm volatile("s_waitcnt vmcnt(0)" : "+v"(xr)::"memory");
    u32x4 o;
    o[0] = pk2(s0[0] + blo(xr[0]), s0[1] + bhi(xr[0]));
    o[1] = pk2(s0[2] + blo(xr[1]), s0[3] + bhi(xr[1]));
    o[2] = pk2(s1[0] + blo(xr[2]), s1[1] + bhi(xr[2]));
    o[3] = pk2(s1[2] + blo(xr[3]), s1[3] + bhi(xr[3]));
    __builtin_amdgcn_raw_buffer_store_b128(o, mkrs(out, 2048 * 896), (unsigned)go, 0, 16);
    STAMP(9); STAMPC(15); TK(23); TK2(23); DUMPTK();
  };

  auto ffn_tail = [&]() AI {
    FRESH_LANE();
    TK2(0);
    asm volatile("s_waitcnt vmcnt(6)" : "+v"(fl)::"memory");
    const bool early = seen(fl, tag, 0xffull);
    const unsigned hs = WS_AFRAG + (rbg * 144 + (16 + fi) * 4 + 2 * role) * 1024 + lane * 16;
    u32x4 o[2];
#pragma unroll
    for (int t = 0; t < 4; t++) {
      const f32x16& a = t >> 1 ? pa1 : pa0;
      if (t == 0) LDA2(0); if (t == 1) LDA2(1); if (t == 2) LDA2(2); if (t == 3) LDA2(3);
      float hv[4];
#pragma unroll
      for (int j = 0; j < 4; j++) {
        const int i = (t & 1) * 4 + j;
        const float e = __builtin_amdgcn_exp2f(a[i] * fmaf(a[i] * a[i], k1, k0));
        hv[j] = (a[i] * a[i + 8]) * __builtin_amdgcn_rcpf(fmaf(e, ia2, ia2));
      }
      o[t >> 1][2 * (t & 1)] = pk2(hv[0], hv[1]); o[t >> 1][2 * (t & 1) + 1] = pk2(hv[2], hv[3]);
      asm volatile("" :: "v"(o[t >> 1][2 * (t & 1)]), "v"(o[t >> 1][2 * (t & 1) + 1])); SB();
    }
    st16(rs, hs, o[0]); st16(rs, hs + 1024, o[1]);
    TK2(3);
    asm volatile("s_waitcnt vmcnt(2)" : PIN8(0)::"memory");
    TK2(4);
    if (!early) {
      poll(ws, WS_AFLAG + VFL, 0xffull, tag); LDA2(0); LDA2(1); LDA2(2); LDA2(3);
      asm volatile("s_waitcnt vmcnt(0)" : PIN8(0)::"memory");
    }
  };

  const float ninf = __uint_as_float(~mlo & 0xff800000u);
  if (!isf) {
    FRESH_LANE();
    ALD16(ca, vca, WS_CS); ALD16(cb, vcb, WS_CS); ALD16(stt, vst, WS_STAT);
    ITEM_W(5, 0); ITEM_W(5, 1); ITEM_W(6, 0); ITEM_W(6, 1);
    asm volatile("s_waitcnt vmcnt(4)" : "+v"(ca), "+v"(cb), "+v"(stt)::"memory");
    g_check<true>(rs, ca, cb, stt, vca, vcb, vst, tag, mlo);
    make_fb();
    pa0 = MFMA(fold(ca), fb, pa0); pa1 = MFMA(fold(cb), fb, pa1);
    Sm sm;
    u32x4 kf1 = pack8u(pa0, 1); kf1[2] &= mlo; kf1[3] &= mlo;
    sm.p = MFMA(wf[0], asf(pack8u(pa0, 0)), zero16());
    sm.p = MFMA(wf[1], asf(kf1), sm.p);
    const unsigned wa = 7 * SLOT + (role * 128 + 32 * rb + l31) * 192 + ((hb ^ (l31 >> 1 & 7)) << 3);
#define IMG(f, v) do { *(LAS u32x2*)((wa ^ ((f) & 1) * 32) + ((f) >> 1) * 64) = u32x2{v[0], v[1]}; *(LAS u32x2*)((wa ^ (((f) & 1) * 32 + 16)) + ((f) >> 1) * 64) = u32x2{v[2], v[3]}; } while (0)
    { u32x4 v0 = pack8u(pa1, 0), v1 = pack8u(pa1, 1); v1[2] = (v1[2] & mlo) | (split2(__builtin_amdgcn_rcpf(rstd)) & ~mlo); v1[3] &= mlo; IMG(4, v0); IMG(5, v1); }
    sm_max(sm, ninf); sm_min(sm, ninf); sm_consts(sm, rstd);
    const unsigned oc = WS_AFRAG + rbg * 144 * 1024 + lane * 16;
#pragma unroll
    for (int i = 0; i < 16; i++) {
      sm_exp(sm, i, ninf);
      if (i % 5 == 0 && i < 15) { st16(rs, oc + (i == 0 ? 96 + 2 * fi + role : 112 + 4 * fi + 2 * role + (i == 5)) * 1024, ONES); SB(); }
    }
    st16(rs, (notile ? WS_KVRED + ((tg - 1 + role) * 16 + m) * 1024 + rb * 256 : 0xf0000000u) + (lane < 16 ? lane * 16 : 0x8000000u), ONES);
    sm_sum(sm, rstd);
#pragma unroll
    for (int f = 0; f < 4; f++) { const u32x4 v = sm_norm(sm, f); IMG(f, v); }
    BAR();
    TK(13);
    if (w < 4) {
      unsigned ra = 7 * SLOT + (w >> 1) * 24576 + (8 * hb + q4) * 192 + (((4 * g16 + p4) ^ (4 * hb + (q4 >> 1))) << 3), rc = (ra ^ 16) + 768;
      unsigned rp = ra + (w & 1) * 64, rq = rc + (w & 1) * 64;
      OPAQUE(ra); OPAQUE(rc); OPAQUE(rp); OPAQUE(rq);
      f32x16 kv = zero16();
#pragma unroll
      for (int s = 0; s < 8; s++) kv = MFMA(trfrag(rp + s * 3072, rq + s * 3072), trfrag(ra + 128 + s * 3072, rc + 128 + s * 3072), kv);
      st16(rs, WS_KVPART + ((fi * 4 + w) * 16 + m) * 2048 + lane * 16, pack8u(kv, 0));
      st16(rs, WS_KVPART + ((fi * 4 + w) * 16 + m) * 2048 + 1024 + lane * 16, pack8u(kv, 1));
    }
    STAMP(2); TK(14);
    aW[1] = 8192 + g16 * 4096 + (8 * hb + q4) * 64 + ((role ^ g16) << 5) + p4 * 8; aW[3] = aW[1] + 65536;
    OPAQUE(aW[0]); OPAQUE(aW[1]); OPAQUE(aW[2]); OPAQUE(aW[3]);
    wq[0][1] = wfrag(0, 1, true); wq[0][0] = wfrag(0, 0); wq[1][1] = wfrag(1, 1, true); wq[1][0] = wfrag(1, 0);
    BAR();
    TK(15);
    passn(IC<3>{});
    {
      FRESH_LANE();
      TK2(0);
      unsigned cf;
      const unsigned vcf = (((2 * (tg | 1) + 1) * 8 + (tg & 1) * 4 + (lane >> 2 & 3)) * 16 + 8 * (ts & 1) + 4 * role + p4) * 64;
      ALD4(cf, vcf, WS_CFLAG);
      ALD4(fl, VFL, WS_AFLAG);
      Sm sq;
      u32x4 q1 = pack8u(pa0, 1); q1[2] &= mlo; q1[3] &= mlo;
      sq.p = MFMA(wf[0], asf(pack8u(pa0, 0)), zero16());
      sq.p = MFMA(wf[1], asf(q1), sq.p);
      gconst();
      float hv[8];
#pragma unroll
      for (int i = 0; i < 8; i++) {
        const float e = __builtin_amdgcn_exp2f(pa1[i] * fmaf(pa1[i] * pa1[i], k1, k0));
        hv[i] = (pa1[i] * pa1[i + 8]) * __builtin_amdgcn_rcpf(fmaf(e, ia2, ia2));
      }
      const unsigned ob = WS_AFRAG + rbg * 144 * 1024 + lane * 16;
      asm volatile("s_waitcnt vmcnt(0)" : "+v"(cf), "+v"(fl)::"memory");
      const bool early = seen(fl, tag, 0xffull);
      st16(rs, ob + (96 + 2 * fi + role) * 1024, u32x4{pk2(hv[0], hv[1]), pk2(hv[2], hv[3]), pk2(hv[4], hv[5]), pk2(hv[6], hv[7])});
      if (!seen(cf, tag, ~0ull)) poll(ws, WS_CFLAG + vcf, ~0ull, tag);
      u32x4 kf[4], ph[4];
      const unsigned vk = (2 * fi + role) * 4096 + lane * 16;
#define LDK() do { ALD16(kf[0], vk, WS_KVRED); ALD16(kf[1], vk, WS_KVRED + 1024); ALD16(kf[2], vk, WS_KVRED + 2048); ALD16(kf[3], vk, WS_KVRED + 3072); } while (0)
#define PINK "+v"(kf[0]), "+v"(kf[1]), "+v"(kf[2]), "+v"(kf[3])
      sm_max(sq, ninf); sm_min(sq, ninf); sm_consts(sq, rstd);
#pragma unroll
      for (int i = 0; i < 16; i += 4) {
        sm_exp(sq, i, ninf); sm_exp(sq, i + 1, ninf); sm_exp(sq, i + 2, ninf); sm_exp(sq, i + 3, ninf);
        if (i == 0) LDA2(0); if (i == 4) LDA2(1); if (i == 8) LDA2(2); if (i == 12) LDA2(3);
        SB();
      }
      LDK();
      sm_sum(sq, 1.f);
#pragma unroll
      for (int f = 0; f < 4; f++) { ph[f] = sm_norm(sq, f); OPAQUE(ph[f]); }
      STAMP(7);
      asm volatile("s_waitcnt vmcnt(0)" : PINK, PIN8(0)::"memory");
#pragma unroll 1
      for (int it = 0; it < SPIN_CAP; ++it) {
        if (fresh(umax4(umax4(umax4(umax4(0u, kf[0]), kf[1]), kf[2]), kf[3]))) break;
        __builtin_amdgcn_s_sleep(8);
        LDK();
        asm volatile("s_waitcnt vmcnt(0)" : PINK::"memory");
      }
      STAMP(10);
      f32x16 nm = zero16();
#pragma unroll
      for (int i = 0; i < 4; i++) nm = MFMA(asf(kf[i]), asf(ph[i]), nm);
      float dl, dh; bcast32(nm[12] + nm[13], dl, dh);
      const float rd = __builtin_amdgcn_rcpf(dh + 1e-6f);
#pragma unroll
      for (int i = 0; i < 16; i++) nm[i] *= rd;
      u32x4 o1 = pack8u(nm, 1); o1[2] &= mlo; o1[3] &= mlo;
      st16(rs, ob + (112 + 4 * fi + 2 * role) * 1024, pack8u(nm, 0));
      st16(rs, ob + (113 + 4 * fi + 2 * role) * 1024, o1);
      STAMP(4); TK2(3);
      if (notile) {
        DUMPTK();
        if (bx == 24 * 8 + 1 && w == 0) {
#pragma unroll 1
          for (int i = 0; i < 8; i++) poll(ws, WS_AFLAG + (2 * i + hb) * 8 * 128 + l31 * 4, 0x00ffffff00ffffffull, tag);
          st4(rs, WS_EPOCH, tag);
        }
        __builtin_amdgcn_endpgm();
      }
      if (!early) {
        poll(ws, WS_AFLAG + VFL, 0xffull, tag); LDA2(0); LDA2(1); LDA2(2); LDA2(3);
        asm volatile("s_waitcnt vmcnt(0)" : PIN8(0)::"memory");
      }
    }
#pragma unroll
    for (int i = 0; i < 28; i++) asm volatile("" : "=v"(xf[i]));
    asm volatile("" : "=v"(wq[0][0]), "=v"(wq[0][1]), "=v"(wq[1][0]), "=v"(wq[1][1]), "=v"(pa0), "=v"(pa1));
  }
  int tso = ts; asm volatile("" : "+s"(tso));
  if (tso < 2) { passn(IC<1>{}); passn(IC<2>{}); ffn_tail(); }
  phase2();
}

extern "C" void kernel_launch(void* const* d_in, const int* in_sizes, int n_in, void* d_out, int out_size, void* d_ws, size_t ws_size, hipStream_t stream) {
  (void)hipFuncSetAttribute((const void*)fused_kernel, hipFuncAttributeMaxDynamicSharedMemorySize, LDS_BYTES);
  fused_kernel<<<256, 512, LDS_BYTES, stream>>>((const char*)d_in[0], (const char*)d_in[1], (const char*)d_in[2], (const char*)d_in[3],
                                                (const char*)d_in[5], (const char*)d_in[6], (const char*)d_in[7], (char*)d_out, (char*)d_ws);
}
```

```cpp
#include <hip/hip_runtime.h>
#include <hip/hip_bf16.h>
#include <stdint.h>
#include <type_traits>
#pragma clang diagnostic ignored "-Wint-to-pointer-cast"
#pragma clang diagnostic ignored "-Warray-bounds"

#define DI __device__ __forceinline__
#define AI __attribute__((always_inline))
#define LAS __attribute__((address_space(3)))
#define GAS __attribute__((address_space(1)))
typedef __attribute__((ext_vector_type(8))) short s16x8;
typedef __attribute__((ext_vector_type(4))) short s16x4;
typedef __attribute__((ext_vector_type(16))) float f32x16;
typedef __attribute__((ext_vector_type(4))) float f32x4;
typedef __attribute__((ext_vector_type(2))) float f32x2;
typedef __attribute__((ext_vector_type(4))) unsigned u32x4;
typedef __attribute__((ext_vector_type(2))) unsigned u32x2;
typedef __attribute__((ext_vector_type(2))) __bf16 bf16x2;
typedef GAS unsigned gu32;
typedef __amdgpu_buffer_rsrc_t rsrc_t;
#define RLX_AGENT __ATOMIC_RELAXED, __HIP_MEMORY_SCOPE_AGENT

constexpr unsigned WS_EPOCH = 0;
constexpr unsigned WS_AFLAG = 16384;
constexpr unsigned WS_STAT = WS_AFLAG + 128 * 128;
constexpr unsigned WS_CS = WS_STAT + 2048 * 16;
constexpr unsigned WS_KVRED = 262144;
constexpr unsigned WS_KVPART = WS_KVRED + 131072;
constexpr unsigned WS_CFLAG = WS_KVPART + (1u << 20);
constexpr unsigned WS_AFRAG = WS_KVPART + (2u << 20);
constexpr unsigned WS_BYTES = WS_AFRAG + 64u * 144u * 1024u;
static_assert(WS_CS + 4928 * 32 <= WS_KVRED, "ws map");

constexpr int LDS_BYTES = 163840;
constexpr unsigned SLOT = 16384;
constexpr unsigned LDS_MISC = 0, LDS_PHI = 2048;
constexpr int CODE_LINES = 246;
constexpr int SPIN_CAP = 1 << 16;

#define MFMA(a, b, c) __builtin_amdgcn_mfma_f32_32x32x16_bf16((a), (b), (c), 0, 0, 0)
#define WAITV(n) asm volatile("s_waitcnt vmcnt(" #n ")" ::: "memory")
#define BAR() do { asm volatile("s_waitcnt lgkmcnt(0)" ::: "memory"); __builtin_amdgcn_s_barrier(); asm volatile("" ::: "memory"); } while (0)
#define SB() __builtin_amdgcn_sched_barrier(0)
#define OPAQUE(v) asm volatile("" : "+v"(v))
template <int N> using IC = std::integral_constant<int, N>;
template <int I, int N, class F> DI void sfor(F&& f) { if constexpr (I < N) { f(IC<I>{}); sfor<I + 1, N>(f); } }

#define DMA1I(voff, rsrc, ldsb, LIMM, SOFF, tmp) asm volatile("s_add_u32 m0, %3, %4\n\ts_mov_b32 %0, %5\n\tbuffer_load_dwordx4 %1, %2, %0 offen lds" \
    : "=&s"(tmp) : "v"(voff), "s"(rsrc), "s"(ldsb), "n"(LIMM), "n"(SOFF) : "memory", "scc")
#define DMA1M(voff, rsrc, ldsb, LIMM, sa, sb, tmp) asm volatile("s_add_u32 m0, %3, %4\n\ts_mul_i32 %0, %6, %5\n\tbuffer_load_dwordx4 %1, %2, %0 offen lds" \
    : "=&s"(tmp) : "v"(voff), "s"(rsrc), "s"(ldsb), "n"(LIMM), "n"(sa), "s"(sb) : "memory", "scc")
#define DMA2(voff, rsrc, soff) asm volatile("buffer_load_dwordx4 %0, %1, %2 offen offset:1024 lds" :: "v"(voff), "s"(rsrc), "s"(soff) : "memory")
#define ALD16(dst, voff, soff) asm volatile("buffer_load_dwordx4 %0, %1, %2, %3 offen sc1" : "=v"(dst) : "v"(voff), "s"(rs), "s"(soff) : "memory")
#define ALD4(dst, voff, soff) asm volatile("buffer_load_dword %0, %1, %2, %3 offen sc1" : "=v"(dst) : "v"(voff), "s"(rs), "s"(soff) : "memory")

#define FRESH_LANE() int lane; asm volatile("v_mbcnt_lo_u32_b32 %0, -1, 0\n\tv_mbcnt_hi_u32_b32 %0, -1, %0" : "=v"(lane)); \
  const int g16 = (lane >> 4) & 1, q4 = (lane & 15) >> 2, p4 = lane & 3, l31 = lane & 31, hb = lane >> 5, rl = lane >> 3; \
  (void)g16; (void)q4; (void)p4; (void)l31; (void)hb; (void)rl
DI unsigned usel(bool c, unsigned a, unsigned b) { const unsigned k = c ? ~0u : 0u; return (a & k) | (b & ~k); }
DI const char* psel(bool c, const char* a, const char* b) { return c ? a : b; }
DI rsrc_t mkoff(const char* p, unsigned off, unsigned bytes) { return __builtin_amdgcn_make_buffer_rsrc((void*)(p + off), 0, bytes - off, 0x00020000); }
DI rsrc_t mkrs(const void* p, unsigned bytes) { return __builtin_amdgcn_make_buffer_rsrc((void*)p, 0, bytes, 0x00020000); }
DI s16x8 trfrag(unsigned a0, unsigned a1) {
  s16x4 lo = __builtin_amdgcn_ds_read_tr16_b64_v4i16((LAS s16x4*)a0);
  s16x4 hi = __builtin_amdgcn_ds_read_tr16_b64_v4i16((LAS s16x4*)a1);
  return __builtin_shufflevector(lo, hi, 0, 1, 2, 3, 4, 5, 6, 7);
}
DI unsigned pk2(float a, float b) { f32x2 p = {a, b}; return __builtin_bit_cast(unsigned, __builtin_convertvector(p, bf16x2)); }
DI float dot2(unsigned a, unsigned b, float c) { return __builtin_amdgcn_fdot2_f32_bf16(__builtin_bit_cast(bf16x2, a), __builtin_bit_cast(bf16x2, b), c, false); }
DI float blo(unsigned u) { return __uint_as_float(u << 16); }
DI float bhi(unsigned u) { return __uint_as_float(u & 0xffff0000u); }
DI float vmul(float a, float b) { float r = a * b; OPAQUE(r); return r; }
DI float vadd(float a, float b) { float r = a + b; OPAQUE(r); return r; }
DI float max3(float a, float b, float c) { return __builtin_elementwise_maximum(__builtin_elementwise_maximum(a, b), c); }
DI float min3(float a, float b, float c) { return __builtin_elementwise_minimum(__builtin_elementwise_minimum(a, b), c); }
DI float add32(float a, float b) { auto r = __builtin_amdgcn_permlane32_swap(__float_as_uint(a), __float_as_uint(b), false, false); return __uint_as_float(r[0]) + __uint_as_float(r[1]); }
DI float add16(float a, float b) { auto r = __builtin_amdgcn_permlane16_swap(__float_as_uint(a), __float_as_uint(b), false, false); return __uint_as_float(r[0]) + __uint_as_float(r[1]); }
DI void bcast32(float v, float& lo, float& hi) { auto r = __builtin_amdgcn_permlane32_swap(__float_as_uint(v), __float_as_uint(v), false, false); lo = __uint_as_float(r[0]); hi = __uint_as_float(r[1]); }
template <int CTRL> DI float dpp(float x) { return __builtin_bit_cast(float, __builtin_amdgcn_mov_dpp(__builtin_bit_cast(int, x), CTRL, 0xf, 0xf, true)); }
DI unsigned split2(float v) {
  const unsigned h = pk2(v, 0.f);
  return pk2(__uint_as_float(h << 16), v - __uint_as_float(h << 16));
}
DI u32x4 pack8u(const f32x16& x, int s) {
  u32x4 u = {pk2(x[8 * s], x[8 * s + 1]), pk2(x[8 * s + 2], x[8 * s + 3]), pk2(x[8 * s + 4], x[8 * s + 5]), pk2(x[8 * s + 6], x[8 * s + 7])};
  return u;
}
DI s16x8 asf(u32x4 u) { return __builtin_bit_cast(s16x8, u); }
DI unsigned rmask(unsigned row) { return (((row >> 1) & 1) << 2) | ((row >> 2) & 1) | ((row >> 3) & 2); }
DI f32x16 zero16() { f32x16 z; for (int i = 0; i < 16; i++) z[i] = 0.f; return z; }
DI void st16(rsrc_t rs, unsigned off, u32x4 v) { __builtin_amdgcn_raw_buffer_store_b128(v, rs, off, 0, 16); }
DI void st4(rsrc_t rs, unsigned off, unsigned v) { __builtin_amdgcn_raw_buffer_store_b32(v, rs, off, 0, 16); }
DI u32x4 ld16(rsrc_t rs, unsigned off) { return __builtin_amdgcn_raw_buffer_load_b128(rs, off, 0, 16); }
DI bool seen(unsigned v, unsigned tag, unsigned long long need) { return (__builtin_amdgcn_ballot_w64(v == tag) & need) == need; }
DI void poll(char* ws, unsigned off, unsigned long long need, unsigned tag) {
#pragma unroll 1
  for (int it = 0; it < SPIN_CAP; ++it) {
    const unsigned v = __hip_atomic_load((gu32*)(ws + off), RLX_AGENT);
    if (seen(v, tag, need)) break;
    __builtin_amdgcn_s_sleep(2);
  }
  __builtin_amdgcn_fence(__ATOMIC_ACQUIRE, "wavefront");
}
#define UNLIKELY(c) __builtin_expect(!!(c), 0)
template <bool ST> DI bool g_bad(const u32x4& a, const u32x4& b, const u32x4& st, unsigned tag, unsigned mlo) {
  unsigned bad = (a[1] ^ tag) | (b[1] ^ tag) | (((a[3] ^ tag) | (b[3] ^ tag)) & mlo);
  if (ST) bad |= (st[1] ^ tag) | (st[3] ^ tag);
  return __builtin_amdgcn_ballot_w64(bad != 0) != 0ull;
}
template <bool ST> DI void g_check(rsrc_t rs, u32x4& a, u32x4& b, u32x4& st, unsigned va, unsigned vb, unsigned vs, unsigned tag, unsigned mlo) {
  if (UNLIKELY(g_bad<ST>(a, b, st, tag, mlo))) {
    int it = SPIN_CAP;
    do {
      ALD16(a, va, WS_CS); ALD16(b, vb, WS_CS); if (ST) ALD16(st, vs, WS_STAT);
      if (ST) asm volatile("s_waitcnt vmcnt(0)" : "+v"(a), "+v"(b), "+v"(st)::"memory"); else asm volatile("s_waitcnt vmcnt(0)" : "+v"(a), "+v"(b)::"memory");
    } while (g_bad<ST>(a, b, st, tag, mlo) && --it);
  }
}

DI unsigned umax4(unsigned m, const u32x4& v) { return max(max(max(max(m, v[0]), v[1]), v[2]), v[3]); }
DI bool fresh(unsigned m) { return __builtin_amdgcn_ballot_w64(m == 0xffffffffu) == 0ull; }

struct Sm { f32x16 p, ep, en; f32x2 a1, a2, L, cx, cn, s1, s2; float mx, mn; };
DI void sm_max(Sm& s, float ninf) {
  const f32x16& p = s.p;
  float a = max3(p[0], p[1], p[2]);
  for (int i = 3; i < 11; i += 2) a = max3(a, p[i], p[i + 1]);
  s.mx = max3(a, p[11], max3(max3(p[12], p[13], p[14]), p[15], p[15]) + ninf);
}
DI void sm_min(Sm& s, float ninf) {
  const f32x16& p = s.p;
  float b = min3(p[0], p[1], p[2]);
  for (int i = 3; i < 11; i += 2) b = min3(b, p[i], p[i + 1]);
  s.mn = min3(b, p[11], min3(min3(p[12], p[13], p[14]), p[15], p[15]) - ninf);
}
DI void sm_consts(Sm& s, float sc) {
  auto r = __builtin_amdgcn_permlane32_swap(__float_as_uint(s.mx), __float_as_uint(-s.mn), false, false);
  const float m = max3(__uint_as_float(r[0]), __uint_as_float(r[1]), __uint_as_float(r[1]));
  float mx, nmn; bcast32(m, mx, nmn);
  const float L = sc * 1.4426950408889634f, cx = -mx * L, cn = -nmn * L;
  s.L = f32x2{L, L}; s.cx = f32x2{cx, cx}; s.cn = f32x2{cn, cn};
}
DI void sm_exp2(Sm& s, int i, float ninf) {
  if (i == 12) { const float cx = s.cx[0] + ninf, cn = s.cn[0] + ninf; s.cx = f32x2{cx, cx}; s.cn = f32x2{cn, cn}; }
  const f32x2 p = {s.p[i], s.p[i + 1]};
  const f32x2 a = __builtin_elementwise_fma(p, s.L, s.cx), b = __builtin_elementwise_fma(p, -s.L, s.cn);
  if (i > 0) { s.a1 += f32x2{s.ep[i - 2], s.ep[i - 1]}; s.a2 += f32x2{s.en[i - 2], s.en[i - 1]}; } else { s.a1 = f32x2{0.f, 0.f}; s.a2 = f32x2{0.f, 0.f}; }
  s.ep[i] = __builtin_amdgcn_exp2f(a[0]); s.ep[i + 1] = __builtin_amdgcn_exp2f(a[1]);
  s.en[i] = __builtin_amdgcn_exp2f(b[0]); s.en[i + 1] = __builtin_amdgcn_exp2f(b[1]);
}
DI void sm_sum(Sm& s, float os) {
  s.a1 += f32x2{s.ep[14], s.ep[15]}; s.a2 += f32x2{s.en[14], s.en[15]};
  const f32x2 t1 = s.a1, t2 = s.a2;
  float r1, r2; bcast32(os * __builtin_amdgcn_rcpf(add32(t1[0] + t1[1], t2[0] + t2[1])), r1, r2);
  s.s1 = f32x2{r1, r1}; s.s2 = f32x2{r2, r2};
}
DI u32x4 sm_norm(const Sm& s, int f) {
  const f32x16& e = f < 2 ? s.ep : s.en; f32x2 r = s.s2; if (f < 2) r = s.s1; const int o = 8 * (f & 1);
  u32x4 u;
#pragma unroll
  for (int i = 0; i < 4; i++) { const f32x2 v = f32x2{e[o + 2 * i], e[o + 2 * i + 1]} * r; u[i] = pk2(v[0], v[1]); }
  return u;
}

struct P2 {
  static constexpr int LA = 5;
  static constexpr int a0(int s) { return s + LA < 18 ? s + LA : 18; }
  static constexpr int vs(int j) { return 1 + 3 * (j >> 1) + (j & 1); }
  static constexpr int vj(int s) { for (int j = 0; j < 8; j++) if (vs(j) == s) return j; return -1; }
  static constexpr int FL2 = 3, N2 = 12 - LA;
  static constexpr bool bar(int c) { return c == 1 || c == 4 || c == 7 || c == 10 || c == 13 || c == 15; }
  static constexpr int vneed(int c) { return c == 7 ? 10 : c == 10 ? 13 : c == 13 ? 15 : c == 15 ? 17 : -1; }
  static constexpr int na(int s) { return 2 * (a0(s + 1) - a0(s)); }
  static constexpr int nops(int s) { return (vj(s) >= 0 ? 2 : 0) + na(s) + (s == 14 || s == FL2 ? 1 : 0); }
  static constexpr int upto(int from, int c) { int n = 0; for (int s = from; s < c; s++) n += nops(s); return n; }
  static constexpr int wait(int c) {
    int n = 63;
    if (c >= 4 && c < LA) n = 2 * (LA - 1 - c) + upto(0, c);
    for (int s = 0; s < c; s++) if (a0(s) <= c && c < a0(s + 1)) n = 2 * (a0(s + 1) - 1 - c) + upto(s + 1, c);
    if (c == N2) { const int f = upto(FL2, c) - 1; if (f < n) n = f; }
    if (vneed(c) >= 10) { const int s = vs(vneed(c) - 10), v = na(s) + upto(s + 1, c); if (v < n) n = v; }
    return n > 63 ? 63 : n;
  }
};

__global__ __launch_bounds__(512, 2) void fused_kernel(const char* __restrict__ x, const char* __restrict__ wqkv, const char* __restrict__ wphi,
                                                       const char* __restrict__ wo, const char* __restrict__ wg, const char* __restrict__ wu,
                                                       const char* __restrict__ wd, char* __restrict__ out, char* ws) {
  const int lane = threadIdx.x & 63, w = __builtin_amdgcn_readfirstlane(threadIdx.x >> 6);
  const int bx = blockIdx.x, xcd = bx & 7, r = bx >> 3;
  const int tg = xcd & 3, m = (xcd >> 2) * 8 + (r & 7), ts = r >> 3;
  const int g16 = (lane >> 4) & 1, q4 = (lane & 15) >> 2, p4 = lane & 3, l31 = lane & 31, hb = lane >> 5, rl = lane >> 3;
  asm volatile("" :: "s"(x), "s"(wqkv), "s"(wphi), "s"(wo), "s"(wg), "s"(wu), "s"(wd), "s"(out), "s"(ws));
  unsigned st0, st1;
  const unsigned ldsw = w * 2048;
  const unsigned voffX = (16 * w + rl) * 896 + (((lane & 7) ^ rmask(16 * w + rl)) << 4), voffX2 = voffX + 8 * 896 - 1024;
  const rsrc_t rsX = mkrs(x + (size_t)m * 128 * 896, 128 * 896);
#define ITEM_X(c, h) do { if (!(h)) DMA1I(voffX, rsX, ldsw, (7 + ((c) + 2) % 3) * SLOT, (c) * 128, st0); else DMA2(voffX2, rsX, st0); } while (0)
#define ITEM_V(c, h) do { if (!(h)) DMA1I(voffX, rsV, ldsw, (((c) + 7) % 10) * SLOT, (c) * (128 * 896), st0); else DMA2(voffX2, rsV, st0); } while (0)
  ITEM_X(0, 0); ITEM_X(0, 1);

  const bool isf = ts < 2;
  const int fi = 2 * tg + (ts & 1);
  const int rb = w & 3, role = w >> 2;
  const int hl1 = 8 + ((fi + 1) & 7), hl2 = 16 + ((fi + 2) & 7);
#define VOFF(f) ((16 * rb + rl) * ((f) ? 3584 : 2688) + (usel(f, (((((lane & 7) >> 1) ^ ((rl >> 1) & 1) ^ (role << 1)) << 1) | (lane & 1)), min((lane & 7) ^ (rl << 1 & 4), 6)) << 4))
  const char* wgu = psel(role, wu, wg);
  unsigned voffW = VOFF(isf), wmul = isf ? 1792 : 1344;
  unsigned voffW2 = voffW + wmul * 16 - 1024;
  rsrc_t rsW = mkoff(psel(isf, wgu, wqkv), fi * (isf ? 128 : 112) + (isf ? 0 : 896 + 896 * role), wmul * 896);
#define ITEM_W(c, h) do { if (!(h)) DMA1M(voffW, rsW, ldsw, (c) * SLOT, (c) * 128, wmul, st1); else DMA2(voffW2, rsW, st1); } while (0)
  ITEM_W(0, 0); ITEM_W(0, 1); ITEM_X(1, 0); ITEM_X(1, 1); ITEM_W(1, 0); ITEM_W(1, 1); ITEM_X(2, 0); ITEM_X(2, 1);
  unsigned pfc;
  {
    unsigned long long pc0; asm volatile("s_getpc_b64 %0" : "=s"(pc0));
    asm volatile("global_load_dword %0, %1, %2" : "=v"(pfc) : "v"(min(r * 8 + w + (lane & 1) * 256, CODE_LINES) * 128), "s"(pc0 & ~127ull) : "memory");
  }

  rsrc_t rs = mkrs(ws, WS_BYTES);
  asm volatile("" : "+s"(rs));
  unsigned tag;
  const int pu = (xcd >> 2) * 32 + r, li = pu / 3;
  const int la = li >= 6, lj = li - 6 * la, ll = 2 * tg + (li & 1);
  const int lc = li < 12 ? 21 + 28 * (lj & 1) + 8 * (lj >> 1) + ((2 * tg + la + (lj >> 1)) & 7) : li < 14 ? 45 + 28 * (li & 1) + tg : 7 * ((li - 14) >> 1) + ll;
  const int cpart = li >= 14 && (ll > 6 || li > 19) ? 3 : pu - 3 * li;
  const int srow = (xcd * 32 + r) * 8 + w;
  const int tidx = (xcd >> 1) * 56 + (xcd & 1) * 32 + r;
  const bool notile = ts == 3 && (xcd & 1);
  const int rt = notile ? 0 : tidx / 7, ct = notile ? 0 : tidx - 7 * rt;
  const rsrc_t rsV = mkrs(wd + ct * 128, notile ? 0 : 1792 * 896 - ct * 128);
  const rsrc_t rsP = mkrs(wphi + 4 * tg * 1568, isf ? 0 : 4 * 1568);
  const unsigned mlo = lane < 32 ? 0xffffffffu : 0u;
  const unsigned lp = w * 1024;

  const int rbg = m * 4 + rb;
  const int rb2 = w & 1, kq = w >> 1;
#define VFL (((rt * 2 + rb2) * 2 + (kq & 1)) * 128 + l31 * 4)
  unsigned abase = usel(notile, 0xe0000000u, ((rt * 2 + rb2) * 144 + 2 * kq) * 1024);
#define AB (abase + lane * 16)
  u32x4 af[36];
#define LDA2(c) asm volatile("s_mov_b32 %2, %5\n\tbuffer_load_dwordx4 %0, %3, %4, %2 offen sc1\n\tbuffer_load_dwordx4 %1, %3, %4, %2 offen offset:1024 sc1" \
    : "=&v"(af[2 * (c)]), "=&v"(af[2 * (c) + 1]), "=&s"(st1) : "v"(AB), "s"(rs), "n"(WS_AFRAG + (c) * 8192) : "memory")
#define PIN8(c) "+v"(af[2 * (c)]), "+v"(af[2 * (c) + 1]), "+v"(af[2 * (c) + 2]), "+v"(af[2 * (c) + 3]), "+v"(af[2 * (c) + 4]), "+v"(af[2 * (c) + 5]), "+v"(af[2 * (c) + 6]), "+v"(af[2 * (c) + 7])
#define NEED(mask) do { if (UNLIKELY(!seen(fl, tag, mask))) poll(ws, WS_AFLAG + VFL, mask, tag); } while (0)
#define R0MASK 0xffull
#define ONES (u32x4{~0u, ~0u, ~0u, ~0u})
  f32x16 pa0, pa1;
  float rstd;
  unsigned fl = 0;

  const unsigned xrow = 32 * rb + l31;
  const unsigned vst = (m * 128 + xrow) * 16;
  s16x8 xf[28], wq[3][2], fb, wf[2];
  u32x4 stt, ca, cb;
#define AW_F() (g16 * 8192 + (8 * hb + q4) * 128 + (((2 * role) ^ ((q4 >> 1) & 1) ^ (g16 << 1)) * 32) + p4 * 8)
#define AW_A(half, sel) ((half) * 8192 + (8 * hb + q4) * 128 + ((56 * (sel) + 32 * g16 + 8 * p4) ^ (q4 << 5 & 64)))
#define VCS(f, col) ((g16 * ((f) ? 1792 : 16) + (lane & 15)) * 32 + hb * 16 + (col) * 32)
  unsigned aW[4], vca, vcb;
  aW[0] = usel(isf, AW_F(), AW_A(0, role)); aW[1] = aW[0] ^ (isf ? 32 : 8192); aW[2] = aW[0] + 65536; aW[3] = aW[1] + 65536;
  OPAQUE(aW[0]); OPAQUE(aW[1]); OPAQUE(aW[2]); OPAQUE(aW[3]);
  auto fold = [&](u32x4 c) AI { const unsigned c2 = c[2] & mlo; u32x4 u = {c[0], c2, c[0], c2}; return asf(u); };
  auto make_fb = [&]() AI { const unsigned a = stt[0] & 0xffffu, b = stt[0] >> 16; u32x4 u = {a | (a << 16), a | (a << 16), b | (b << 16), b | (b << 16)}; fb = asf(u); rstd = __uint_as_float(stt[2]); };
  auto wfrag = [&](int n, int t, bool narrow = false) AI {
    const int sl = (n >> 2) % 7;
    const unsigned o = (sl & 3) * SLOT + (n & 3) * (narrow ? 1024 : 2048), b = aW[(sl >> 2) * 2 + t];
    return trfrag(b + o, b + o + (narrow ? 256 : 512));
  };

  {
    u32x4 px, pw[3];
    unsigned aX[4];
#pragma unroll
    for (int ks = 0; ks < 4; ks++) { aX[ks] = 7 * SLOT + xrow * 128 + (((2 * ks + hb) ^ rmask(xrow)) << 4); OPAQUE(aX[ks]); }
    auto xread = [&](int n) AI { xf[n] = *(LAS s16x8*)(aX[n & 3] + (((n >> 2) + 2) % 3) * SLOT); };
    f32x16 acc0 = zero16(), acc1 = zero16();
    const unsigned m56 = lane < 56 ? 0xffffffffu : 0u;
    float cs[8], s = 0.f, q = 0.f;
    const bool hw = !isf && role;
    unsigned voffN = usel(hw, (32 * (w & 1) + (lane >> 2)) * 3584 + ((p4 ^ (w & 2)) << 4), voffW); OPAQUE(voffN);
    const char* pcb = psel(lc < 21, wqkv, psel(lc < 49, wg, wu)) + (lc - (lc < 21 ? 0 : lc < 49 ? 21 : 49)) * 128;
    const unsigned ppitch = lc < 21 ? 2688 : 3584;
    rsrc_t rsC = mkoff(pcb, min(152 * cpart + 8 * w, 440) * ppitch, 447 * ppitch + 128);
    unsigned pst = 64 * ppitch;
    const char* pxr = x + (size_t)srow * 896;
    rsrc_t rsN = mkoff(psel(isf, wgu, psel(hw, psel(w & 2, wu, wg), wqkv)), usel(isf, hl1 * 128, usel(hw, 3072 + 64 * fi, 112 * fi)), (isf || hw ? 1792 : 1344) * 896);
    const int u = bx * 8 + w;
    rsrc_t rsF = mkrs(psel(u < 784, wd, wo) + (u < 784 ? u : u - 784) * 2048, u < 980 ? 2048 : 0);
    unsigned pfd;
    unsigned pha = LDS_PHI + (4 * hb + q4) * 56 + g16 * 32 + p4 * 8 + role * 1568;
    asm volatile("" : "+s"(rsC), "+s"(pst), "+s"(pxr), "+s"(rsN), "+s"(rsF)); OPAQUE(pha);
    asm volatile("" : "+s"(abase) : "s"(rsV), "s"(rt), "s"(ct));
    WAITV(7);
    BAR();
    xread(0); wq[0][1] = wfrag(0, 1); wq[0][0] = wfrag(0, 0);
    xread(1); wq[1][1] = wfrag(1, 1); wq[1][0] = wfrag(1, 0);
    auto pre = [&](int p) AI {
      if (p == 2) {
        asm volatile("buffer_load_dword %0, off, %1, 0 sc1" : "=v"(tag) : "s"(rs) : "memory");
        asm volatile("global_load_dwordx4 %0, %1, %2" : "=v"(px) : "v"(min(lane, 55) * 16), "s"(pxr) : "memory");
      }
      if (p == 3) {
        unsigned vo = rl * ppitch + (lane & 7) * 16;
        asm volatile("buffer_load_dwordx4 %0, %1, %2, 0 offen" : "=v"(pw[0]) : "v"(vo), "s"(rsC) : "memory"); vo += pst;
        asm volatile("buffer_load_dwordx4 %0, %1, %2, 0 offen" : "=v"(pw[1]) : "v"(vo), "s"(rsC) : "memory"); vo += pst;
        asm volatile("buffer_load_dwordx4 %0, %1, %2, 0 offen" : "=v"(pw[2]) : "v"(vo), "s"(rsC) : "memory");
      }
      if (p == 12) { tag += 1; for (int i = 0; i < 4; i++) px[i] &= m56; }
      if (p == 13) { for (int i = 0; i < 4; i++) { s = dot2(px[i], 0x3f803f80u, s); q = dot2(px[i], px[i], q); } OPAQUE(s); OPAQUE(q); }
      if (p == 14) { s = add32(s, q); s += dpp<0xB1>(s); OPAQUE(s); }
      if (p == 15) { s += dpp<0x4E>(s); s += dpp<0x141>(s); OPAQUE(s); }
      if (p == 16) { s += dpp<0x128>(s); s = add16(s, s); OPAQUE(s); }
      if (p == 17) { bcast32(s * (1.f / 448.f), s, q); q = fmaxf(fmaf(-s, s, q), 0.f) + 1e-5f; OPAQUE(s); OPAQUE(q); }
      if (p == 18) {
        u32x4 o = {split2(-s), tag, __float_as_uint(__builtin_amdgcn_rsqf(q)), tag};
        st16(rs, lane == 0 ? WS_STAT + srow * 16 : 0xfffffff0u, o);
      }
      if (p >= 15 && p < 23) {
        const int i = (p - 15) >> 1;
        if (p & 1) { pw[2][i] &= (w + 16 < (cpart == 2 ? 18 : 19) ? 0xffffffffu : 0u); cs[2 * i] = vadd(vadd(blo(pw[0][i]), blo(pw[1][i])), blo(pw[2][i])); }
        else cs[2 * i + 1] = vadd(vadd(bhi(pw[0][i]), bhi(pw[1][i])), bhi(pw[2][i]));
      }
      if (p == 23 || p == 24) for (int i = 2 * (p - 23); i < 2 * (p - 22); i++) { cs[i] = add32(cs[i], cs[i + 4]); OPAQUE(cs[i]); }
      if (p == 25) for (int i = 0; i < 2; i++) { cs[i] = add16(cs[i], cs[i + 2]); OPAQUE(cs[i]); }
      if (p == 26) for (int i = 0; i < 2; i++) { cs[i] += dpp<0x128>(cs[i]); OPAQUE(cs[i]); }
      if (p == 27) {
        *(LAS f32x2*)(LDS_MISC + w * 256 + ((lane & 7) * 8 + hb * 4 + g16 * 2) * 4) = f32x2{cs[0], cs[1]};
        *(LAS u32x2*)(LDS_PHI + 4 * 1568 + lane * 8) = u32x2{0u, 0u};
      }
    };
    sfor<0, 28>([&](auto nn) AI {
      constexpr int n = decltype(nn)::value;
      constexpr int c = (n + 2) >> 2, j = (n + 2) & 3;
      if (j == 0) {
        if (c == 2) asm volatile("s_waitcnt vmcnt(7)" : "+v"(tag), "+v"(px), "+v"(pw[0]), "+v"(pw[1]), "+v"(pw[2]) : "v"(pfc) : "memory");
        if (c == 1) WAITV(10); if (c == 3) WAITV(7); if (c == 4) WAITV(6); if (c == 5) WAITV(9); if (c == 6) WAITV(9);
        if (c == 7) asm volatile("s_waitcnt vmcnt(7)" :: "v"(pfd) : "memory");
        BAR();
      }
      if (n == 14 && UNLIKELY(w == 7 && cpart < 3)) {
        float t = 0.f;
        for (int i = 0; i < 8; i++) t += *(LAS float*)(LDS_MISC + i * 256 + lane * 4);
        u32x2 o = {split2(t), tag};
        __builtin_amdgcn_raw_buffer_store_b64(o, rs, WS_CS + (lc * 64 + lane) * 32 + cpart * 8, 0, 16);
      }
#define P0_ISSUE(h) do { \
      if (n == 0 || n == 3) ITEM_W(n == 0 ? 2 : 3, h); \
      if (c >= 1 && c <= 4 && j == 0) ITEM_X(c + 2, h); \
      if (c >= 1 && c <= 3 && j == 2) ITEM_W(c + 3, h); \
      if (c == 4 && j >= 2) ITEM_W(j - 1, h); \
      if (c == 5 && j == 0) ITEM_W(0, h); \
      if (c == 5 && j == 2) ITEM_W(3, h); \
      if (c == 6 && j == 0) ITEM_W(4, h); \
      if (n >= 26) ITEM_W(n - 21, h); } while (0)
      SB();
      if (n == 27) pa0 = MFMA(wq[n % 3][0], xf[n], acc0); else acc0 = MFMA(wq[n % 3][0], xf[n], acc0);
      SB();
      if (n + 2 < 28) xread(n + 2);
      wq[(n + 2) % 3][1] = wfrag(n + 2, 1);
      pre(2 * n);
      P0_ISSUE(0);
      if (n == 5) DMA1I(lp + lane * 16, rsP, lp, LDS_PHI, 0, st0);
      if (n == 15) asm volatile("buffer_load_dword %0, %1, %2, 0 offen" : "=v"(pfd) : "v"((lane & 15) * 128), "s"(rsF) : "memory");
      if (n == 23) { vca = VCS(isf, fi * (isf ? 64 : 56) + role * (isf ? 32 : 28) + (isf ? 1344 : 448)); vcb = vca + (isf ? 512 : 448 * 32); ALD16(ca, vca, WS_CS); }
      if (n == 24) ALD16(stt, vst, WS_STAT);
      SB();
      if (n == 27) pa1 = MFMA(wq[n % 3][1], xf[n], acc1); else acc1 = MFMA(wq[n % 3][1], xf[n], acc1);
      SB();
      wq[(n + 2) % 3][0] = wfrag(n + 2, 0);
      pre(2 * n + 1);
      P0_ISSUE(1);
      if (n == 23) ALD16(cb, vcb, WS_CS);
      if (n == 14) {
        wmul = isf || hw ? 1792 : 1344;
        rsW = rsN;
        voffW = voffN; voffW2 = voffW + (hw ? 16 * 3584 : wmul * 16) - 1024;
      }
      if (n == 15) {
        const unsigned pa = pha + (ts & 1) * 3136;
        wf[0] = trfrag(pa, pa + 448); wf[1] = trfrag(pa + 896, pa + 1344);
      }
      SB();
    });
    wq[0][0] = wq[1][0]; wq[0][1] = wq[1][1]; wq[1][0] = wq[2][0]; wq[1][1] = wq[2][1];
    asm volatile("s_waitcnt vmcnt(4)" : "+v"(ca), "+v"(cb), "+v"(stt)::"memory");
  }

  float k0, k1, ia2;
  auto gconst = [&]() AI {
    const float c0 = -2.f * 0.7978845608028654f * 1.4426950408889634f;
    k0 = vmul(c0, rstd); k1 = vmul(c0 * 0.044715f * rstd * rstd, rstd); ia2 = __builtin_amdgcn_rcpf(rstd * rstd);
  };
  auto passn = [&](auto kind) AI {
    constexpr int K = decltype(kind)::value;
    constexpr bool F = K == 1, L = K != 1, G = K != 3;
    FRESH_LANE();
    const unsigned hb0 = WS_AFRAG + (rbg * 144 + 2 * role) * 1024 + lane * 16;
    const unsigned ho = hb0 + (F ? fi : hl1) * 4096;
    unsigned fo = 0;
    if (K == 3) {
      const unsigned f0 = WS_CFLAG + ((fi * 8 + w) * 16 + m) * 64, fa = WS_AFLAG + rbg * 256 + (8 + 2 * fi + role) * 4, fb2 = fa + 128;
      fo = lane == 0 ? f0 : 0xffffff00u; fo = lane == 1 ? fa : fo; fo = lane == 2 ? fb2 : fo;
      OPAQUE(fo);
    }
    const rsrc_t rsB = mkoff(wgu, hl2 * 128, 448 * 3584);
    f32x16 acc0 = zero16(), acc1 = zero16();
    f32x16 qa0, qa1;
    unsigned pfo = 0;
    float gt[2], gu[2], hprev; u32x4 hq;
    auto geglu = [&](int hs) AI {
#pragma unroll
      for (int e = 0; e < 16; e++) {
        const int d = hs - ((F ? 8 : 4) + 12 * e / 5);
        const float Gv = e < 8 ? qa0[e & 7] : qa1[e & 7], U = e < 8 ? qa0[(e & 7) + 8] : qa1[(e & 7) + 8];
        if (d == 0) gt[e & 1] = vmul(Gv, fmaf(Gv * Gv, k1, k0));
        if (d == 1) { gt[e & 1] = __builtin_amdgcn_exp2f(gt[e & 1]); gu[e & 1] = vmul(Gv, U); }
        if (d == 2) gt[e & 1] = __builtin_amdgcn_rcpf(fmaf(gt[e & 1], ia2, ia2));
        if (d == 3) {
          if (e & 1) hq[(e >> 1) & 3] = pk2(hprev, gu[1] * gt[1]); else hprev = vmul(gu[0], gt[0]);
          if ((e & 7) == 7) st16(rs, ho + (e >> 3) * 1024, hq);
        }
      }
    };
    sfor<0, 28>([&](auto nn) AI {
      constexpr int n = decltype(nn)::value;
      if (K == 2 && n == 6) { WAITV(5); st4(rs, lane == 0 ? WS_AFLAG + (rbg * 2 + role) * 128 + fi * 4 : 0xffffff00u, tag); }
      if (K == 3 && n == 6) { WAITV(0); st4(rs, fo, tag); }
      if (n == 10) { BAR(); }
      if (n == 18) {
        asm volatile("s_waitcnt vmcnt(%2)" : "+v"(ca), "+v"(cb) : "n"(K == 2 ? 7 : 8), "v"(pfo) : "memory"); BAR();
      }
      if (n == 13) {
        if (K == 3 && UNLIKELY(notile)) {
          const int gg = tg - 1 + role;
          poll(ws, WS_CFLAG + (((2 * gg + (m >> 3)) * 8 + (m >> 1 & 3)) * 16 + (lane & 15)) * 64, ~0ull, tag);
          const unsigned pb = WS_KVPART + ((gg * 8 + (m >> 1)) * 16 + (lane >> 4)) * 2048 + (m & 1) * 1024 + (rb * 16 + (lane & 15)) * 16;
          u32x4 pv[4];
#pragma unroll
          for (int i = 0; i < 4; i++) pv[i] = ld16(rs, pb + i * 8192);
          float t[8];
#pragma unroll
          for (int j = 0; j < 4; j++) {
            t[2 * j] = (blo(pv[0][j]) + blo(pv[1][j])) + (blo(pv[2][j]) + blo(pv[3][j]));
            t[2 * j + 1] = (bhi(pv[0][j]) + bhi(pv[1][j])) + (bhi(pv[2][j]) + bhi(pv[3][j]));
          }
#pragma unroll
          for (int j = 0; j < 8; j++) { t[j] = add32(t[j], t[j]); t[j] = add16(t[j], t[j]); }
          st16(rs, lane < 16 ? WS_KVRED + (gg * 16 + m) * 1024 + (rb * 16 + lane) * 16 : 0xf0000000u, u32x4{pk2(t[0], t[1]), pk2(t[2], t[3]), pk2(t[4], t[5]), pk2(t[6], t[7])});
        }
      }
      if (L && n == 19) g_check<false>(rs, ca, cb, stt, vca, vcb, vst, tag, mlo);
      if (n == 26) { if (F) WAITV(9); if (K == 3) WAITV(12); BAR(); }
      if (G && n == (F ? 2 : 0)) {
        g_check<F>(rs, ca, cb, stt, vca, vcb, vst, tag, mlo);
      }
      SB();
      if (n == 27) pa0 = MFMA(wq[n % 3][0], xf[n], acc0); else acc0 = MFMA(wq[n % 3][0], xf[n], acc0);
      SB();
      if (!L || n + 2 < 28) wq[(n + 2) % 3][1] = wfrag(n + 2, 1, K == 3);
      if (F && n == 2) make_fb();
      if (G && n == (F ? 3 : 1)) qa0 = MFMA(fold(ca), fb, pa0);
      if (L && n == 20) acc0 = MFMA(fold(ca), fb, acc0);
      if (K == 2 && n < 4) ITEM_W(5 + n / 2, n & 1);
      if (F && n == 4) rsW = rsB;
      if (F && n >= 4 && n < 8) ITEM_V((n - 4) / 2, n & 1);
      if (F && (n == 23 || n == 24)) ITEM_V(2, !(n & 1));
      if (K == 3 && n >= 10 && n < 26) ITEM_V((n - 10) / 2, n & 1);
      if (K == 2 && n >= 10 && n < 16) ITEM_V((n - 4) / 2, n & 1);
      if (K == 2 && (n == 18 || n == 19)) ITEM_V(6, n & 1);
      if (K == 2 && (n == 21 || n == 22)) ITEM_V(7, !(n & 1));
      if (L && n >= 26) ITEM_V(n - 18, 0);
      if (F && n >= 10 && n < 16) ITEM_W((n - 10) / 2, n & 1);
      if (F && (n == 18 || n == 19)) ITEM_W(3, n & 1);
      if (F && (n == 21 || n == 22)) ITEM_W(4, !(n & 1));
      if (G) geglu(2 * n);
      SB();
      if (n == 27) pa1 = MFMA(wq[n % 3][1], xf[n], acc1); else acc1 = MFMA(wq[n % 3][1], xf[n], acc1);
      SB();
      if (!L || n + 2 < 28) wq[(n + 2) % 3][0] = wfrag(n + 2, 0);
      if (F && n == 2) gconst();
      if (G && n == (F ? 3 : 1)) qa1 = MFMA(fold(cb), fb, pa1);
      if (L && n == 20) acc1 = MFMA(fold(cb), fb, acc1);
      if (F && (n == 0 || n == 7 || n == 17 || n == 20)) st16(rs, hb0 + (n < 8 ? hl1 : hl2) * 4096 + (n & 1) * 1024, ONES);
      if (K == 2 && n == 24) ALD4(fl, VFL, WS_AFLAG);
      if (n == 7) {
        if (G) { vca += (F ? hl1 - fi : hl2 - hl1) * 2048; vcb = vca + 512; }
        else { vca -= 448 * 32; vcb = VCS(true, 1344 + 1536 + 32 * fi + 16 * role); }
      }
      if (K != 2 && n == 6) asm volatile("global_load_dword %0, %1, %2" : "=v"(pfo) : "v"(0), "s"(out + (size_t)(rt * 64 + 8 * w) * 896) : "memory");
      if (n == 8) ALD16(ca, vca, WS_CS);
      if (n == 9) ALD16(cb, vcb, WS_CS);
      if (L && n >= 26) ITEM_V(n - 18, 1);
      if (G) geglu(2 * n + 1);
      if (n < 27) asm volatile("" :: "v"(acc0), "v"(acc1));
      SB();
    });
    if (!L) { wq[0][0] = wq[1][0]; wq[0][1] = wq[1][1]; wq[1][0] = wq[2][0]; wq[1][1] = wq[2][1]; }
  };

  auto phase2 = [&]() AI {
    FRESH_LANE();
    const int tid = w * 64 + lane;
    const unsigned swz = (((lane & 7) ^ rmask(16 * w + rl)) << 4);
    LDA2(4);
    const unsigned r0 = 32 * kq + 4 * hb + q4;
    unsigned bV[3][4];
#pragma unroll
    for (int i = 0; i < 12; i++) { bV[i >> 2][i & 3] = ((r0 * 128 + (((2 * g16 + (p4 >> 1)) ^ rmask(r0)) << 4) + (p4 & 1) * 8) ^ ((i & 1) * 64) ^ ((i & 2) * 16)) + (i >> 2) * 65536; OPAQUE(bV[i >> 2][i & 3]); }
    const unsigned voffO = (28 * (w >> 1) + 16 * (w & 1) + rl) * 896 + swz;
    const unsigned voffO2 = voffO + (((w & 1) && rl >= 4) ? 0 : 8 * 896) - 1024;
    const rsrc_t rsO = mkrs(wo + ct * 128, 448 * 896 - ct * 128);
    const size_t go = (size_t)(rt * 64 + (tid >> 3)) * 896 + ct * 128 + (tid & 7) * 16;
    u32x4 xr;
    f32x16 acc0 = zero16(), acc1 = zero16();
    s16x8 wv[2][4];
    auto vread = [&](int c, int hi_, int lo_) AI {
      const int sl = (c + 7) % 10; const unsigned so = (sl & 3) * SLOT;
      for (int i = hi_; i >= lo_; i--) wv[c & 1][i] = trfrag(bV[sl >> 2][i] + so + (i >> 1) * 2048, bV[sl >> 2][i] + so + (i >> 1) * 2048 + 1024);
    };
    vread(0, 3, 0);
    sfor<0, 18>([&](auto cc) AI {
      constexpr int c = decltype(cc)::value;
      asm volatile("s_waitcnt vmcnt(%3)" : "+v"(af[2 * c]), "+v"(af[2 * c + 1]), "+v"(fl) : "n"(P2::wait(c)) : "memory");
      if constexpr (c >= 4) {
        constexpr int ce = c < 8 ? 8 : c < 12 ? 12 : c < 14 ? 14 : 18, c0 = ce - 4;
        if (UNLIKELY(!fresh(umax4(umax4(0u, af[2 * c]), af[2 * c + 1])))) {
          int it = SPIN_CAP;
          do {
            WAITV(0);
            __builtin_amdgcn_s_sleep(4);
            LDA2(c); if (c < c0 + 3) LDA2(c0 + 3); if (c < c0 + 2) LDA2(c0 + 2); if (c < c0 + 1) LDA2(c0 + 1);
            asm volatile("s_waitcnt vmcnt(0)" : PIN8(c0)::"memory");
          } while (!fresh(umax4(umax4(0u, af[2 * c]), af[2 * c + 1])) && --it);
        }
      }
      if (P2::bar(c)) BAR();
      SB();
      acc0 = MFMA(wv[c & 1][0], asf(af[2 * c]), acc0);
      SB();
      if (c + 1 < 18) vread(c + 1, 3, 2);
      SB();
      acc1 = MFMA(wv[c & 1][1], asf(af[2 * c]), acc1);
      SB();
      if (c + 1 < 18) vread(c + 1, 1, 0);
      SB();
      acc0 = MFMA(wv[c & 1][2], asf(af[2 * c + 1]), acc0);
      SB();
      if (c == P2::FL2) ALD4(fl, VFL, WS_AFLAG);
      constexpr int j = P2::vj(c);
      if (j >= 0 && j < 4) { ITEM_V(j + 10, 0); ITEM_V(j + 10, 1); }
      if (j >= 4) { DMA1I(voffO, rsO, ldsw, ((j + 7) % 10) * SLOT, (j - 4) * (112 * 896), st0); DMA2(voffO2, rsO, st0); }
      SB();
      acc1 = MFMA(wv[c & 1][3], asf(af[2 * c + 1]), acc1);
      SB();
      if (c == P2::N2) NEED(0xffff00ull);
      if constexpr (P2::a0(c) < P2::a0(c + 1)) LDA2(P2::a0(c));
      if constexpr (P2::a0(c) + 1 < P2::a0(c + 1)) LDA2(P2::a0(c) + 1);
      if constexpr (P2::a0(c) + 2 < P2::a0(c + 1)) LDA2(P2::a0(c) + 2);
      if constexpr (P2::a0(c) + 3 < P2::a0(c + 1)) LDA2(P2::a0(c) + 3);
      static_assert(P2::a0(c + 1) - P2::a0(c) <= 4, "schedule");
      if (c == 14) asm volatile("global_load_dwordx4 %0, %1, off" : "=v"(xr) : "v"(x + go) : "memory");
      SB();
    });
    const unsigned wa = 5 * SLOT + (kq * 64 + rb2 * 32 + l31) * 272 + hb * 16;
#pragma unroll
    for (int gq = 0; gq < 4; gq++) {
      f32x4 v0 = {acc0[4 * gq], acc0[4 * gq + 1], acc0[4 * gq + 2], acc0[4 * gq + 3]};
      f32x4 v1 = {acc1[4 * gq], acc1[4 * gq + 1], acc1[4 * gq + 2], acc1[4 * gq + 3]};
      *(LAS f32x4*)(wa + gq * 32) = v0; *(LAS f32x4*)(wa + 128 + gq * 32) = v1;
    }
    BAR();
    const unsigned ra = 5 * SLOT + (tid >> 3) * 272 + (tid & 7) * 32;
    f32x4 s0 = *(LAS f32x4*)ra, s1 = *(LAS f32x4*)(ra + 16);
#pragma unroll
    for (int k = 1; k < 4; k++) { s0 += *(LAS f32x4*)(ra + k * 64 * 272); s1 += *(LAS f32x4*)(ra + k * 64 * 272 + 16); }
    asm volatile("s_waitcnt vmcnt(0)" : "+v"(xr)::"memory");
    u32x4 o;
    o[0] = pk2(s0[0] + blo(xr[0]), s0[1] + bhi(xr[0]));
    o[1] = pk2(s0[2] + blo(xr[1]), s0[3] + bhi(xr[1]));
    o[2] = pk2(s1[0] + blo(xr[2]), s1[1] + bhi(xr[2]));
    o[3] = pk2(s1[2] + blo(xr[3]), s1[3] + bhi(xr[3]));
    __builtin_amdgcn_raw_buffer_store_b128(o, mkrs(out, 2048 * 896), (unsigned)go, 0, 16);
  };

  auto ffn_tail = [&]() AI {
    FRESH_LANE();
    asm volatile("s_waitcnt vmcnt(4)" : "+v"(fl)::"memory");
    const bool early = seen(fl, tag, R0MASK);
    const unsigned hs = WS_AFRAG + (rbg * 144 + hl2 * 4 + 2 * role) * 1024 + lane * 16;
    u32x4 o[2];
#pragma unroll
    for (int t = 0; t < 4; t++) {
      const f32x16& a = t >> 1 ? pa1 : pa0;
      if (t == 0) LDA2(0); if (t == 1) LDA2(1); if (t == 2) LDA2(2); if (t == 3) LDA2(3);
      float hv[4];
#pragma unroll
      for (int j = 0; j < 4; j++) {
        const int i = (t & 1) * 4 + j;
        const float e = __builtin_amdgcn_exp2f(a[i] * fmaf(a[i] * a[i], k1, k0));
        hv[j] = (a[i] * a[i + 8]) * __builtin_amdgcn_rcpf(fmaf(e, ia2, ia2));
      }
      o[t >> 1][2 * (t & 1)] = pk2(hv[0], hv[1]); o[t >> 1][2 * (t & 1) + 1] = pk2(hv[2], hv[3]);
      asm volatile("" :: "v"(o[t >> 1][2 * (t & 1)]), "v"(o[t >> 1][2 * (t & 1) + 1])); SB();
    }
    st16(rs, hs, o[0]); st16(rs, hs + 1024, o[1]);
    asm volatile("s_waitcnt vmcnt(2)" : PIN8(0)::"memory");
    if (UNLIKELY(!early)) {
      poll(ws, WS_AFLAG + VFL, R0MASK, tag); LDA2(0); LDA2(1); LDA2(2); LDA2(3);
      asm volatile("s_waitcnt vmcnt(0)" : PIN8(0)::"memory");
    }
  };

  const float ninf = __uint_as_float(~mlo & 0xff800000u);
  if (!isf) {
    FRESH_LANE();
    g_check<true>(rs, ca, cb, stt, vca, vcb, vst, tag, mlo);
    make_fb();
    pa0 = MFMA(fold(ca), fb, pa0); pa1 = MFMA(fold(cb), fb, pa1);
    Sm sm;
    u32x4 kf1 = pack8u(pa0, 1); kf1[2] &= mlo; kf1[3] &= mlo;
    sm.p = MFMA(wf[0], asf(pack8u(pa0, 0)), zero16());
    sm.p = MFMA(wf[1], asf(kf1), sm.p);
    const unsigned wa = 7 * SLOT + (role * 128 + 32 * rb + l31) * 192 + ((hb ^ (l31 >> 1 & 7)) << 3);
#define IMG(f, v) do { *(LAS u32x2*)((wa ^ ((f) & 1) * 32) + ((f) >> 1) * 64) = u32x2{v[0], v[1]}; *(LAS u32x2*)((wa ^ (((f) & 1) * 32 + 16)) + ((f) >> 1) * 64) = u32x2{v[2], v[3]}; } while (0)
    { u32x4 v0 = pack8u(pa1, 0), v1 = pack8u(pa1, 1); v1[2] = (v1[2] & mlo) | (split2(__builtin_amdgcn_rcpf(rstd)) & ~mlo); v1[3] &= mlo; IMG(4, v0); IMG(5, v1); }
    sm_max(sm, ninf); sm_min(sm, ninf); sm_consts(sm, rstd);
    const unsigned oc = WS_AFRAG + rbg * 144 * 1024 + lane * 16;
#pragma unroll
    for (int i = 0; i < 16; i += 2) {
      sm_exp2(sm, i, ninf);
      if (i % 6 == 0) { st16(rs, oc + (i == 0 ? 96 + 2 * fi + role : 112 + 4 * fi + 2 * role + (i == 6)) * 1024, ONES); SB(); }
    }
    st16(rs, usel(notile, WS_KVRED + ((tg - 1 + role) * 16 + m) * 1024 + rb * 256, 0xf0000000u) + (lane < 16 ? lane * 16 : 0x8000000u), ONES);
    sm_sum(sm, rstd);
#pragma unroll
    for (int f = 0; f < 4; f++) { const u32x4 v = sm_norm(sm, f); IMG(f, v); }
    BAR();
    if (w < 4) {
      unsigned ra = 7 * SLOT + (w >> 1) * 24576 + (8 * hb + q4) * 192 + (((4 * g16 + p4) ^ (4 * hb + (q4 >> 1))) << 3), rc = (ra ^ 16) + 768;
      unsigned rp = ra + (w & 1) * 64, rq = rc + (w & 1) * 64;
      OPAQUE(ra); OPAQUE(rc); OPAQUE(rp); OPAQUE(rq);
      f32x16 kv = zero16();
#pragma unroll
      for (int s = 0; s < 8; s++) kv = MFMA(trfrag(rp + s * 3072, rq + s * 3072), trfrag(ra + 128 + s * 3072, rc + 128 + s * 3072), kv);
      st16(rs, WS_KVPART + ((fi * 4 + w) * 16 + m) * 2048 + lane * 16, pack8u(kv, 0));
      st16(rs, WS_KVPART + ((fi * 4 + w) * 16 + m) * 2048 + 1024 + lane * 16, pack8u(kv, 1));
    }
    aW[1] = 8192 + g16 * 4096 + (8 * hb + q4) * 64 + ((role ^ g16) << 5) + p4 * 8; aW[3] = aW[1] + 65536;
    OPAQUE(aW[0]); OPAQUE(aW[1]); OPAQUE(aW[2]); OPAQUE(aW[3]);
    wq[0][1] = wfrag(0, 1, true); wq[0][0] = wfrag(0, 0); wq[1][1] = wfrag(1, 1, true); wq[1][0] = wfrag(1, 0);
    passn(IC<3>{});
    {
      FRESH_LANE();
      unsigned cf;
      const unsigned vcf = (((2 * (tg | 1) + 1) * 8 + (tg & 1) * 4 + (lane >> 2 & 3)) * 16 + 8 * (ts & 1) + 4 * role + p4) * 64;
      ALD4(cf, vcf, WS_CFLAG);
      ALD4(fl, VFL, WS_AFLAG);
      Sm sq;
      u32x4 q1 = pack8u(pa0, 1); q1[2] &= mlo; q1[3] &= mlo;
      sq.p = MFMA(wf[0], asf(pack8u(pa0, 0)), zero16());
      sq.p = MFMA(wf[1], asf(q1), sq.p);
      gconst();
      float hv[8];
#pragma unroll
      for (int i = 0; i < 8; i++) {
        const float e = __builtin_amdgcn_exp2f(pa1[i] * fmaf(pa1[i] * pa1[i], k1, k0));
        hv[i] = (pa1[i] * pa1[i + 8]) * __builtin_amdgcn_rcpf(fmaf(e, ia2, ia2));
      }
      const unsigned ob = WS_AFRAG + rbg * 144 * 1024 + lane * 16;
      asm volatile("s_waitcnt vmcnt(0)" : "+v"(cf), "+v"(fl)::"memory");
      const bool early = seen(fl, tag, R0MASK);
      st16(rs, ob + (96 + 2 * fi + role) * 1024, u32x4{pk2(hv[0], hv[1]), pk2(hv[2], hv[3]), pk2(hv[4], hv[5]), pk2(hv[6], hv[7])});
      if (UNLIKELY(!seen(cf, tag, ~0ull))) poll(ws, WS_CFLAG + vcf, ~0ull, tag);
      u32x4 kf[4], ph[4];
      const unsigned vk = (2 * fi + role) * 4096 + lane * 16;
#define LDK() do { ALD16(kf[0], vk, WS_KVRED); ALD16(kf[1], vk, WS_KVRED + 1024); ALD16(kf[2], vk, WS_KVRED + 2048); ALD16(kf[3], vk, WS_KVRED + 3072); } while (0)
#define PINK "+v"(kf[0]), "+v"(kf[1]), "+v"(kf[2]), "+v"(kf[3])
      sm_max(sq, ninf); sm_min(sq, ninf); sm_consts(sq, rstd);
#pragma unroll
      for (int i = 0; i < 16; i += 4) {
        sm_exp2(sq, i, ninf); sm_exp2(sq, i + 2, ninf);
        if (i == 0) LDA2(0); if (i == 4) LDA2(1); if (i == 8) LDA2(2); if (i == 12) LDA2(3);
        SB();
      }
      LDK();
      sm_sum(sq, 1.f);
#pragma unroll
      for (int f = 0; f < 4; f++) { ph[f] = sm_norm(sq, f); OPAQUE(ph[f]); }
      asm volatile("s_waitcnt vmcnt(0)" : PINK, PIN8(0)::"memory");
      if (UNLIKELY(!fresh(umax4(umax4(umax4(umax4(0u, kf[0]), kf[1]), kf[2]), kf[3])))) {
        int it = SPIN_CAP;
        do {
          __builtin_amdgcn_s_sleep(8);
          LDK();
          asm volatile("s_waitcnt vmcnt(0)" : PINK::"memory");
        } while (!fresh(umax4(umax4(umax4(umax4(0u, kf[0]), kf[1]), kf[2]), kf[3])) && --it);
      }
      f32x16 nm = zero16();
#pragma unroll
      for (int i = 0; i < 4; i++) nm = MFMA(asf(kf[i]), asf(ph[i]), nm);
      float dl, dh; bcast32(nm[12] + nm[13], dl, dh);
      const float rd = __builtin_amdgcn_rcpf(dh + 1e-6f);
#pragma unroll
      for (int i = 0; i < 16; i++) nm[i] *= rd;
      u32x4 o1 = pack8u(nm, 1); o1[2] &= mlo; o1[3] &= mlo;
      st16(rs, ob + (112 + 4 * fi + 2 * role) * 1024, pack8u(nm, 0));
      st16(rs, ob + (113 + 4 * fi + 2 * role) * 1024, o1);
      if (UNLIKELY(notile)) {
        if (bx == 24 * 8 + 1 && w == 0) {
#pragma unroll 1
          for (int i = 0; i < 8; i++) poll(ws, WS_AFLAG + (2 * i + hb) * 8 * 128 + l31 * 4, 0x00ffffff00ffffffull, tag);
          st4(rs, WS_EPOCH, tag);
        }
        __builtin_amdgcn_endpgm();
      }
      if (UNLIKELY(!early)) {
        poll(ws, WS_AFLAG + VFL, R0MASK, tag); LDA2(0); LDA2(1); LDA2(2); LDA2(3);
        asm volatile("s_waitcnt vmcnt(0)" : PIN8(0)::"memory");
      }
    }
#pragma unroll
    for (int i = 0; i < 28; i++) asm volatile("" : "=v"(xf[i]));
    asm volatile("" : "=v"(wq[0][0]), "=v"(wq[0][1]), "=v"(wq[1][0]), "=v"(wq[1][1]), "=v"(pa0), "=v"(pa1));
  }
  int tso = ts; asm volatile("" : "+s"(tso));
  if (tso < 2) { passn(IC<1>{}); passn(IC<2>{}); ffn_tail(); }
  phase2();
}

extern "C" void kernel_launch(void* const* d_in, const int* in_sizes, int n_in, void* d_out, int out_size, void* d_ws, size_t ws_size, hipStream_t stream) {
  (void)hipFuncSetAttribute((const void*)fused_kernel, hipFuncAttributeMaxDynamicSharedMemorySize, LDS_BYTES);
  fused_kernel<<<256, 512, LDS_BYTES, stream>>>((const char*)d_in[0], (const char*)d_in[1], (const char*)d_in[2], (const char*)d_in[3],
                                                (const char*)d_in[5], (const char*)d_in[6], (const char*)d_in[7], (char*)d_out, (char*)d_ws);
}
```
